```python
import torch

def _softmax(x, axis=-1):
    return torch.softmax(x, dim=axis)

def _gelu(x):
    return torch.nn.functional.gelu(x, approximate='tanh')

def _silu(x):
    return torch.nn.functional.silu(x)

def _sigmoid(x):
    return torch.sigmoid(x)

def _layernorm(x, axis=-1, eps=1e-05):
    m = x.mean(dim=axis, keepdim=True)
    v = x.var(dim=axis, keepdim=True, unbiased=False)
    return (x - m) / torch.sqrt(v + eps)

def _scatter_add(tgt, idx, src, axis=0):
    return tgt.clone().index_add_(axis, idx, src)

def _segment_sum(src, seg, n):
    out = torch.zeros((n,) + src.shape[1:], dtype=src.dtype)
    return out.index_add_(0, seg, src)

def _pad(x, pw):
    flat = tuple((v for pair in reversed(pw) for v in pair))
    return torch.nn.functional.pad(x, flat)
kernel_name = 'adaLN_cond__compressed_kv_cla__dense_activation_relu__prompt'

def setup_inputs():

    _w = lambda a: (lambda t: t.to(torch.bfloat16) if t.is_floating_point() else t)(torch.as_tensor(a))

    def _A_setup():
        B, H, Sq, Sk, D, M = (1, 32, 2048, 2048, 128, 512)
        Dc = 4 * D
        return {'x': _w((lambda *s: torch.randn(*s, dtype=torch.bfloat16) * 0.05)(B, Sq, M)), 'c_kv': _w((lambda *s: torch.randn(*s, dtype=torch.bfloat16) * 1.6)(B, Sk, Dc)), 'w_q': _w((lambda *s: torch.randn(*s, dtype=torch.bfloat16) * 0.8)(M, H, D) * M ** (-0.5)), 'w_uk': _w((lambda *s: torch.randn(*s, dtype=torch.bfloat16) * 0.8)(Dc, H, D) * Dc ** (-0.5)), 'w_uv': _w((lambda *s: torch.randn(*s, dtype=torch.bfloat16) * 1.6)(Dc, H, D) * Dc ** (-0.5)), 'w_o': _w((lambda *s: torch.randn(*s, dtype=torch.bfloat16) * 1.6)(H, D, M) * (H * D) ** (-0.5))}
    _A_in = _A_setup()

    def _B_setup():
        B, S, D, Dff = (1, 2048, 512, 1792)
        return {'x': _w((lambda *s: torch.randn(*s, dtype=torch.bfloat16) * 0.05)(B, S, D)), 'w1': _w((lambda *s: torch.randn(*s, dtype=torch.bfloat16) * 0.05)(D, Dff)), 'w2': _w((lambda *s: torch.randn(*s, dtype=torch.bfloat16) * 0.05)(Dff, D))}
    _B_in = _B_setup()
    return {'A_x': _A_in['x'], 'A_c_kv': _A_in['c_kv'], 'A_w_q': _A_in['w_q'], 'A_w_uk': _A_in['w_uk'], 'A_w_uv': _A_in['w_uv'], 'A_w_o': _A_in['w_o'], 'B_x': _B_in['x'], 'B_w1': _B_in['w1'], 'B_w2': _B_in['w2'], 'c': _w((lambda *s: torch.randn(*s, dtype=torch.bfloat16) * 0.1)(1, 512) * 8.0), 'w_mod': _w((lambda *s: torch.randn(*s, dtype=torch.bfloat16) * 0.1)(512, 6 * 512) * 512 ** (-0.5) * 8.0)}

def reference(A_x, A_c_kv, A_w_q, A_w_uk, A_w_uv, A_w_o, B_x, B_w1, B_w2, c, w_mod):

    def _A_ref(x, c_kv, w_q, w_uk, w_uv, w_o):
        B, Sq, M = (x.shape[0], x.shape[1], x.shape[2])
        Sk = c_kv.shape[1]
        H, D = (w_q.shape[1], w_q.shape[2])
        q = torch.einsum('bqm,mhd->bhqd', x, w_q)
        k = torch.einsum('bkc,chd->bhkd', c_kv, w_uk)
        v = torch.einsum('bkc,chd->bhkd', c_kv, w_uv)
        scores = torch.einsum('bhqd,bhkd->bhqk', q, k) * D ** (-0.5)
        off = Sk - Sq
        qi = torch.arange(Sq)[:, None]
        kj = torch.arange(Sk)[None, :]
        cmask = kj <= qi + off
        scores = torch.where(cmask[None, None], scores, -1000000000.0)
        p = _softmax(scores, axis=-1)
        ctx = torch.einsum('bhqk,bhkd->bhqd', p, v)
        return torch.einsum('bhqd,hdm->bqm', ctx, w_o)

    def _B_ref(x, w1, w2):
        h = torch.einsum('bsd,df->bsf', x, w1)
        zero = torch.zeros_like(h)
        h = torch.maximum(h, zero)
        return torch.einsum('bsf,fd->bsd', h, w2)
    x = A_x
    mod = (_silu(c) @ w_mod).reshape(c.shape[0], 6, 1, -1)
    sa, ca, ga, sb, cb, gb = (mod[:, 0].clone(), mod[:, 1].clone(), mod[:, 2].clone(), mod[:, 3].clone(), mod[:, 4].clone(), mod[:, 5].clone())
    h = _layernorm(x, axis=-1) * (1 + ca) + sa
    x = x + ga * _A_ref(h, A_c_kv, A_w_q, A_w_uk, A_w_uv, A_w_o)
    h = _layernorm(x, axis=-1) * (1 + cb) + sb
    x = x + gb * _B_ref(h, B_w1, B_w2)
    return x
```

```cpp
#include <hip/hip_runtime.h>
#include <hip/hip_bf16.h>
#include <stdint.h>

typedef unsigned short u16;
typedef short bf16x8 __attribute__((ext_vector_type(8)));
typedef short s16x4 __attribute__((ext_vector_type(4)));
typedef short v4i16_t __attribute__((ext_vector_type(4)));
typedef float f32x16 __attribute__((ext_vector_type(16)));
typedef float f32x4 __attribute__((ext_vector_type(4)));
typedef float f32x2 __attribute__((ext_vector_type(2)));
typedef unsigned u32x4 __attribute__((ext_vector_type(4)));
typedef __bf16 bf16x2_t __attribute__((ext_vector_type(2)));
typedef __attribute__((address_space(3))) const char* lds_cptr;
#define DI __device__ __forceinline__
#define MFMA(a, b, c) __builtin_amdgcn_mfma_f32_32x32x16_bf16((a), (b), (c), 0, 0, 0)
#define SBAR() __builtin_amdgcn_sched_barrier(0)

constexpr int SEQ = 2048, DM = 512, NH = 32, HD = 128, DFF = 1792;

constexpr size_t WS_KV = 0;
constexpr size_t WS_XH = WS_KV + (size_t)32 * 32 * 32768;
constexpr size_t WS_MOD = WS_XH + (size_t)64 * 32 * 1024;
constexpr size_t WS_CTX = WS_MOD + 16384;
constexpr size_t WS_PART = WS_CTX + (size_t)64 * 256 * 1024;
constexpr size_t WS_X1 = WS_PART + (size_t)4 * 2048 * 512 * 4;
constexpr size_t WS_H2 = WS_X1 + (size_t)2048 * 512 * 2;
constexpr size_t WS_H1 = WS_H2 + (size_t)64 * 32 * 1024;

DI unsigned pk2(float a, float b) { f32x2 v = {a, b}; bf16x2_t r = __builtin_convertvector(v, bf16x2_t); return __builtin_bit_cast(unsigned, r); }
DI float bf2f(u16 v) { return __uint_as_float(((unsigned)v) << 16); }
DI float bfr(float x) { return __uint_as_float(pk2(x, 0.f) << 16); }
DI float bflo(unsigned w) { return __uint_as_float(w << 16); }
DI float bfhi(unsigned w) { return __uint_as_float(w & 0xffff0000u); }
DI bf16x8 pack8(const f32x16& a, int s) {
    u32x4 w = {pk2(a[8 * s], a[8 * s + 1]), pk2(a[8 * s + 2], a[8 * s + 3]), pk2(a[8 * s + 4], a[8 * s + 5]), pk2(a[8 * s + 6], a[8 * s + 7])};
    return __builtin_bit_cast(bf16x8, w);
}
DI int crow(int r, int hi) { return (r & 3) + 8 * (r >> 2) + 4 * hi; }
DI void glds16(const void* g, unsigned lds_base) {
    unsigned sv;
    asm volatile("s_mov_b32 %0, m0\n\ts_mov_b32 m0, %2\n\ts_nop 0\n\tglobal_load_lds_dwordx4 %1, off\n\ts_mov_b32 m0, %0"
                 : "=&s"(sv) : "v"(g), "s"(lds_base) : "memory");
}
DI bf16x8 ldsb128(lds_cptr p) { return *(const __attribute__((address_space(3))) bf16x8*)p; }
DI s16x4 ldstr(lds_cptr p) { return __builtin_bit_cast(s16x4, __builtin_amdgcn_ds_read_tr16_b64_v4i16((__attribute__((address_space(3))) v4i16_t*)p)); }
DI bf16x8 wfrag(lds_cptr p0, lds_cptr p1) { s16x4 lo = ldstr(p0), hi = ldstr(p1); return __builtin_shufflevector(lo, hi, 0, 1, 2, 3, 4, 5, 6, 7); }
#define WAIT_ALL_BAR() asm volatile("s_waitcnt vmcnt(0) lgkmcnt(0)\n\ts_barrier" ::: "memory")
#define BAR_LGKM() asm volatile("s_waitcnt lgkmcnt(0)\n\ts_barrier" ::: "memory")
DI float wave_sum(float v) {
#pragma unroll
    for (int m = 1; m < 64; m <<= 1) v += __shfl_xor(v, m);
    return v;
}
DI float half_swap_max(float m) { auto rr = __builtin_amdgcn_permlane32_swap(__float_as_uint(m), __float_as_uint(m), false, false); return fmaxf(__uint_as_float(rr[0]), __uint_as_float(rr[1])); }
DI float half_swap_sum(float m) { auto rr = __builtin_amdgcn_permlane32_swap(__float_as_uint(m), __float_as_uint(m), false, false); return __uint_as_float(rr[0]) + __uint_as_float(rr[1]); }

DI void ln_row(const float (&xv)[8], float (&o)[8]) {
    float s = 0.f;
#pragma unroll
    for (int j = 0; j < 8; ++j) s += xv[j];
    const float mean = wave_sum(s) * (1.f / 512.f);
    float q = 0.f;
#pragma unroll
    for (int j = 0; j < 8; ++j) { const float d = xv[j] - mean; q += d * d; }
    const float var = wave_sum(q) * (1.f / 512.f);
    const float mb = bfr(mean), vb = bfr(var);
    const float den = bfr(sqrtf(bfr(vb + 1e-5f)));
#pragma unroll
    for (int j = 0; j < 8; ++j) o[j] = bfr(bfr(xv[j] - mb) / den);
}

constexpr int K1_LDS = 131072;
template <bool VT>
DI void k1_gemm(const u16* __restrict__ ckv, const u16* __restrict__ W, char* __restrict__ kvimg, char* lds, int key0, int h0, int h1) {
    const int tid = threadIdx.x, l = tid & 63, r32 = l & 31, hi = l >> 5;
    const int w = __builtin_amdgcn_readfirstlane(tid >> 6);
    const int wk = w >> 2, wc = w & 3;
    const unsigned lds0 = (unsigned)(uintptr_t)lds;
    f32x16 acc[4][2];
#pragma unroll
    for (int a = 0; a < 4; ++a) { acc[a][0] = f32x16{}; acc[a][1] = f32x16{}; }
    const int q4 = (l & 15) >> 2, p4 = l & 3, blk = (l >> 4) & 1;
    const int xa = (l >> 1) & 7;
    for (int kt = 0; kt < 8; ++kt) {
        WAIT_ALL_BAR();
        if (kt + 1 < 8) {
            const unsigned bb = lds0 + ((kt + 1) & 1) * 65536;
#pragma unroll
            for (int n = 0; n < 4; ++n) {
                const int P = n * 8 + w, row = 8 * P + (l >> 3), c = (l & 7) ^ ((row >> 1) & 7);
                glds16(ckv + (size_t)(key0 + row) * 512 + (kt + 1) * 64 + c * 8, bb + P * 1024);
            }
#pragma unroll
            for (int n = 0; n < 4; ++n) {
                const int P = n * 8 + w, half = P >> 4, row = 4 * (P & 15) + (l >> 4), c = (l & 15) ^ ((row & 3) << 2);
                glds16(W + (size_t)((kt + 1) * 64 + row) * 4096 + (half ? h1 : h0) * 128 + c * 8, bb + 32768 + P * 1024);
            }
        }
        lds_cptr ab = (lds_cptr)lds + (kt & 1) * 65536;
        lds_cptr wb = ab + 32768 + (wc >> 1) * 16384;
#pragma unroll
        for (int s = 0; s < 4; ++s) {
            bf16x8 af[4], wf[2];
#pragma unroll
            for (int kb = 0; kb < 4; ++kb) af[kb] = ldsb128(ab + (128 * wk + 32 * kb + r32) * 128 + (((2 * s + hi) ^ xa) << 4));
#pragma unroll
            for (int j = 0; j < 2; ++j) {
                const int db = 2 * (wc & 1) + j;
                lds_cptr p = wb + (16 * s + 8 * hi + q4) * 256 + 64 * (db ^ q4) + 32 * blk + 16 * (p4 >> 1) + 8 * (p4 & 1);
                wf[j] = wfrag(p, p + 1024);
            }
#pragma unroll
            for (int kb = 0; kb < 4; ++kb)
#pragma unroll
                for (int j = 0; j < 2; ++j) {
                    if (VT) acc[kb][j] = MFMA(af[kb], wf[j], acc[kb][j]);
                    else acc[kb][j] = MFMA(wf[j], af[kb], acc[kb][j]);
                }
        }
    }
    const int head = (wc >> 1) ? h1 : h0;
#pragma unroll
    for (int kb = 0; kb < 4; ++kb) {
        const int key = key0 + 128 * wk + 32 * kb, tile = key >> 6, kbl = kb & 1;
        char* tb = kvimg + ((size_t)(head * 32 + tile) << 15);
#pragma unroll
        for (int j = 0; j < 2; ++j) {
            const int db = 2 * (wc & 1) + j;
#pragma unroll
            for (int s2 = 0; s2 < 2; ++s2) {
                const int fo = VT ? (16384 + (db * 4 + kbl * 2 + s2) * 1024) : ((kbl * 8 + db * 2 + s2) * 1024);
                *(bf16x8*)(tb + fo + l * 16) = pack8(acc[kb][j], s2);
            }
        }
    }
}

__global__ __launch_bounds__(512, 2) void k1_kvproj(const u16* __restrict__ ckv, const u16* __restrict__ wuk, const u16* __restrict__ wuv,
                                                    const u16* __restrict__ x, const u16* __restrict__ cvec, const u16* __restrict__ wmod,
                                                    char* __restrict__ ws) {
    extern __shared__ __attribute__((aligned(16))) char lds[];
    const int b = blockIdx.x, tid = threadIdx.x, l = tid & 63;
    const int w = __builtin_amdgcn_readfirstlane(tid >> 6);
    const int xcd = b & 7, r = b >> 3, kb8 = r & 7, combo = r >> 3, type = combo & 1, hp = combo >> 1;
    const int h0 = xcd + 16 * hp, h1 = h0 + 8, key0 = kb8 * 256;
    const u16* W = type ? wuv : wuk;
    const unsigned lds0 = (unsigned)(uintptr_t)lds;
#pragma unroll
    for (int n = 0; n < 4; ++n) {
        const int P = n * 8 + w, row = 8 * P + (l >> 3), c = (l & 7) ^ ((row >> 1) & 7);
        glds16(ckv + (size_t)(key0 + row) * 512 + c * 8, lds0 + P * 1024);
    }
#pragma unroll
    for (int n = 0; n < 4; ++n) {
        const int P = n * 8 + w, half = P >> 4, row = 4 * (P & 15) + (l >> 4), c = (l & 15) ^ ((row & 3) << 2);
        glds16(W + (size_t)row * 4096 + (half ? h1 : h0) * 128 + c * 8, lds0 + 32768 + P * 1024);
    }
    if (b < 192) {
        float* scr = (float*)(lds + 65536);
        const int half = tid & 1, rowi = tid >> 1;
        float a8[8];
#pragma unroll
        for (int j = 0; j < 8; ++j) a8[j] = 0.f;
#pragma unroll
        for (int p = 0; p < 2; ++p) {
            const int row = rowi + 256 * p;
            const u32x4 wv = *(const u32x4*)(wmod + (size_t)row * 3072 + 16 * b + 8 * half);
            const float cv = bf2f(cvec[row]);
            const float sc = bfr(cv / (1.f + __expf(-cv)));
#pragma unroll
            for (int j = 0; j < 4; ++j) { a8[2 * j] += sc * bflo(wv[j]); a8[2 * j + 1] += sc * bfhi(wv[j]); }
        }
#pragma unroll
        for (int j = 0; j < 8; ++j)
#pragma unroll
            for (int m = 2; m < 64; m <<= 1) a8[j] += __shfl_xor(a8[j], m);
        if (l < 2) {
#pragma unroll
            for (int j = 0; j < 8; ++j) scr[(w * 2 + l) * 8 + j] = a8[j];
        }
        BAR_LGKM();
        if (tid < 16) {
            float s = 0.f;
#pragma unroll
            for (int ww = 0; ww < 8; ++ww) s += scr[(ww * 2 + (tid >> 3)) * 8 + (tid & 7)];
            ((float*)(ws + WS_MOD))[16 * b + tid] = bfr(s);
        }
    }
    {
        const int row = 8 * b + w;
        const u32x4 xv4 = *(const u32x4*)(x + (size_t)row * 512 + 8 * l);
        float xv[8], o[8];
#pragma unroll
        for (int j = 0; j < 4; ++j) { xv[2 * j] = bflo(xv4[j]); xv[2 * j + 1] = bfhi(xv4[j]); }
        ln_row(xv, o);
        u32x4 ov = {pk2(o[0], o[1]), pk2(o[2], o[3]), pk2(o[4], o[5]), pk2(o[6], o[7])};
        *(u32x4*)(ws + WS_XH + ((size_t)((row >> 5) * 32 + (l >> 1)) * 64 + (row & 31) + 32 * (l & 1)) * 16) = ov;
    }
    if (type) k1_gemm<true>(ckv, W, ws + WS_KV, lds, key0, h0, h1);
    else k1_gemm<false>(ckv, W, ws + WS_KV, lds, key0, h0, h1);
}

constexpr int K2_LDS = 131072 + 4096;
constexpr float QSCALE = 0.08838834764831845f * 1.4426950408889634f;
__global__ __launch_bounds__(512, 2) void k2_attn(const u16* __restrict__ wq, char* __restrict__ ws) {
    extern __shared__ __attribute__((aligned(16))) char lds[];
    const int b = blockIdx.x, tid = threadIdx.x, l = tid & 63, r32 = l & 31, hi = l >> 5;
    const int w = __builtin_amdgcn_readfirstlane(tid >> 6);
    const int xcd = b & 7, kk = b >> 3, head = xcd + 8 * (kk >> 3), pi = kk & 7;
    const int qb = (w < 4) ? pi : 15 - pi, sub = w & 3, rb = qb * 4 + sub;
    const int tl = 2 * qb + (sub >> 1), NT = 32 - 2 * pi;
    const unsigned lds0 = (unsigned)(uintptr_t)lds;
    const float* mod = (const float*)(ws + WS_MOD);
#pragma unroll
    for (int n = 0; n < 16; ++n) {
        const int P = n * 8 + w, row = 4 * P + (l >> 4), c = (l & 15) ^ ((row & 3) << 2);
        glds16(wq + (size_t)row * 4096 + head * 128 + c * 8, lds0 + P * 1024);
    }
    float* gs = (float*)(lds + 131072);
    gs[tid] = bfr(1.f + mod[512 + tid]);
    gs[512 + tid] = mod[tid];
    bf16x8 xf[32];
#pragma unroll
    for (int ks = 0; ks < 32; ++ks) xf[ks] = *(const bf16x8*)(ws + WS_XH + ((size_t)(rb * 32 + ks) * 64 + l) * 16);
    WAIT_ALL_BAR();
    const int q4 = (l & 15) >> 2, p4 = l & 3, blk = (l >> 4) & 1;
    bf16x8 qf[8];
    {
        f32x16 qa[4];
#pragma unroll
        for (int d = 0; d < 4; ++d) qa[d] = f32x16{};
#pragma unroll
        for (int ks = 0; ks < 32; ++ks) {
            const f32x4 g0 = *(const f32x4*)(gs + 16 * ks + 8 * hi), g1 = *(const f32x4*)(gs + 16 * ks + 8 * hi + 4);
            const f32x4 s0 = *(const f32x4*)(gs + 512 + 16 * ks + 8 * hi), s1 = *(const f32x4*)(gs + 512 + 16 * ks + 8 * hi + 4);
            const u32x4 xw = __builtin_bit_cast(u32x4, xf[ks]);
            u32x4 hw = {pk2(fmaf(bflo(xw[0]), g0[0], s0[0]), fmaf(bfhi(xw[0]), g0[1], s0[1])),
                        pk2(fmaf(bflo(xw[1]), g0[2], s0[2]), fmaf(bfhi(xw[1]), g0[3], s0[3])),
                        pk2(fmaf(bflo(xw[2]), g1[0], s1[0]), fmaf(bfhi(xw[2]), g1[1], s1[1])),
                        pk2(fmaf(bflo(xw[3]), g1[2], s1[2]), fmaf(bfhi(xw[3]), g1[3], s1[3]))};
            const bf16x8 hf = __builtin_bit_cast(bf16x8, hw);
#pragma unroll
            for (int db = 0; db < 4; ++db) {
                lds_cptr p = (lds_cptr)lds + (16 * ks + 8 * hi + q4) * 256 + 64 * (db ^ q4) + 32 * blk + 16 * (p4 >> 1) + 8 * (p4 & 1);
                qa[db] = MFMA(wfrag(p, p + 1024), hf, qa[db]);
            }
        }
#pragma unroll
        for (int db = 0; db < 4; ++db) {
#pragma unroll
            for (int r = 0; r < 16; ++r) qa[db][r] *= QSCALE;
            qf[2 * db] = pack8(qa[db], 0); qf[2 * db + 1] = pack8(qa[db], 1);
        }
    }
    BAR_LGKM();
    const char* kvh = ws + WS_KV + ((size_t)head << 20);
#pragma unroll
    for (int n = 0; n < 4; ++n) glds16(kvh + (n * 8 + w) * 1024 + l * 16, lds0 + (n * 8 + w) * 1024);
    float m_run = -1e30f, l_run = 0.f;
    f32x16 o[4];
#pragma unroll
    for (int d = 0; d < 4; ++d) o[d] = f32x16{};
    const int qrow = rb * 32 + r32;
    for (int t = 0; t < NT; ++t) {
        WAIT_ALL_BAR();
        if (t + 1 < NT) {
            const unsigned sb = lds0 + ((t + 1) & 1) * 32768;
#pragma unroll
            for (int n = 0; n < 4; ++n) glds16(kvh + (size_t)(t + 1) * 32768 + (n * 8 + w) * 1024 + l * 16, sb + (n * 8 + w) * 1024);
        }
        if (t <= tl) {
            lds_cptr kb_ = (lds_cptr)lds + (t & 1) * 32768 + l * 16;
            f32x16 s0 = f32x16{}, s1 = f32x16{};
#pragma unroll
            for (int ds = 0; ds < 8; ++ds) {
                s0 = MFMA(ldsb128(kb_ + ds * 1024), qf[ds], s0);
                s1 = MFMA(ldsb128(kb_ + 8192 + ds * 1024), qf[ds], s1);
            }
            if (t == tl) {
                const int dq = qrow - t * 64 - 4 * hi;
#pragma unroll
                for (int r = 0; r < 16; ++r) {
                    const int c = (r & 3) + 8 * (r >> 2);
                    if (c > dq) s0[r] = -INFINITY;
                    if (c + 32 > dq) s1[r] = -INFINITY;
                }
            }
            float mx = s0[0];
#pragma unroll
            for (int r = 1; r < 16; ++r) mx = fmaxf(mx, s0[r]);
#pragma unroll
            for (int r = 0; r < 16; ++r) mx = fmaxf(mx, s1[r]);
            mx = half_swap_max(mx);
            const float mn = fmaxf(m_run, mx), alpha = __builtin_amdgcn_exp2f(m_run - mn);
            m_run = mn;
            float ps = 0.f;
#pragma unroll
            for (int r = 0; r < 16; ++r) { s0[r] = __builtin_amdgcn_exp2f(s0[r] - mn); s1[r] = __builtin_amdgcn_exp2f(s1[r] - mn); ps += s0[r] + s1[r]; }
            l_run = l_run * alpha + ps;
#pragma unroll
            for (int d = 0; d < 4; ++d)
#pragma unroll
                for (int r = 0; r < 16; ++r) o[d][r] *= alpha;
            const bf16x8 pf0 = pack8(s0, 0), pf1 = pack8(s0, 1), pf2 = pack8(s1, 0), pf3 = pack8(s1, 1);
            lds_cptr vb_ = kb_ + 16384;
#pragma unroll
            for (int d = 0; d < 4; ++d) {
                o[d] = MFMA(ldsb128(vb_ + (d * 4 + 0) * 1024), pf0, o[d]);
                o[d] = MFMA(ldsb128(vb_ + (d * 4 + 1) * 1024), pf1, o[d]);
                o[d] = MFMA(ldsb128(vb_ + (d * 4 + 2) * 1024), pf2, o[d]);
                o[d] = MFMA(ldsb128(vb_ + (d * 4 + 3) * 1024), pf3, o[d]);
            }
        }
    }
    const float inv = 1.f / half_swap_sum(l_run);
    char* cb = ws + WS_CTX + ((size_t)(rb * 256 + head * 8) * 64 + l) * 16;
#pragma unroll
    for (int d = 0; d < 4; ++d) {
#pragma unroll
        for (int r = 0; r < 16; ++r) o[d][r] *= inv;
        *(bf16x8*)(cb + (2 * d) * 1024) = pack8(o[d], 0);
        *(bf16x8*)(cb + (2 * d + 1) * 1024) = pack8(o[d], 1);
    }
}

template <int TQ, int TN, int KW, int BKS, bool APERM, bool CT>
struct G {
    static constexpr int NWAVE = TQ * TN * KW, RB = TN * 128, A_BYTES = 2 * TQ * BKS * 1024, W_BYTES = 16 * BKS * RB, STAGE = A_BYTES + W_BYTES;
    static constexpr int CPR = RB / 16;
    static DI int swz(int row) { return RB == 256 ? ((row & 3) << 2) : (((row >> 1) & 1) << 2); }
    static DI void stage(const char* AF, size_t a_rb_stride, const u16* Wp, int ldw, int kst, unsigned ldsaddr, int w, int l) {
#pragma unroll
        for (int p0 = 0; p0 < 2 * TQ * BKS; p0 += NWAVE) {
            const int p = p0 + w, rbi = p / BKS, ks = p % BKS;
            glds16(AF + rbi * a_rb_stride + (size_t)(kst * BKS + ks) * 1024 + l * 16, ldsaddr + p * 1024);
        }
#pragma unroll
        for (int p0 = 0; p0 < W_BYTES / 1024; p0 += NWAVE) {
            const int p = p0 + w, row = p * (1024 / RB) + l / CPR, c = (l % CPR) ^ swz(row);
            glds16(Wp + (size_t)(kst * BKS * 16 + row) * ldw + c * 8, ldsaddr + A_BYTES + p * 1024);
        }
    }
    static DI void run(const char* AF, size_t a_rb_stride, const u16* Wp, int ldw, int nstage, char* lds, float (&red)[64 / KW], int& tq, int& tn, int& kw) {
        const int tid = threadIdx.x, l = tid & 63, hi = l >> 5;
        const int w = __builtin_amdgcn_readfirstlane(tid >> 6);
        kw = w % KW; const int tt = w / KW; tq = tt / TN; tn = tt % TN;
        const unsigned lds0 = (unsigned)(uintptr_t)lds;
        const int q4 = (l & 15) >> 2, p4 = l & 3, blk = (l >> 4) & 1;
        f32x16 acc[2][2];
#pragma unroll
        for (int i = 0; i < 2; ++i) { acc[i][0] = f32x16{}; acc[i][1] = f32x16{}; }
        stage(AF, a_rb_stride, Wp, ldw, 0, lds0, w, l);
        for (int st = 0; st < nstage; ++st) {
            WAIT_ALL_BAR();
            if (st + 1 < nstage) stage(AF, a_rb_stride, Wp, ldw, st + 1, lds0 + ((st + 1) & 1) * STAGE, w, l);
            lds_cptr ab = (lds_cptr)lds + (st & 1) * STAGE;
            lds_cptr wb = ab + A_BYTES;
#pragma unroll
            for (int kk = 0; kk < BKS / KW; ++kk) {
                const int ks = kw * (BKS / KW) + kk;
                bf16x8 af[2], wf[2];
#pragma unroll
                for (int i = 0; i < 2; ++i) af[i] = ldsb128(ab + ((2 * tq + i) * BKS + ks) * 1024 + l * 16);
#pragma unroll
                for (int j = 0; j < 2; ++j) {
                    const int r0 = 16 * ks + (APERM ? 4 * hi : 8 * hi) + q4, r1 = r0 + (APERM ? 8 : 4);
                    const int ch = 4 * (2 * tn + j) + 2 * blk + (p4 >> 1);
                    wf[j] = wfrag(wb + r0 * RB + ((ch ^ swz(r0)) << 4) + 8 * (p4 & 1), wb + r1 * RB + ((ch ^ swz(r1)) << 4) + 8 * (p4 & 1));
                }
#pragma unroll
                for (int i = 0; i < 2; ++i)
#pragma unroll
                    for (int j = 0; j < 2; ++j) {
                        if (CT) acc[i][j] = MFMA(wf[j], af[i], acc[i][j]);
                        else acc[i][j] = MFMA(af[i], wf[j], acc[i][j]);
                    }
            }
        }
        BAR_LGKM();
        float* rb_ = (float*)lds;
#pragma unroll
        for (int i = 0; i < 2; ++i)
#pragma unroll
            for (int j = 0; j < 2; ++j)
#pragma unroll
                for (int r = 0; r < 16; ++r) rb_[(w * 64 + (i * 2 + j) * 16 + r) * 64 + l] = acc[i][j][r];
        BAR_LGKM();
#pragma unroll
        for (int e = 0; e < 64 / KW; ++e) {
            float s = 0.f;
#pragma unroll
            for (int k2 = 0; k2 < KW; ++k2) s += rb_[((tt * KW + k2) * 64 + kw * (64 / KW) + e) * 64 + l];
            red[e] = s;
        }
    }
};

typedef G<2, 2, 2, 4, true, false> G3;
constexpr int K3_LDS = 131072;
__global__ __launch_bounds__(512, 2) void k3_oproj(const u16* __restrict__ wo, char* __restrict__ ws) {
    extern __shared__ __attribute__((aligned(16))) char lds[];
    const int b = blockIdx.x, xcd = b & 7, r = b >> 3, ksl = xcd & 3, rp = (xcd >> 2) * 8 + (r >> 2), ct = r & 3;
    const int l = threadIdx.x & 63, hi = l >> 5;
    float red[32]; int tq, tn, kw;
    G3::run(ws + WS_CTX + ((size_t)(rp * 4) * 256 + ksl * 64) * 1024, (size_t)256 * 1024, wo + (size_t)ksl * 1024 * 512 + ct * 128, 512, 16, lds, red, tq, tn, kw);
    float* part = (float*)(ws + WS_PART) + (size_t)ksl * 2048 * 512;
#pragma unroll
    for (int e = 0; e < 32; ++e) {
        const int idx = kw * 32 + e, i = idx >> 5, j = (idx >> 4) & 1, rr = idx & 15;
        const int row = rp * 128 + 64 * tq + 32 * i + crow(rr, hi), col = ct * 128 + 64 * tn + 32 * j + (l & 31);
        part[(size_t)row * 512 + col] = red[e];
    }
}

__global__ __launch_bounds__(512) void k3b_ln2(const u16* __restrict__ x, char* __restrict__ ws) {
    const int l = threadIdx.x & 63, row = blockIdx.x * 8 + (threadIdx.x >> 6);
    const float* part = (const float*)(ws + WS_PART) + (size_t)row * 512 + 8 * l;
    const float* mod = (const float*)(ws + WS_MOD);
    float a[8];
#pragma unroll
    for (int j = 0; j < 8; ++j) a[j] = 0.f;
#pragma unroll
    for (int s = 0; s < 4; ++s) {
        const f32x4 v0 = *(const f32x4*)(part + (size_t)s * 2048 * 512), v1 = *(const f32x4*)(part + (size_t)s * 2048 * 512 + 4);
#pragma unroll
        for (int j = 0; j < 4; ++j) { a[j] += v0[j]; a[4 + j] += v1[j]; }
    }
    const u32x4 xv4 = *(const u32x4*)(x + (size_t)row * 512 + 8 * l);
    float x1[8], xh[8];
#pragma unroll
    for (int j = 0; j < 8; ++j) {
        const float xv = (j & 1) ? bfhi(xv4[j >> 1]) : bflo(xv4[j >> 1]);
        x1[j] = bfr(xv + bfr(mod[1024 + 8 * l + j] * bfr(a[j])));
    }
    u32x4 o1 = {pk2(x1[0], x1[1]), pk2(x1[2], x1[3]), pk2(x1[4], x1[5]), pk2(x1[6], x1[7])};
    *(u32x4*)(ws + WS_X1 + ((size_t)row * 512 + 8 * l) * 2) = o1;
    ln_row(x1, xh);
    float h[8];
#pragma unroll
    for (int j = 0; j < 8; ++j) h[j] = bfr(xh[j] * bfr(1.f + mod[2048 + 8 * l + j])) + mod[1536 + 8 * l + j];
    u32x4 oh = {pk2(h[0], h[1]), pk2(h[2], h[3]), pk2(h[4], h[5]), pk2(h[6], h[7])};
    *(u32x4*)(ws + WS_H2 + ((size_t)((row >> 5) * 32 + (l >> 1)) * 64 + (row & 31) + 32 * (l & 1)) * 16) = oh;
}

typedef G<2, 2, 2, 4, false, true> G4;
__global__ __launch_bounds__(512, 2) void k4_ffn1(const u16* __restrict__ w1, char* __restrict__ ws) {
    extern __shared__ __attribute__((aligned(16))) char lds[];
    const int b = blockIdx.x, rp = b / 14, ct = b % 14;
    const int l = threadIdx.x & 63;
    float red[32]; int tq, tn, kw;
    G4::run(ws + WS_H2 + (size_t)(rp * 4) * 32 * 1024, (size_t)32 * 1024, w1 + ct * 128, DFF, 8, lds, red, tq, tn, kw);
#pragma unroll
    for (int j = 0; j < 2; ++j)
#pragma unroll
        for (int s = 0; s < 2; ++s) {
            const int e = j * 16 + 8 * s;
            u32x4 v = {pk2(fmaxf(red[e], 0.f), fmaxf(red[e + 1], 0.f)), pk2(fmaxf(red[e + 2], 0.f), fmaxf(red[e + 3], 0.f)),
                       pk2(fmaxf(red[e + 4], 0.f), fmaxf(red[e + 5], 0.f)), pk2(fmaxf(red[e + 6], 0.f), fmaxf(red[e + 7], 0.f))};
            const int rbq = rp * 4 + 2 * tq + kw, kstep = (ct * 128 + 64 * tn + 32 * j) / 16 + s;
            *(u32x4*)(ws + WS_H1 + ((size_t)(rbq * 112 + kstep) * 64 + l) * 16) = v;
        }
}

typedef G<1, 1, 8, 8, true, false> G5;
__global__ __launch_bounds__(512, 2) void k5_ffn2(const u16* __restrict__ w2, char* __restrict__ ws, u16* __restrict__ out) {
    extern __shared__ __attribute__((aligned(16))) char lds[];
    const int b = blockIdx.x, rp = b >> 3, ct = b & 7;
    const int l = threadIdx.x & 63, hi = l >> 5;
    float red[8]; int tq, tn, kw;
    G5::run(ws + WS_H1 + (size_t)(rp * 2) * 112 * 1024, (size_t)112 * 1024, w2 + ct * 64, 512, 14, lds, red, tq, tn, kw);
    const float* mod = (const float*)(ws + WS_MOD);
    const u16* x1 = (const u16*)(ws + WS_X1);
#pragma unroll
    for (int e = 0; e < 8; ++e) {
        const int idx = kw * 8 + e, i = idx >> 5, j = (idx >> 4) & 1, rr = idx & 15;
        const int row = rp * 64 + 32 * i + crow(rr, hi), col = ct * 64 + 32 * j + (l & 31);
        const float v = bfr(bf2f(x1[(size_t)row * 512 + col]) + bfr(mod[2560 + col] * bfr(red[e])));
        out[(size_t)row * 512 + col] = (u16)(__float_as_uint(v) >> 16);
    }
}

extern "C" void kernel_launch(void* const* d_in, const int* in_sizes, int n_in, void* d_out, int out_size, void* d_ws, size_t ws_size,
                              hipStream_t stream) {
    (void)in_sizes; (void)n_in; (void)out_size; (void)ws_size;
    const u16* x = (const u16*)d_in[0];
    const u16* ckv = (const u16*)d_in[1];
    const u16* wq = (const u16*)d_in[2];
    const u16* wuk = (const u16*)d_in[3];
    const u16* wuv = (const u16*)d_in[4];
    const u16* wo = (const u16*)d_in[5];
    const u16* w1 = (const u16*)d_in[7];
    const u16* w2 = (const u16*)d_in[8];
    const u16* cvec = (const u16*)d_in[9];
    const u16* wmod = (const u16*)d_in[10];
    char* ws = (char*)d_ws;
    hipFuncSetAttribute((const void*)k1_kvproj, hipFuncAttributeMaxDynamicSharedMemorySize, K1_LDS);
    hipFuncSetAttribute((const void*)k2_attn, hipFuncAttributeMaxDynamicSharedMemorySize, K2_LDS);
    hipFuncSetAttribute((const void*)k3_oproj, hipFuncAttributeMaxDynamicSharedMemorySize, K3_LDS);
    hipFuncSetAttribute((const void*)k4_ffn1, hipFuncAttributeMaxDynamicSharedMemorySize, K3_LDS);
    hipFuncSetAttribute((const void*)k5_ffn2, hipFuncAttributeMaxDynamicSharedMemorySize, K3_LDS);
    k1_kvproj<<<256, 512, K1_LDS, stream>>>(ckv, wuk, wuv, x, cvec, wmod, ws);
    k2_attn<<<256, 512, K2_LDS, stream>>>(wq, ws);
    k3_oproj<<<256, 512, K3_LDS, stream>>>(wo, ws);
    k3b_ln2<<<256, 512, 0, stream>>>(x, ws);
    k4_ffn1<<<224, 512, K3_LDS, stream>>>(w1, ws);
    k5_ffn2<<<256, 512, K3_LDS, stream>>>(w2, ws, (u16*)d_out);
}
```

```cpp
#include <hip/hip_runtime.h>
#include <hip/hip_bf16.h>
#include <stdint.h>

typedef unsigned short u16;
typedef short bf16x8 __attribute__((ext_vector_type(8)));
typedef short s16x4 __attribute__((ext_vector_type(4)));
typedef short v4i16_t __attribute__((ext_vector_type(4)));
typedef float f32x16 __attribute__((ext_vector_type(16)));
typedef float f32x4 __attribute__((ext_vector_type(4)));
typedef float f32x2 __attribute__((ext_vector_type(2)));
typedef unsigned u32x4 __attribute__((ext_vector_type(4)));
typedef __bf16 bf16x2_t __attribute__((ext_vector_type(2)));
typedef __attribute__((address_space(3))) const char* lds_cptr;
#define DI __device__ __forceinline__
#define MFMA(a, b, c) __builtin_amdgcn_mfma_f32_32x32x16_bf16((a), (b), (c), 0, 0, 0)
#define SBAR() __builtin_amdgcn_sched_barrier(0)
#define STAMP_AT(ws_, kid, idx) do { } while (0)

constexpr int SEQ = 2048, DM = 512, NH = 32, HD = 128, DFF = 1792;

constexpr size_t WS_KV = 0;
constexpr size_t WS_XH = WS_KV + (size_t)32 * 32 * 32768;
constexpr size_t WS_MOD = WS_XH + (size_t)64 * 32 * 1024;
constexpr size_t WS_CTX = WS_MOD + 16384;
constexpr size_t WS_PART = WS_CTX + (size_t)64 * 256 * 1024;
constexpr size_t WS_X1 = WS_PART + (size_t)4 * 2048 * 512 * 4;
constexpr size_t WS_H2 = WS_X1 + (size_t)2048 * 512 * 2;
constexpr size_t WS_H1 = WS_H2 + (size_t)64 * 32 * 1024;

DI unsigned pk2(float a, float b) { f32x2 v = {a, b}; bf16x2_t r = __builtin_convertvector(v, bf16x2_t); return __builtin_bit_cast(unsigned, r); }
DI float bf2f(u16 v) { return __uint_as_float(((unsigned)v) << 16); }
DI float bfr(float x) { return __uint_as_float(pk2(x, 0.f) << 16); }
DI float bflo(unsigned w) { return __uint_as_float(w << 16); }
DI float bfhi(unsigned w) { return __uint_as_float(w & 0xffff0000u); }
DI bf16x8 pack8(const f32x16& a, int s) {
    u32x4 w = {pk2(a[8 * s], a[8 * s + 1]), pk2(a[8 * s + 2], a[8 * s + 3]), pk2(a[8 * s + 4], a[8 * s + 5]), pk2(a[8 * s + 6], a[8 * s + 7])};
    return __builtin_bit_cast(bf16x8, w);
}
DI int crow(int r, int hi) { return (r & 3) + 8 * (r >> 2) + 4 * hi; }
DI void glds16(const void* g, unsigned lds_base) {
    unsigned sv;
    asm volatile("s_mov_b32 %0, m0\n\ts_mov_b32 m0, %2\n\ts_nop 0\n\tglobal_load_lds_dwordx4 %1, off\n\ts_mov_b32 m0, %0"
                 : "=&s"(sv) : "v"(g), "s"(lds_base) : "memory");
}
DI bf16x8 ldsb128(lds_cptr p) { return *(const __attribute__((address_space(3))) bf16x8*)p; }
DI s16x4 ldstr(lds_cptr p) { return __builtin_bit_cast(s16x4, __builtin_amdgcn_ds_read_tr16_b64_v4i16((__attribute__((address_space(3))) v4i16_t*)p)); }
DI bf16x8 wfrag(lds_cptr p0, lds_cptr p1) { s16x4 lo = ldstr(p0), hi = ldstr(p1); return __builtin_shufflevector(lo, hi, 0, 1, 2, 3, 4, 5, 6, 7); }
#define WAIT_ALL_BAR() asm volatile("s_waitcnt vmcnt(0) lgkmcnt(0)\n\ts_barrier" ::: "memory")
#define BAR_LGKM() asm volatile("s_waitcnt lgkmcnt(0)\n\ts_barrier" ::: "memory")
DI float wave_sum(float v) {
#pragma unroll
    for (int m = 1; m < 64; m <<= 1) v += __shfl_xor(v, m);
    return v;
}
DI float half_swap_max(float m) { auto rr = __builtin_amdgcn_permlane32_swap(__float_as_uint(m), __float_as_uint(m), false, false); return fmaxf(__uint_as_float(rr[0]), __uint_as_float(rr[1])); }
DI float half_swap_sum(float m) { auto rr = __builtin_amdgcn_permlane32_swap(__float_as_uint(m), __float_as_uint(m), false, false); return __uint_as_float(rr[0]) + __uint_as_float(rr[1]); }

DI void ln_row(const float (&xv)[8], float (&o)[8]) {
    float s = 0.f;
#pragma unroll
    for (int j = 0; j < 8; ++j) s += xv[j];
    const float mean = wave_sum(s) * (1.f / 512.f);
    float q = 0.f;
#pragma unroll
    for (int j = 0; j < 8; ++j) { const float d = xv[j] - mean; q += d * d; }
    const float var = wave_sum(q) * (1.f / 512.f);
    const float mb = bfr(mean), vb = bfr(var);
    const float den = bfr(sqrtf(bfr(vb + 1e-5f)));
#pragma unroll
    for (int j = 0; j < 8; ++j) o[j] = bfr(bfr(xv[j] - mb) / den);
}

constexpr int K1_LDS = 131072;
template <bool VT>
DI void k1_gemm(const u16* __restrict__ ckv, const u16* __restrict__ W, char* __restrict__ kvimg, char* lds, int key0, int h0, int h1) {
    const int tid = threadIdx.x, l = tid & 63, r32 = l & 31, hi = l >> 5;
    const int w = __builtin_amdgcn_readfirstlane(tid >> 6);
    const int wk = w >> 2, wc = w & 3;
    const unsigned lds0 = (unsigned)(uintptr_t)lds;
    f32x16 acc[4][2];
#pragma unroll
    for (int a = 0; a < 4; ++a) { acc[a][0] = f32x16{}; acc[a][1] = f32x16{}; }
    const int q4 = (l & 15) >> 2, p4 = l & 3, blk = (l >> 4) & 1;
    const int xa = (l >> 1) & 7;
    STAMP_AT(kvimg, 0, 1);
    for (int kt = 0; kt < 8; ++kt) {
        WAIT_ALL_BAR();
        if (kt == 0) STAMP_AT(kvimg, 0, 2);
        if (kt + 1 < 8) {
            const unsigned bb = lds0 + ((kt + 1) & 1) * 65536;
#pragma unroll
            for (int n = 0; n < 4; ++n) {
                const int P = n * 8 + w, row = 8 * P + (l >> 3), c = (l & 7) ^ ((row >> 1) & 7);
                glds16(ckv + (size_t)(key0 + row) * 512 + (kt + 1) * 64 + c * 8, bb + P * 1024);
            }
#pragma unroll
            for (int n = 0; n < 4; ++n) {
                const int P = n * 8 + w, half = P >> 4, row = 4 * (P & 15) + (l >> 4), c = (l & 15) ^ ((row & 3) << 2);
                glds16(W + (size_t)((kt + 1) * 64 + row) * 4096 + (half ? h1 : h0) * 128 + c * 8, bb + 32768 + P * 1024);
            }
        }
        lds_cptr ab = (lds_cptr)lds + (kt & 1) * 65536;
        lds_cptr wb = ab + 32768 + (wc >> 1) * 16384;
#pragma unroll
        for (int s = 0; s < 4; ++s) {
            bf16x8 af[4], wf[2];
#pragma unroll
            for (int kb = 0; kb < 4; ++kb) af[kb] = ldsb128(ab + (128 * wk + 32 * kb + r32) * 128 + (((2 * s + hi) ^ xa) << 4));
#pragma unroll
            for (int j = 0; j < 2; ++j) {
                const int db = 2 * (wc & 1) + j;
                lds_cptr p = wb + (16 * s + 8 * hi + q4) * 256 + 64 * (db ^ q4) + 32 * blk + 16 * (p4 >> 1) + 8 * (p4 & 1);
                wf[j] = wfrag(p, p + 1024);
            }
#pragma unroll
            for (int kb = 0; kb < 4; ++kb)
#pragma unroll
                for (int j = 0; j < 2; ++j) {
                    if (VT) acc[kb][j] = MFMA(af[kb], wf[j], acc[kb][j]);
                    else acc[kb][j] = MFMA(wf[j], af[kb], acc[kb][j]);
                }
        }
    }
    STAMP_AT(kvimg, 0, 3);
    const int head = (wc >> 1) ? h1 : h0;
#pragma unroll
    for (int kb = 0; kb < 4; ++kb) {
        const int key = key0 + 128 * wk + 32 * kb, tile = key >> 6, kbl = kb & 1;
        char* tb = kvimg + ((size_t)(head * 32 + tile) << 15);
#pragma unroll
        for (int j = 0; j < 2; ++j) {
            const int db = 2 * (wc & 1) + j;
#pragma unroll
            for (int s2 = 0; s2 < 2; ++s2) {
                const int fo = VT ? (16384 + (db * 4 + kbl * 2 + s2) * 1024) : ((kbl * 8 + db * 2 + s2) * 1024);
                *(bf16x8*)(tb + fo + l * 16) = pack8(acc[kb][j], s2);
            }
        }
    }
}

__global__ __launch_bounds__(512, 2) void k1_kvproj(const u16* __restrict__ ckv, const u16* __restrict__ wuk, const u16* __restrict__ wuv,
                                                    const u16* __restrict__ x, const u16* __restrict__ cvec, const u16* __restrict__ wmod,
                                                    char* __restrict__ ws) {
    extern __shared__ __attribute__((aligned(16))) char lds[];
    const int b = blockIdx.x, tid = threadIdx.x, l = tid & 63;
    const int w = __builtin_amdgcn_readfirstlane(tid >> 6);
    const int xcd = b & 7, r = b >> 3, kb8 = r & 7, combo = r >> 3, type = combo & 1, hp = combo >> 1;
    const int h0 = xcd + 16 * hp, h1 = h0 + 8, key0 = kb8 * 256;
    const u16* W = type ? wuv : wuk;
    const unsigned lds0 = (unsigned)(uintptr_t)lds;
    STAMP_AT(ws, 0, 0);
#pragma unroll
    for (int n = 0; n < 4; ++n) {
        const int P = n * 8 + w, row = 8 * P + (l >> 3), c = (l & 7) ^ ((row >> 1) & 7);
        glds16(ckv + (size_t)(key0 + row) * 512 + c * 8, lds0 + P * 1024);
    }
#pragma unroll
    for (int n = 0; n < 4; ++n) {
        const int P = n * 8 + w, half = P >> 4, row = 4 * (P & 15) + (l >> 4), c = (l & 15) ^ ((row & 3) << 2);
        glds16(W + (size_t)row * 4096 + (half ? h1 : h0) * 128 + c * 8, lds0 + 32768 + P * 1024);
    }
    if (b < 192) {
        float* scr = (float*)(lds + 65536);
        const int half = tid & 1, rowi = tid >> 1;
        float a8[8];
#pragma unroll
        for (int j = 0; j < 8; ++j) a8[j] = 0.f;
#pragma unroll
        for (int p = 0; p < 2; ++p) {
            const int row = rowi + 256 * p;
            const u32x4 wv = *(const u32x4*)(wmod + (size_t)row * 3072 + 16 * b + 8 * half);
            const float cv = bf2f(cvec[row]);
            const float sc = bfr(cv / (1.f + __expf(-cv)));
#pragma unroll
            for (int j = 0; j < 4; ++j) { a8[2 * j] += sc * bflo(wv[j]); a8[2 * j + 1] += sc * bfhi(wv[j]); }
        }
#pragma unroll
        for (int j = 0; j < 8; ++j)
#pragma unroll
            for (int m = 2; m < 64; m <<= 1) a8[j] += __shfl_xor(a8[j], m);
        if (l < 2) {
#pragma unroll
            for (int j = 0; j < 8; ++j) scr[(w * 2 + l) * 8 + j] = a8[j];
        }
        BAR_LGKM();
        if (tid < 16) {
            float s = 0.f;
#pragma unroll
            for (int ww = 0; ww < 8; ++ww) s += scr[(ww * 2 + (tid >> 3)) * 8 + (tid & 7)];
            ((float*)(ws + WS_MOD))[16 * b + tid] = bfr(s);
        }
    }
    {
        const int row = 8 * b + w;
        const u32x4 xv4 = *(const u32x4*)(x + (size_t)row * 512 + 8 * l);
        float xv[8], o[8];
#pragma unroll
        for (int j = 0; j < 4; ++j) { xv[2 * j] = bflo(xv4[j]); xv[2 * j + 1] = bfhi(xv4[j]); }
        ln_row(xv, o);
        u32x4 ov = {pk2(o[0], o[1]), pk2(o[2], o[3]), pk2(o[4], o[5]), pk2(o[6], o[7])};
        *(u32x4*)(ws + WS_XH + ((size_t)((row >> 5) * 32 + (l >> 1)) * 64 + (row & 31) + 32 * (l & 1)) * 16) = ov;
    }
    if (type) k1_gemm<true>(ckv, W, ws + WS_KV, lds, key0, h0, h1);
    else k1_gemm<false>(ckv, W, ws + WS_KV, lds, key0, h0, h1);
    STAMP_AT(ws, 0, 4);
}

constexpr float ATT_THR = 8.f;
#define PIN(x) asm volatile("" : "+v"(x))
#define MX3(a, b, c) __builtin_fmaxf(__builtin_fmaxf((a), (b)), (c))
DI float att_rowmax(const f32x16& p0, const f32x16& p1) {
    float a = MX3(p0[0], p0[1], p1[0]), b = MX3(p0[2], p0[3], p1[1]); a = MX3(a, p1[2], p1[3]);
#pragma unroll
    for (int r = 4; r < 16; r += 4) { a = MX3(a, p0[r], p0[r + 1]); b = MX3(b, p0[r + 2], p0[r + 3]); a = MX3(a, p1[r], p1[r + 1]); b = MX3(b, p1[r + 2], p1[r + 3]); }
    return __builtin_fmaxf(a, b);
}
DI void att_mask(f32x16& s0, f32x16& s1, int dq) {
#pragma unroll
    for (int r = 0; r < 16; ++r) {
        const int c = (r & 3) + 8 * (r >> 2);
        if (c > dq) s0[r] = -INFINITY;
        if (c + 32 > dq) s1[r] = -INFINITY;
    }
}
template <bool MASK>
DI void att_step(f32x16& C0, f32x16& C1, const f32x16& P0, const f32x16& P1, f32x16 (&o)[4], f32x16& negm, float& l_run,
                 const bf16x8 (&qf)[8], lds_cptr kp, lds_cptr vp, int dq) {
    bf16x8 kf[4], vf[4];
    u32x4 pw[4];
    kf[0] = ldsb128(kp); kf[1] = ldsb128(kp + 8192); kf[2] = ldsb128(kp + 1024);
    float sacc = 0.f;
    SBAR();
#pragma unroll
    for (int g = 0; g < 16; ++g) {
        if (g + 3 < 16) kf[(g + 3) & 3] = ldsb128(kp + (((g + 3) & 1) * 8 + ((g + 3) >> 1)) * 1024);
        else vf[g - 13] = ldsb128(vp + (g - 13) * 4096);
        if (g < 8) { sacc += P0[2 * g]; sacc += P0[2 * g + 1]; pw[g >> 2][g & 3] = pk2(P0[2 * g], P0[2 * g + 1]); PIN(pw[g >> 2]); }
        else { sacc += P1[2 * g - 16]; sacc += P1[2 * g - 15]; pw[g >> 2][g & 3] = pk2(P1[2 * g - 16], P1[2 * g - 15]); PIN(pw[g >> 2]); }
        PIN(sacc);
        if ((g & 1) == 0) C0 = MFMA(kf[g & 3], qf[g >> 1], g < 2 ? negm : C0);
        else C1 = MFMA(kf[g & 3], qf[g >> 1], g < 2 ? negm : C1);
        SBAR();
    }
    l_run += sacc;
    bool resc = false; float f = 1.f;
#pragma unroll
    for (int g = 0; g < 16; ++g) {
        if (g + 3 < 16) vf[(g + 3) & 3] = ldsb128(vp + ((((g + 3) & 3) * 4) + ((g + 3) >> 2)) * 1024);
        o[g & 3] = MFMA(vf[g & 3], __builtin_bit_cast(bf16x8, pw[g >> 2]), o[g & 3]);
        if (g == 2) {
            if (MASK) att_mask(C0, C1, dq);
            const float rm = att_rowmax(C0, C1);
            if (__builtin_expect(__any(rm > ATT_THR), 0)) {
                const float dl = __builtin_fmaxf(half_swap_max(rm), 0.f);
                f = __builtin_amdgcn_exp2f(-dl); l_run *= f; resc = true;
#pragma unroll
                for (int r = 0; r < 16; ++r) { C0[r] -= dl; C1[r] -= dl; negm[r] -= dl; }
            }
        }
        if (g >= 3 && g < 11) {
#pragma unroll
            for (int e = 0; e < 2; ++e) { C0[2 * (g - 3) + e] = __builtin_amdgcn_exp2f(C0[2 * (g - 3) + e]); C1[2 * (g - 3) + e] = __builtin_amdgcn_exp2f(C1[2 * (g - 3) + e]); }
            PIN(C0); PIN(C1);
        }
        SBAR();
    }
    if (__builtin_expect(resc, 0)) {
#pragma unroll
        for (int d = 0; d < 4; ++d)
#pragma unroll
            for (int r = 0; r < 16; ++r) o[d][r] *= f;
    }
}
constexpr int K2_LDS = 131072 + 4096;
constexpr float QSCALE = 0.08838834764831845f * 1.4426950408889634f;
__global__ __launch_bounds__(512, 2) void k2_attn(const u16* __restrict__ wq, char* __restrict__ ws) {
    extern __shared__ __attribute__((aligned(16))) char lds[];
    const int b = blockIdx.x, tid = threadIdx.x, l = tid & 63, r32 = l & 31, hi = l >> 5;
    const int w = __builtin_amdgcn_readfirstlane(tid >> 6);
    const int xcd = b & 7, kk = b >> 3, head = xcd + 8 * (kk >> 3), pi = kk & 7;
    const int qb = (w < 4) ? pi : 15 - pi, sub = w & 3, rb = qb * 4 + sub;
    const int tl = 2 * qb + (sub >> 1), NT = 32 - 2 * pi;
    const unsigned lds0 = (unsigned)(uintptr_t)lds;
    const float* mod = (const float*)(ws + WS_MOD);
    STAMP_AT(ws, 1, 0);
#pragma unroll
    for (int n = 0; n < 16; ++n) {
        const int P = n * 8 + w, row = 4 * P + (l >> 4), c = (l & 15) ^ ((row & 3) << 2);
        glds16(wq + (size_t)row * 4096 + head * 128 + c * 8, lds0 + P * 1024);
    }
    float* gs = (float*)(lds + 131072);
    gs[tid] = bfr(1.f + mod[512 + tid]);
    gs[512 + tid] = mod[tid];
    bf16x8 xf[32];
#pragma unroll
    for (int ks = 0; ks < 32; ++ks) xf[ks] = *(const bf16x8*)(ws + WS_XH + ((size_t)(rb * 32 + ks) * 64 + l) * 16);
    WAIT_ALL_BAR();
    STAMP_AT(ws, 1, 1);
    const int q4 = (l & 15) >> 2, p4 = l & 3, blk = (l >> 4) & 1;
    bf16x8 qf[8];
    {
        f32x16 qa[4];
#pragma unroll
        for (int d = 0; d < 4; ++d) qa[d] = f32x16{};
#pragma unroll
        for (int ks = 0; ks < 32; ++ks) {
            const f32x4 g0 = *(const f32x4*)(gs + 16 * ks + 8 * hi), g1 = *(const f32x4*)(gs + 16 * ks + 8 * hi + 4);
            const f32x4 s0 = *(const f32x4*)(gs + 512 + 16 * ks + 8 * hi), s1 = *(const f32x4*)(gs + 512 + 16 * ks + 8 * hi + 4);
            const u32x4 xw = __builtin_bit_cast(u32x4, xf[ks]);
            u32x4 hw = {pk2(fmaf(bflo(xw[0]), g0[0], s0[0]), fmaf(bfhi(xw[0]), g0[1], s0[1])),
                        pk2(fmaf(bflo(xw[1]), g0[2], s0[2]), fmaf(bfhi(xw[1]), g0[3], s0[3])),
                        pk2(fmaf(bflo(xw[2]), g1[0], s1[0]), fmaf(bfhi(xw[2]), g1[1], s1[1])),
                        pk2(fmaf(bflo(xw[3]), g1[2], s1[2]), fmaf(bfhi(xw[3]), g1[3], s1[3]))};
            const bf16x8 hf = __builtin_bit_cast(bf16x8, hw);
#pragma unroll
            for (int db = 0; db < 4; ++db) {
                lds_cptr p = (lds_cptr)lds + (16 * ks + 8 * hi + q4) * 256 + 64 * (db ^ q4) + 32 * blk + 16 * (p4 >> 1) + 8 * (p4 & 1);
                qa[db] = MFMA(wfrag(p, p + 1024), hf, qa[db]);
            }
        }
#pragma unroll
        for (int db = 0; db < 4; ++db) {
#pragma unroll
            for (int r = 0; r < 16; ++r) qa[db][r] *= QSCALE;
            qf[2 * db] = pack8(qa[db], 0); qf[2 * db + 1] = pack8(qa[db], 1);
        }
    }
    BAR_LGKM();
    STAMP_AT(ws, 1, 2);
    const char* kvh = ws + WS_KV + ((size_t)head << 20);
    const char* ksrc = kvh + w * 2048 + l * 16;
    const unsigned kdst = lds0 + w * 2048, vdst = lds0 + 49152 + w * 2048;
#define DMA_K(t_, slot_) do { const char* g_ = ksrc + (size_t)(t_) * 32768; glds16(g_, kdst + (slot_)); glds16(g_ + 1024, kdst + (slot_) + 1024); } while (0)
#define DMA_V(t_, slot_) do { const char* g_ = ksrc + (size_t)(t_) * 32768 + 16384; glds16(g_, vdst + (slot_)); glds16(g_ + 1024, vdst + (slot_) + 1024); } while (0)
#define WAIT_BAR4() asm volatile("s_waitcnt vmcnt(4) lgkmcnt(0)\n\ts_barrier" ::: "memory")
    DMA_K(0, 0); DMA_V(0, 0); DMA_K(1, 16384);
    float l_run = 0.f;
    f32x16 o[4], negm;
#pragma unroll
    for (int d = 0; d < 4; ++d) o[d] = f32x16{};
    const int qrow = rb * 32 + r32;
    const lds_cptr kp0 = (lds_cptr)lds + l * 16, vp0 = (lds_cptr)lds + 49152 + l * 16;
    f32x16 A0, A1, B0, B1;
    bf16x8 pf0, pf1, pf2, pf3;
    int sl_prev = 32768, sl_cur = 0, sl_next = 16384;
#define ROT() do { const int x_ = sl_prev; sl_prev = sl_cur; sl_cur = sl_next; sl_next = x_; } while (0)
#define STAGE_ISSUE(t_) do { const int tk_ = (t_) + 2 < NT ? (t_) + 2 : NT - 1, tv_ = (t_) + 1 < NT ? (t_) + 1 : NT - 1; DMA_K(tk_, sl_prev); DMA_V(tv_, sl_next); } while (0)
    WAIT_ALL_BAR();
    STAGE_ISSUE(0);
    {
        A0 = f32x16{}; A1 = f32x16{};
#pragma unroll
        for (int ds = 0; ds < 8; ++ds) {
            A0 = MFMA(ldsb128(kp0 + ds * 1024), qf[ds], A0);
            A1 = MFMA(ldsb128(kp0 + 8192 + ds * 1024), qf[ds], A1);
        }
        if (tl == 0) att_mask(A0, A1, qrow - 4 * hi);
        const float mx = half_swap_max(att_rowmax(A0, A1));
#pragma unroll
        for (int r = 0; r < 16; ++r) { A0[r] = __builtin_amdgcn_exp2f(A0[r] - mx); A1[r] = __builtin_amdgcn_exp2f(A1[r] - mx); negm[r] = -mx; }
    }
    WAIT_BAR4(); ROT();
    int t = 1;
    for (; t + 1 <= tl - 1; t += 2) {
        STAGE_ISSUE(t);     att_step<false>(B0, B1, A0, A1, o, negm, l_run, qf, kp0 + sl_cur, vp0 + sl_prev, 0); WAIT_BAR4(); ROT();
        STAGE_ISSUE(t + 1); att_step<false>(A0, A1, B0, B1, o, negm, l_run, qf, kp0 + sl_cur, vp0 + sl_prev, 0); WAIT_BAR4(); ROT();
    }
    if (t <= tl - 1) {
        STAGE_ISSUE(t); att_step<false>(B0, B1, A0, A1, o, negm, l_run, qf, kp0 + sl_cur, vp0 + sl_prev, 0); WAIT_BAR4(); ROT();
        A0 = B0; A1 = B1; ++t;
    }
    if (tl >= 1) {
        STAGE_ISSUE(t); att_step<true>(B0, B1, A0, A1, o, negm, l_run, qf, kp0 + sl_cur, vp0 + sl_prev, qrow - t * 64 - 4 * hi); WAIT_BAR4(); ROT();
        ++t;
    } else { B0 = A0; B1 = A1; }
    if (t < NT) STAGE_ISSUE(t);
    {
        float sacc = 0.f;
#pragma unroll
        for (int r = 0; r < 16; ++r) sacc += B0[r] + B1[r];
        l_run += sacc;
        pf0 = pack8(B0, 0); pf1 = pack8(B0, 1); pf2 = pack8(B1, 0); pf3 = pack8(B1, 1);
        const lds_cptr vp = vp0 + sl_prev;
#pragma unroll
        for (int d = 0; d < 4; ++d) {
            o[d] = MFMA(ldsb128(vp + (d * 4 + 0) * 1024), pf0, o[d]);
            o[d] = MFMA(ldsb128(vp + (d * 4 + 1) * 1024), pf1, o[d]);
            o[d] = MFMA(ldsb128(vp + (d * 4 + 2) * 1024), pf2, o[d]);
            o[d] = MFMA(ldsb128(vp + (d * 4 + 3) * 1024), pf3, o[d]);
        }
    }
    STAMP_AT(ws, 1, 3);
    {
        const float inv = 1.f / half_swap_sum(l_run);
        char* cb = ws + WS_CTX + ((size_t)(rb * 256 + head * 8) * 64 + l) * 16;
#pragma unroll
        for (int d = 0; d < 4; ++d) {
#pragma unroll
            for (int r = 0; r < 16; ++r) o[d][r] *= inv;
            *(bf16x8*)(cb + (2 * d) * 1024) = pack8(o[d], 0);
            *(bf16x8*)(cb + (2 * d + 1) * 1024) = pack8(o[d], 1);
        }
    }
    if (t < NT) {
        WAIT_ALL_BAR(); ROT(); ++t;
        for (; t < NT; ++t) { STAGE_ISSUE(t); WAIT_BAR4(); ROT(); }
    }
    asm volatile("s_waitcnt vmcnt(0)" ::: "memory");
    STAMP_AT(ws, 1, 4);
#undef DMA_K
#undef DMA_V
#undef ROT
#undef STAGE_ISSUE
}

template <int TQ, int TN, int KW, int BKS, bool APERM, bool CT>
struct G {
    static constexpr int NWAVE = TQ * TN * KW, RB = TN * 128, A_BYTES = 2 * TQ * BKS * 1024, W_BYTES = 16 * BKS * RB, STAGE = A_BYTES + W_BYTES;
    static constexpr int CPR = RB / 16;
    static_assert(STAGE == 32768 && (2 * TQ * BKS + W_BYTES / 1024) / NWAVE == 4, "4 glds per wave per stage, 4 x 32 KB ring");
    static DI int swz(int row) { return RB == 256 ? ((row & 3) << 2) : (((row >> 1) & 1) << 2); }
    static DI void stage(const char* AF, size_t a_rb_stride, const u16* Wp, int ldw, int kst, unsigned ldsaddr, int w, int l) {
#pragma unroll
        for (int p0 = 0; p0 < 2 * TQ * BKS; p0 += NWAVE) {
            const int p = p0 + w, rbi = p / BKS, ks = p % BKS;
            glds16(AF + rbi * a_rb_stride + (size_t)(kst * BKS + ks) * 1024 + l * 16, ldsaddr + p * 1024);
        }
#pragma unroll
        for (int p0 = 0; p0 < W_BYTES / 1024; p0 += NWAVE) {
            const int p = p0 + w, row = p * (1024 / RB) + l / CPR, c = (l % CPR) ^ swz(row);
            glds16(Wp + (size_t)(kst * BKS * 16 + row) * ldw + c * 8, ldsaddr + A_BYTES + p * 1024);
        }
    }
    static DI void run(const char* AF, size_t a_rb_stride, const u16* Wp, int ldw, int nstage, char* lds, float (&red)[64 / KW], int& tq, int& tn, int& kw, char* ws, int kid) {
        const int tid = threadIdx.x, l = tid & 63, hi = l >> 5;
        STAMP_AT(ws, kid, 0);
        const int w = __builtin_amdgcn_readfirstlane(tid >> 6);
        kw = w % KW; const int tt = w / KW; tq = tt / TN; tn = tt % TN;
        const unsigned lds0 = (unsigned)(uintptr_t)lds;
        const int q4 = (l & 15) >> 2, p4 = l & 3, blk = (l >> 4) & 1;
        f32x16 acc[2][2];
#pragma unroll
        for (int i = 0; i < 2; ++i) { acc[i][0] = f32x16{}; acc[i][1] = f32x16{}; }
        stage(AF, a_rb_stride, Wp, ldw, 0, lds0, w, l);
        stage(AF, a_rb_stride, Wp, ldw, 1, lds0 + STAGE, w, l);
        stage(AF, a_rb_stride, Wp, ldw, 2, lds0 + 2 * STAGE, w, l);
        for (int st = 0; st < nstage; ++st) {
            if (st + 2 < nstage) asm volatile("s_waitcnt vmcnt(8) lgkmcnt(0)\n\ts_barrier" ::: "memory");
            else if (st + 1 < nstage) asm volatile("s_waitcnt vmcnt(4) lgkmcnt(0)\n\ts_barrier" ::: "memory");
            else WAIT_ALL_BAR();
            if (st == 0) STAMP_AT(ws, kid, 1);
            if (st + 3 < nstage) stage(AF, a_rb_stride, Wp, ldw, st + 3, lds0 + ((st + 3) & 3) * STAGE, w, l);
            lds_cptr ab = (lds_cptr)lds + (st & 3) * STAGE;
            lds_cptr wb = ab + A_BYTES;
#pragma unroll
            for (int kk = 0; kk < BKS / KW; ++kk) {
                const int ks = kw * (BKS / KW) + kk;
                bf16x8 af[2], wf[2];
#pragma unroll
                for (int i = 0; i < 2; ++i) af[i] = ldsb128(ab + ((2 * tq + i) * BKS + ks) * 1024 + l * 16);
#pragma unroll
                for (int j = 0; j < 2; ++j) {
                    const int r0 = 16 * ks + (APERM ? 4 * hi : 8 * hi) + q4, r1 = r0 + (APERM ? 8 : 4);
                    const int ch = 4 * (2 * tn + j) + 2 * blk + (p4 >> 1);
                    wf[j] = wfrag(wb + r0 * RB + ((ch ^ swz(r0)) << 4) + 8 * (p4 & 1), wb + r1 * RB + ((ch ^ swz(r1)) << 4) + 8 * (p4 & 1));
                }
#pragma unroll
                for (int i = 0; i < 2; ++i)
#pragma unroll
                    for (int j = 0; j < 2; ++j) {
                        if (CT) acc[i][j] = MFMA(wf[j], af[i], acc[i][j]);
                        else acc[i][j] = MFMA(af[i], wf[j], acc[i][j]);
                    }
            }
        }
        BAR_LGKM();
        STAMP_AT(ws, kid, 2);
        float* rb_ = (float*)lds;
#pragma unroll
        for (int i = 0; i < 2; ++i)
#pragma unroll
            for (int j = 0; j < 2; ++j)
#pragma unroll
                for (int r = 0; r < 16; ++r) rb_[(w * 64 + (i * 2 + j) * 16 + r) * 64 + l] = acc[i][j][r];
        BAR_LGKM();
#pragma unroll
        for (int e = 0; e < 64 / KW; ++e) {
            float s = 0.f;
#pragma unroll
            for (int k2 = 0; k2 < KW; ++k2) s += rb_[((tt * KW + k2) * 64 + kw * (64 / KW) + e) * 64 + l];
            red[e] = s;
        }
        STAMP_AT(ws, kid, 3);
    }
};

typedef G<2, 2, 2, 4, true, false> G3;
constexpr int K3_LDS = 131072;
__global__ __launch_bounds__(512, 2) void k3_oproj(const u16* __restrict__ wo, char* __restrict__ ws) {
    extern __shared__ __attribute__((aligned(16))) char lds[];
    const int b = blockIdx.x, xcd = b & 7, r = b >> 3, ksl = xcd & 3, rp = (xcd >> 2) * 8 + (r >> 2), ct = r & 3;
    const int l = threadIdx.x & 63, hi = l >> 5;
    float red[32]; int tq, tn, kw;
    G3::run(ws + WS_CTX + ((size_t)(rp * 4) * 256 + ksl * 64) * 1024, (size_t)256 * 1024, wo + (size_t)ksl * 1024 * 512 + ct * 128, 512, 16, lds, red, tq, tn, kw, ws, 2);
    float* part = (float*)(ws + WS_PART) + (size_t)ksl * 2048 * 512;
#pragma unroll
    for (int e = 0; e < 32; ++e) {
        const int idx = kw * 32 + e, i = idx >> 5, j = (idx >> 4) & 1, rr = idx & 15;
        const int row = rp * 128 + 64 * tq + 32 * i + crow(rr, hi), col = ct * 128 + 64 * tn + 32 * j + (l & 31);
        part[(size_t)row * 512 + col] = red[e];
    }
    STAMP_AT(ws, 2, 4);
}

__global__ __launch_bounds__(512) void k3b_ln2(const u16* __restrict__ x, char* __restrict__ ws) {
    const int l = threadIdx.x & 63, row = blockIdx.x * 8 + (threadIdx.x >> 6);
    const float* part = (const float*)(ws + WS_PART) + (size_t)row * 512 + 8 * l;
    const float* mod = (const float*)(ws + WS_MOD);
    float a[8];
#pragma unroll
    for (int j = 0; j < 8; ++j) a[j] = 0.f;
#pragma unroll
    for (int s = 0; s < 4; ++s) {
        const f32x4 v0 = *(const f32x4*)(part + (size_t)s * 2048 * 512), v1 = *(const f32x4*)(part + (size_t)s * 2048 * 512 + 4);
#pragma unroll
        for (int j = 0; j < 4; ++j) { a[j] += v0[j]; a[4 + j] += v1[j]; }
    }
    const u32x4 xv4 = *(const u32x4*)(x + (size_t)row * 512 + 8 * l);
    float x1[8], xh[8];
#pragma unroll
    for (int j = 0; j < 8; ++j) {
        const float xv = (j & 1) ? bfhi(xv4[j >> 1]) : bflo(xv4[j >> 1]);
        x1[j] = bfr(xv + bfr(mod[1024 + 8 * l + j] * bfr(a[j])));
    }
    u32x4 o1 = {pk2(x1[0], x1[1]), pk2(x1[2], x1[3]), pk2(x1[4], x1[5]), pk2(x1[6], x1[7])};
    *(u32x4*)(ws + WS_X1 + ((size_t)row * 512 + 8 * l) * 2) = o1;
    ln_row(x1, xh);
    float h[8];
#pragma unroll
    for (int j = 0; j < 8; ++j) h[j] = bfr(xh[j] * bfr(1.f + mod[2048 + 8 * l + j])) + mod[1536 + 8 * l + j];
    u32x4 oh = {pk2(h[0], h[1]), pk2(h[2], h[3]), pk2(h[4], h[5]), pk2(h[6], h[7])};
    *(u32x4*)(ws + WS_H2 + ((size_t)((row >> 5) * 32 + (l >> 1)) * 64 + (row & 31) + 32 * (l & 1)) * 16) = oh;
}

typedef G<2, 2, 2, 4, false, true> G4;
__global__ __launch_bounds__(512, 2) void k4_ffn1(const u16* __restrict__ w1, char* __restrict__ ws) {
    extern __shared__ __attribute__((aligned(16))) char lds[];
    const int b = blockIdx.x, rp = b / 14, ct = b % 14;
    const int l = threadIdx.x & 63;
    float red[32]; int tq, tn, kw;
    G4::run(ws + WS_H2 + (size_t)(rp * 4) * 32 * 1024, (size_t)32 * 1024, w1 + ct * 128, DFF, 8, lds, red, tq, tn, kw, ws, 3);
#pragma unroll
    for (int j = 0; j < 2; ++j)
#pragma unroll
        for (int s = 0; s < 2; ++s) {
            const int e = j * 16 + 8 * s;
            u32x4 v = {pk2(fmaxf(red[e], 0.f), fmaxf(red[e + 1], 0.f)), pk2(fmaxf(red[e + 2], 0.f), fmaxf(red[e + 3], 0.f)),
                       pk2(fmaxf(red[e + 4], 0.f), fmaxf(red[e + 5], 0.f)), pk2(fmaxf(red[e + 6], 0.f), fmaxf(red[e + 7], 0.f))};
            const int rbq = rp * 4 + 2 * tq + kw, kstep = (ct * 128 + 64 * tn + 32 * j) / 16 + s;
            *(u32x4*)(ws + WS_H1 + ((size_t)(rbq * 112 + kstep) * 64 + l) * 16) = v;
        }
    STAMP_AT(ws, 3, 4);
}

typedef G<1, 1, 8, 8, true, false> G5;
__global__ __launch_bounds__(512, 2) void k5_ffn2(const u16* __restrict__ w2, char* __restrict__ ws, u16* __restrict__ out) {
    extern __shared__ __attribute__((aligned(16))) char lds[];
    const int b = blockIdx.x, rp = b >> 3, ct = b & 7;
    const int l = threadIdx.x & 63, hi = l >> 5;
    float red[8]; int tq, tn, kw;
    G5::run(ws + WS_H1 + (size_t)(rp * 2) * 112 * 1024, (size_t)112 * 1024, w2 + ct * 64, 512, 14, lds, red, tq, tn, kw, ws, 4);
    const float* mod = (const float*)(ws + WS_MOD);
    const u16* x1 = (const u16*)(ws + WS_X1);
#pragma unroll
    for (int e = 0; e < 8; ++e) {
        const int idx = kw * 8 + e, i = idx >> 5, j = (idx >> 4) & 1, rr = idx & 15;
        const int row = rp * 64 + 32 * i + crow(rr, hi), col = ct * 64 + 32 * j + (l & 31);
        const float v = bfr(bf2f(x1[(size_t)row * 512 + col]) + bfr(mod[2560 + col] * bfr(red[e])));
        out[(size_t)row * 512 + col] = (u16)(__float_as_uint(v) >> 16);
    }
    STAMP_AT(ws, 4, 4);
}

extern "C" void kernel_launch(void* const* d_in, const int* in_sizes, int n_in, void* d_out, int out_size, void* d_ws, size_t ws_size,
                              hipStream_t stream) {
    (void)in_sizes; (void)n_in; (void)out_size; (void)ws_size;
    const u16* x = (const u16*)d_in[0];
    const u16* ckv = (const u16*)d_in[1];
    const u16* wq = (const u16*)d_in[2];
    const u16* wuk = (const u16*)d_in[3];
    const u16* wuv = (const u16*)d_in[4];
    const u16* wo = (const u16*)d_in[5];
    const u16* w1 = (const u16*)d_in[7];
    const u16* w2 = (const u16*)d_in[8];
    const u16* cvec = (const u16*)d_in[9];
    const u16* wmod = (const u16*)d_in[10];
    char* ws = (char*)d_ws;
    hipFuncSetAttribute((const void*)k1_kvproj, hipFuncAttributeMaxDynamicSharedMemorySize, K1_LDS);
    hipFuncSetAttribute((const void*)k2_attn, hipFuncAttributeMaxDynamicSharedMemorySize, K2_LDS);
    hipFuncSetAttribute((const void*)k3_oproj, hipFuncAttributeMaxDynamicSharedMemorySize, K3_LDS);
    hipFuncSetAttribute((const void*)k4_ffn1, hipFuncAttributeMaxDynamicSharedMemorySize, K3_LDS);
    hipFuncSetAttribute((const void*)k5_ffn2, hipFuncAttributeMaxDynamicSharedMemorySize, K3_LDS);
    k1_kvproj<<<256, 512, K1_LDS, stream>>>(ckv, wuk, wuv, x, cvec, wmod, ws);
    k2_attn<<<256, 512, K2_LDS, stream>>>(wq, ws);
    k3_oproj<<<256, 512, K3_LDS, stream>>>(wo, ws);
    k3b_ln2<<<256, 512, 0, stream>>>(x, ws);
    k4_ffn1<<<224, 512, K3_LDS, stream>>>(w1, ws);
    k5_ffn2<<<256, 512, K3_LDS, stream>>>(w2, ws, (u16*)d_out);
}
```

```cpp
#include <hip/hip_runtime.h>
#include <hip/hip_bf16.h>
#include <stdint.h>

typedef unsigned short u16;
typedef short bf16x8 __attribute__((ext_vector_type(8)));
typedef short s16x4 __attribute__((ext_vector_type(4)));
typedef short v4i16_t __attribute__((ext_vector_type(4)));
typedef float f32x16 __attribute__((ext_vector_type(16)));
typedef float f32x4 __attribute__((ext_vector_type(4)));
typedef float f32x2 __attribute__((ext_vector_type(2)));
typedef unsigned u32x4 __attribute__((ext_vector_type(4)));
typedef __bf16 bf16x2_t __attribute__((ext_vector_type(2)));
typedef __attribute__((address_space(3))) const char* lds_cptr;
#define DI __device__ __forceinline__
#define MFMA(a, b, c) __builtin_amdgcn_mfma_f32_32x32x16_bf16((a), (b), (c), 0, 0, 0)
#define SBAR() __builtin_amdgcn_sched_barrier(0)
#define STAMP_AT(ws_, kid, idx) do { } while (0)

constexpr int SEQ = 2048, DM = 512, NH = 32, HD = 128, DFF = 1792;

constexpr size_t WS_KV = 0;
constexpr size_t WS_XH = WS_KV + (size_t)32 * 32 * 32768;
constexpr size_t WS_MOD = WS_XH + (size_t)64 * 32 * 1024;
constexpr size_t WS_GS = WS_MOD + 12288;
constexpr size_t WS_CTX = WS_MOD + 16384;
constexpr size_t WS_PART = WS_CTX + (size_t)64 * 256 * 1024;
constexpr size_t WS_X1 = WS_PART + (size_t)4 * 2048 * 512 * 4;
constexpr size_t WS_H2 = WS_X1 + (size_t)2048 * 512 * 2;
constexpr size_t WS_H1 = WS_H2 + (size_t)64 * 32 * 1024;

DI unsigned pk2(float a, float b) { f32x2 v = {a, b}; bf16x2_t r = __builtin_convertvector(v, bf16x2_t); return __builtin_bit_cast(unsigned, r); }
DI float bf2f(u16 v) { return __uint_as_float(((unsigned)v) << 16); }
DI float bfr(float x) { return __uint_as_float(pk2(x, 0.f) << 16); }
DI float bflo(unsigned w) { return __uint_as_float(w << 16); }
DI float bfhi(unsigned w) { return __uint_as_float(w & 0xffff0000u); }
DI bf16x8 pack8(const f32x16& a, int s) {
    u32x4 w = {pk2(a[8 * s], a[8 * s + 1]), pk2(a[8 * s + 2], a[8 * s + 3]), pk2(a[8 * s + 4], a[8 * s + 5]), pk2(a[8 * s + 6], a[8 * s + 7])};
    return __builtin_bit_cast(bf16x8, w);
}
DI int crow(int r, int hi) { return (r & 3) + 8 * (r >> 2) + 4 * hi; }
DI void glds16(const void* g, unsigned lds_base) {
    unsigned sv;
    asm volatile("s_mov_b32 %0, m0\n\ts_mov_b32 m0, %2\n\ts_nop 0\n\tglobal_load_lds_dwordx4 %1, off\n\ts_mov_b32 m0, %0"
                 : "=&s"(sv) : "v"(g), "s"(lds_base) : "memory");
}
DI bf16x8 ldsb128(lds_cptr p) { return *(const __attribute__((address_space(3))) bf16x8*)p; }
DI s16x4 ldstr(lds_cptr p) { return __builtin_bit_cast(s16x4, __builtin_amdgcn_ds_read_tr16_b64_v4i16((__attribute__((address_space(3))) v4i16_t*)p)); }
DI bf16x8 wfrag(lds_cptr p0, lds_cptr p1) { s16x4 lo = ldstr(p0), hi = ldstr(p1); return __builtin_shufflevector(lo, hi, 0, 1, 2, 3, 4, 5, 6, 7); }
#define WAIT_ALL_BAR() asm volatile("s_waitcnt vmcnt(0) lgkmcnt(0)\n\ts_barrier" ::: "memory")
#define BAR_LGKM() asm volatile("s_waitcnt lgkmcnt(0)\n\ts_barrier" ::: "memory")
DI float wave_sum(float v) {
#pragma unroll
    for (int m = 1; m < 64; m <<= 1) v += __shfl_xor(v, m);
    return v;
}
DI float half_swap_max(float m) { auto rr = __builtin_amdgcn_permlane32_swap(__float_as_uint(m), __float_as_uint(m), false, false); return fmaxf(__uint_as_float(rr[0]), __uint_as_float(rr[1])); }
DI float half_swap_sum(float m) { auto rr = __builtin_amdgcn_permlane32_swap(__float_as_uint(m), __float_as_uint(m), false, false); return __uint_as_float(rr[0]) + __uint_as_float(rr[1]); }

DI void ln_row(const float (&xv)[8], float (&o)[8]) {
    float s = 0.f;
#pragma unroll
    for (int j = 0; j < 8; ++j) s += xv[j];
    const float mean = wave_sum(s) * (1.f / 512.f);
    float q = 0.f;
#pragma unroll
    for (int j = 0; j < 8; ++j) { const float d = xv[j] - mean; q += d * d; }
    const float var = wave_sum(q) * (1.f / 512.f);
    const float mb = bfr(mean), vb = bfr(var);
    const float den = bfr(sqrtf(bfr(vb + 1e-5f)));
#pragma unroll
    for (int j = 0; j < 8; ++j) o[j] = bfr(bfr(xv[j] - mb) / den);
}

constexpr int K1_LDS = 131072;
template <bool VT>
DI void k1_gemm(const u16* __restrict__ ckv, const u16* __restrict__ W, char* __restrict__ kvimg, char* lds, int key0, int h0, int h1) {
    const int tid = threadIdx.x, l = tid & 63, r32 = l & 31, hi = l >> 5;
    const int w = __builtin_amdgcn_readfirstlane(tid >> 6);
    const int wk = w >> 2, wc = w & 3;
    const unsigned lds0 = (unsigned)(uintptr_t)lds;
    f32x16 acc[4][2];
#pragma unroll
    for (int a = 0; a < 4; ++a) { acc[a][0] = f32x16{}; acc[a][1] = f32x16{}; }
    const int q4 = (l & 15) >> 2, p4 = l & 3, blk = (l >> 4) & 1;
    const int xa = (l >> 1) & 7;
    STAMP_AT(kvimg, 0, 1);
    for (int kt = 0; kt < 8; ++kt) {
        WAIT_ALL_BAR();
        if (kt == 0) STAMP_AT(kvimg, 0, 2);
        if (kt + 1 < 8) {
            const unsigned bb = lds0 + ((kt + 1) & 1) * 65536;
#pragma unroll
            for (int n = 0; n < 4; ++n) {
                const int P = n * 8 + w, row = 8 * P + (l >> 3), c = (l & 7) ^ ((row >> 1) & 7);
                glds16(ckv + (size_t)(key0 + row) * 512 + (kt + 1) * 64 + c * 8, bb + P * 1024);
            }
#pragma unroll
            for (int n = 0; n < 4; ++n) {
                const int P = n * 8 + w, half = P >> 4, row = 4 * (P & 15) + (l >> 4), c = (l & 15) ^ ((row & 3) << 2);
                glds16(W + (size_t)((kt + 1) * 64 + row) * 4096 + (half ? h1 : h0) * 128 + c * 8, bb + 32768 + P * 1024);
            }
        }
        lds_cptr ab = (lds_cptr)lds + (kt & 1) * 65536;
        lds_cptr wb = ab + 32768 + (wc >> 1) * 16384;
#pragma unroll
        for (int s = 0; s < 4; ++s) {
            bf16x8 af[4], wf[2];
#pragma unroll
            for (int kb = 0; kb < 4; ++kb) af[kb] = ldsb128(ab + (128 * wk + 32 * kb + r32) * 128 + (((2 * s + hi) ^ xa) << 4));
#pragma unroll
            for (int j = 0; j < 2; ++j) {
                const int db = 2 * (wc & 1) + j;
                lds_cptr p = wb + (16 * s + 8 * hi + q4) * 256 + 64 * (db ^ q4) + 32 * blk + 16 * (p4 >> 1) + 8 * (p4 & 1);
                wf[j] = wfrag(p, p + 1024);
            }
#pragma unroll
            for (int kb = 0; kb < 4; ++kb)
#pragma unroll
                for (int j = 0; j < 2; ++j) {
                    if (VT) acc[kb][j] = MFMA(af[kb], wf[j], acc[kb][j]);
                    else acc[kb][j] = MFMA(wf[j], af[kb], acc[kb][j]);
                }
        }
    }
    STAMP_AT(kvimg, 0, 3);
    const int head = (wc >> 1) ? h1 : h0;
#pragma unroll
    for (int kb = 0; kb < 4; ++kb) {
        const int key = key0 + 128 * wk + 32 * kb, tile = key >> 6, kbl = kb & 1;
        char* tb = kvimg + ((size_t)(head * 32 + tile) << 15);
#pragma unroll
        for (int j = 0; j < 2; ++j) {
            const int db = 2 * (wc & 1) + j;
#pragma unroll
            for (int s2 = 0; s2 < 2; ++s2) {
                const int fo = VT ? (16384 + (db * 4 + kbl * 2 + s2) * 1024) : ((kbl * 8 + db * 2 + s2) * 1024);
                *(bf16x8*)(tb + fo + l * 16) = pack8(acc[kb][j], s2);
            }
        }
    }
}

__global__ __launch_bounds__(512, 2) void k1_kvproj(const u16* __restrict__ ckv, const u16* __restrict__ wuk, const u16* __restrict__ wuv,
                                                    const u16* __restrict__ x, const u16* __restrict__ cvec, const u16* __restrict__ wmod,
                                                    char* __restrict__ ws) {
    extern __shared__ __attribute__((aligned(16))) char lds[];
    const int b = blockIdx.x, tid = threadIdx.x, l = tid & 63;
    const int w = __builtin_amdgcn_readfirstlane(tid >> 6);
    const int xcd = b & 7, r = b >> 3, kb8 = r & 7, combo = r >> 3, type = combo & 1, hp = combo >> 1;
    const int h0 = xcd + 16 * hp, h1 = h0 + 8, key0 = kb8 * 256;
    const u16* W = type ? wuv : wuk;
    const unsigned lds0 = (unsigned)(uintptr_t)lds;
    STAMP_AT(ws, 0, 0);
#pragma unroll
    for (int n = 0; n < 4; ++n) {
        const int P = n * 8 + w, row = 8 * P + (l >> 3), c = (l & 7) ^ ((row >> 1) & 7);
        glds16(ckv + (size_t)(key0 + row) * 512 + c * 8, lds0 + P * 1024);
    }
#pragma unroll
    for (int n = 0; n < 4; ++n) {
        const int P = n * 8 + w, half = P >> 4, row = 4 * (P & 15) + (l >> 4), c = (l & 15) ^ ((row & 3) << 2);
        glds16(W + (size_t)row * 4096 + (half ? h1 : h0) * 128 + c * 8, lds0 + 32768 + P * 1024);
    }
    if (b < 192) {
        float* scr = (float*)(lds + 65536);
        const int half = tid & 1, rowi = tid >> 1;
        float a8[8];
#pragma unroll
        for (int j = 0; j < 8; ++j) a8[j] = 0.f;
#pragma unroll
        for (int p = 0; p < 2; ++p) {
            const int row = rowi + 256 * p;
            const u32x4 wv = *(const u32x4*)(wmod + (size_t)row * 3072 + 16 * b + 8 * half);
            const float cv = bf2f(cvec[row]);
            const float sc = bfr(cv / (1.f + __expf(-cv)));
#pragma unroll
            for (int j = 0; j < 4; ++j) { a8[2 * j] += sc * bflo(wv[j]); a8[2 * j + 1] += sc * bfhi(wv[j]); }
        }
#pragma unroll
        for (int j = 0; j < 8; ++j)
#pragma unroll
            for (int m = 2; m < 64; m <<= 1) a8[j] += __shfl_xor(a8[j], m);
        if (l < 2) {
#pragma unroll
            for (int j = 0; j < 8; ++j) scr[(w * 2 + l) * 8 + j] = a8[j];
        }
        BAR_LGKM();
        if (tid < 16) {
            float s = 0.f;
#pragma unroll
            for (int ww = 0; ww < 8; ++ww) s += scr[(ww * 2 + (tid >> 3)) * 8 + (tid & 7)];
            const int col = 16 * b + tid; const float mv = bfr(s);
            ((float*)(ws + WS_MOD))[col] = mv;
            if (col < 512) ((float*)(ws + WS_GS))[512 + col] = mv;
            else if (col < 1024) ((float*)(ws + WS_GS))[col - 512] = bfr(1.f + mv);
        }
    }
    {
        const int row = 8 * b + w;
        const u32x4 xv4 = *(const u32x4*)(x + (size_t)row * 512 + 8 * l);
        float xv[8], o[8];
#pragma unroll
        for (int j = 0; j < 4; ++j) { xv[2 * j] = bflo(xv4[j]); xv[2 * j + 1] = bfhi(xv4[j]); }
        ln_row(xv, o);
        u32x4 ov = {pk2(o[0], o[1]), pk2(o[2], o[3]), pk2(o[4], o[5]), pk2(o[6], o[7])};
        *(u32x4*)(ws + WS_XH + ((size_t)((row >> 5) * 32 + (l >> 1)) * 64 + (row & 31) + 32 * (l & 1)) * 16) = ov;
    }
    if (type) k1_gemm<true>(ckv, W, ws + WS_KV, lds, key0, h0, h1);
    else k1_gemm<false>(ckv, W, ws + WS_KV, lds, key0, h0, h1);
    STAMP_AT(ws, 0, 4);
}

constexpr float ATT_THR = 8.f;
#define PIN(x) asm volatile("" : "+v"(x))
#define MX3(a, b, c) __builtin_fmaxf(__builtin_fmaxf((a), (b)), (c))
DI float att_rowmax(const f32x16& p0, const f32x16& p1) {
    float a = MX3(p0[0], p0[1], p1[0]), b = MX3(p0[2], p0[3], p1[1]); a = MX3(a, p1[2], p1[3]);
#pragma unroll
    for (int r = 4; r < 16; r += 4) { a = MX3(a, p0[r], p0[r + 1]); b = MX3(b, p0[r + 2], p0[r + 3]); a = MX3(a, p1[r], p1[r + 1]); b = MX3(b, p1[r + 2], p1[r + 3]); }
    return __builtin_fmaxf(a, b);
}
DI void att_mask(f32x16& s0, f32x16& s1, int dq) {
#pragma unroll
    for (int r = 0; r < 16; ++r) {
        const int c = (r & 3) + 8 * (r >> 2);
        if (c > dq) s0[r] = -INFINITY;
        if (c + 32 > dq) s1[r] = -INFINITY;
    }
}
constexpr int PD = 6;
DI int kfrag_off(int g) { return ((g & 1) * 8 + (g >> 1)) * 1024; }
DI int vfrag_off(int g) { return ((g & 3) * 4 + (g >> 2)) * 1024; }
template <bool MASK>
DI void att_step(f32x16& C0, f32x16& C1, const f32x16& P0, f32x16& P1, f32x16 (&o)[4], f32x16& negm, float& l_run,
                 const bf16x8 (&qf)[8], bf16x8 (&fr)[8], lds_cptr kp, lds_cptr vp, lds_cptr kpn, int dq) {
    unsigned pw[16];
    float sacc = 0.f, sacc2 = 0.f;
    SBAR();
#pragma unroll
    for (int g = 0; g < 16; ++g) {
        if ((g & 1) == 0) C0 = MFMA(fr[g & 7], qf[g >> 1], g < 2 ? negm : C0);
        else C1 = MFMA(fr[g & 7], qf[g >> 1], g < 2 ? negm : C1);
        if (g + PD < 16) fr[(g + PD) & 7] = ldsb128(kp + kfrag_off(g + PD));
        else fr[(g + PD) & 7] = ldsb128(vp + vfrag_off(g + PD - 16));
        P1[g] = __builtin_amdgcn_exp2f(P1[g]); PIN(P1[g]);
        sacc += P0[g]; PIN(sacc);
        if (g & 1) { pw[g >> 1] = pk2(P0[g - 1], P0[g]); PIN(pw[g >> 1]); }
        if (g >= 1) { sacc2 += P1[g - 1]; PIN(sacc2); }
        if (g >= 2 && (g & 1) == 0) { pw[8 + ((g - 2) >> 1)] = pk2(P1[g - 2], P1[g - 1]); PIN(pw[8 + ((g - 2) >> 1)]); }
        SBAR();
    }
    sacc2 += P1[15]; pw[15] = pk2(P1[14], P1[15]);
    l_run += sacc + sacc2;
    bool resc = false; float f = 1.f;
    float ma, mb;
#pragma unroll
    for (int g = 0; g < 16; ++g) {
        { const u32x4 pv = {pw[4 * (g >> 2)], pw[4 * (g >> 2) + 1], pw[4 * (g >> 2) + 2], pw[4 * (g >> 2) + 3]};
          o[g & 3] = MFMA(fr[g & 7], __builtin_bit_cast(bf16x8, pv), o[g & 3]); }
        if (g + PD < 16) fr[(g + PD) & 7] = ldsb128(vp + vfrag_off(g + PD));
        else fr[(g + PD) & 7] = ldsb128(kpn + kfrag_off(g + PD - 16));
        if (g == 1 && MASK) att_mask(C0, C1, dq);
        if (g == 1) { ma = MX3(C0[0], C0[1], C0[2]); mb = MX3(C0[3], C0[4], C0[5]); ma = MX3(ma, C0[6], C0[7]); mb = MX3(mb, C0[8], C0[9]); PIN(ma); PIN(mb); }
        if (g == 2) { ma = MX3(ma, C0[10], C0[11]); mb = MX3(mb, C0[12], C0[13]); ma = MX3(ma, C0[14], C0[15]); mb = MX3(mb, C1[0], C1[1]); PIN(ma); PIN(mb); }
        if (g == 3) { ma = MX3(ma, C1[2], C1[3]); mb = MX3(mb, C1[4], C1[5]); ma = MX3(ma, C1[6], C1[7]); mb = MX3(mb, C1[8], C1[9]); PIN(ma); PIN(mb); }
        if (g == 4) {
            ma = MX3(ma, C1[10], C1[11]); mb = MX3(mb, C1[12], C1[13]); ma = MX3(ma, C1[14], C1[15]);
            const float rm = __builtin_fmaxf(ma, mb);
            if (__builtin_expect(__any(rm > ATT_THR), 0)) {
                const float dl = __builtin_fmaxf(half_swap_max(rm), 0.f);
                f = __builtin_amdgcn_exp2f(-dl); l_run *= f; resc = true;
#pragma unroll
                for (int r = 0; r < 16; ++r) { C0[r] -= dl; C1[r] -= dl; negm[r] -= dl; }
            }
        }
        if (g >= 5) {
            const int e0 = g < 10 ? 2 * (g - 5) : 10 + (g - 10), e1 = g < 10 ? e0 + 2 : e0 + 1;
#pragma unroll
            for (int e = e0; e < e1; ++e) { C0[e] = __builtin_amdgcn_exp2f(C0[e]); PIN(C0[e]); }
        }
        SBAR();
    }
    if (__builtin_expect(resc, 0)) {
#pragma unroll
        for (int d = 0; d < 4; ++d)
#pragma unroll
            for (int r = 0; r < 16; ++r) o[d][r] *= f;
    }
}
constexpr int K2_LDS = 147456 + 4096;
constexpr float QSCALE = 0.08838834764831845f * 1.4426950408889634f;
__global__ __launch_bounds__(512, 2) void k2_attn(const u16* __restrict__ wq, char* __restrict__ ws) {
    extern __shared__ __attribute__((aligned(16))) char lds[];
    const int b = blockIdx.x, tid = threadIdx.x, l = tid & 63, r32 = l & 31, hi = l >> 5;
    const int w = __builtin_amdgcn_readfirstlane(tid >> 6);
    const int xcd = b & 7, kk = b >> 3, head = xcd + 8 * (kk >> 3), pi = kk & 7;
    const int qb = (w < 4) ? pi : 15 - pi, sub = w & 3, rb = qb * 4 + sub;
    const int tl = 2 * qb + (sub >> 1), NT = 32 - 2 * pi;
    const unsigned lds0 = (unsigned)(uintptr_t)lds;
    STAMP_AT(ws, 1, 0);
    const char* kvh = ws + WS_KV + ((size_t)head << 20);
    const char* ksrc = kvh + w * 2048 + l * 16;
    const unsigned kdst = lds0 + w * 2048, vdst = lds0 + 65536 + w * 2048;
#define DMA_K(t_, slot_) do { const char* g_ = ksrc + (size_t)(t_) * 32768; glds16(g_, kdst + (slot_)); glds16(g_ + 1024, kdst + (slot_) + 1024); } while (0)
#define DMA_V(t_, slot_) do { const char* g_ = ksrc + (size_t)(t_) * 32768 + 16384; glds16(g_, vdst + (slot_)); glds16(g_ + 1024, vdst + (slot_) + 1024); } while (0)
    DMA_K(0, 0);
    glds16(ws + WS_GS + (w & 3) * 1024 + l * 16, lds0 + 147456 + (w & 3) * 1024);
    u32x4 xf[32];
    const char* xsrc = ws + WS_XH + ((size_t)(rb * 32) * 64 + l) * 16;
#pragma unroll
    for (int c = 0; c < 4; ++c) {
#pragma unroll
        for (int k8 = 0; k8 < 8; ++k8) asm volatile("global_load_dwordx4 %0, %1, off" : "=v"(xf[8 * c + k8]) : "v"(xsrc + (8 * c + k8) * 1024) : "memory");
#pragma unroll
        for (int n = 0; n < 4; ++n) {
            const int P = (4 * c + n) * 8 + w, row = 4 * P + (l >> 4), cc = (l & 15) ^ ((row & 3) << 2);
            glds16(wq + (size_t)row * 4096 + head * 128 + cc * 8, lds0 + 16384 + P * 1024);
        }
    }
    const float* gs = (const float*)(lds + 147456);
    const int q4 = (l & 15) >> 2, p4 = l & 3, blk = (l >> 4) & 1;
    bf16x8 qf[8];
    {
        f32x16 qa[4];
#pragma unroll
        for (int d = 0; d < 4; ++d) qa[d] = f32x16{};
#define XWAIT(N, c) asm volatile("s_waitcnt vmcnt(" #N ") lgkmcnt(0)\n\ts_barrier" : "+v"(xf[8 * c]), "+v"(xf[8 * c + 1]), "+v"(xf[8 * c + 2]), "+v"(xf[8 * c + 3]), \
                                 "+v"(xf[8 * c + 4]), "+v"(xf[8 * c + 5]), "+v"(xf[8 * c + 6]), "+v"(xf[8 * c + 7]) :: "memory")
#pragma unroll
        for (int c = 0; c < 4; ++c) {
            if (c == 0) { XWAIT(36, 0); STAMP_AT(ws, 1, 1); }
            if (c == 1) { XWAIT(24, 1); DMA_K(1, 16384); DMA_K(2, 32768); }
            if (c == 2) { XWAIT(16, 2); DMA_V(0, 0); }
            if (c == 3) { XWAIT(6, 3); DMA_V(1, 16384); }
#pragma unroll
            for (int k8 = 0; k8 < 8; ++k8) {
                const int ks = 8 * c + k8;
                const f32x4 g0 = *(const f32x4*)(gs + 16 * ks + 8 * hi), g1 = *(const f32x4*)(gs + 16 * ks + 8 * hi + 4);
                const f32x4 s0 = *(const f32x4*)(gs + 512 + 16 * ks + 8 * hi), s1 = *(const f32x4*)(gs + 512 + 16 * ks + 8 * hi + 4);
                const u32x4 xw = xf[ks];
                u32x4 hw = {pk2(fmaf(bflo(xw[0]), g0[0], s0[0]), fmaf(bfhi(xw[0]), g0[1], s0[1])),
                            pk2(fmaf(bflo(xw[1]), g0[2], s0[2]), fmaf(bfhi(xw[1]), g0[3], s0[3])),
                            pk2(fmaf(bflo(xw[2]), g1[0], s1[0]), fmaf(bfhi(xw[2]), g1[1], s1[1])),
                            pk2(fmaf(bflo(xw[3]), g1[2], s1[2]), fmaf(bfhi(xw[3]), g1[3], s1[3]))};
                const bf16x8 hf = __builtin_bit_cast(bf16x8, hw);
#pragma unroll
                for (int db = 0; db < 4; ++db) {
                    lds_cptr p = (lds_cptr)lds + 16384 + (16 * ks + 8 * hi + q4) * 256 + 64 * (db ^ q4) + 32 * blk + 16 * (p4 >> 1) + 8 * (p4 & 1);
                    qa[db] = MFMA(wfrag(p, p + 1024), hf, qa[db]);
                }
            }
        }
#undef XWAIT
#pragma unroll
        for (int db = 0; db < 4; ++db) {
#pragma unroll
            for (int r = 0; r < 16; ++r) qa[db][r] *= QSCALE;
            qf[2 * db] = pack8(qa[db], 0); qf[2 * db + 1] = pack8(qa[db], 1);
        }
    }
    STAMP_AT(ws, 1, 2);
#define WAIT_BAR4() asm volatile("s_waitcnt vmcnt(4) lgkmcnt(0)\n\ts_barrier" ::: "memory")
#define WAITB(t_) do { if ((t_) < tsw) WAIT_BAR4(); else WAIT_ALL_BAR(); } while (0)
    float l_run = 0.f;
    f32x16 o[4], negm;
#pragma unroll
    for (int d = 0; d < 4; ++d) o[d] = f32x16{};
    const int qrow = rb * 32 + r32;
    const lds_cptr kp0 = (lds_cptr)lds + l * 16, vp0 = (lds_cptr)lds + 65536 + l * 16;
    f32x16 A0, A1, B0, B1;
    bf16x8 pf0, pf1, pf2, pf3, fr[8];
    int sl_prev = 49152, sl_cur = 0;
#define ROT() do { sl_prev = sl_cur; sl_cur = (sl_cur + 16384) & 65535; } while (0)
    const int tsw = 2 * pi + 3;
    const char* hsrc = kvh + (w & 3) * 4096 + l * 16;
    const unsigned hk = lds0 + (w & 3) * 4096, hv = lds0 + 65536 + (w & 3) * 4096;
#define STAGE_ISSUE(t_) do { if ((t_) < tsw) { const int tk_ = (t_) + 3 < NT ? (t_) + 3 : NT - 1, tv_ = (t_) + 2 < NT ? (t_) + 2 : NT - 1; DMA_K(tk_, sl_prev); DMA_V(tv_, (sl_cur + 32768) & 65535); } } while (0)
#define STAGE_HELP(t_) do { const int tk_ = (t_) + 3 < NT ? (t_) + 3 : NT - 1, tv_ = (t_) + 2 < NT ? (t_) + 2 : NT - 1; \
        const char* gk_ = hsrc + (size_t)tk_ * 32768; const char* gv_ = hsrc + (size_t)tv_ * 32768 + 16384; const unsigned sk_ = hk + sl_prev, sv_ = hv + ((sl_cur + 32768) & 65535); \
        glds16(gk_, sk_); glds16(gk_ + 1024, sk_ + 1024); glds16(gk_ + 2048, sk_ + 2048); glds16(gk_ + 3072, sk_ + 3072); \
        glds16(gv_, sv_); glds16(gv_ + 1024, sv_ + 1024); glds16(gv_ + 2048, sv_ + 2048); glds16(gv_ + 3072, sv_ + 3072); } while (0)
    asm volatile("s_waitcnt vmcnt(2) lgkmcnt(0)\n\ts_barrier" ::: "memory");
    STAGE_ISSUE(0);
    {
        A0 = f32x16{}; A1 = f32x16{};
#pragma unroll
        for (int ds = 0; ds < 8; ++ds) {
            A0 = MFMA(ldsb128(kp0 + ds * 1024), qf[ds], A0);
            A1 = MFMA(ldsb128(kp0 + 8192 + ds * 1024), qf[ds], A1);
        }
        if (tl == 0) att_mask(A0, A1, qrow - 4 * hi);
        const float mx = half_swap_max(att_rowmax(A0, A1));
#pragma unroll
        for (int r = 0; r < 16; ++r) { A0[r] = __builtin_amdgcn_exp2f(A0[r] - mx); A1[r] = A1[r] - mx; negm[r] = -mx; }
#pragma unroll
        for (int g = 0; g < PD; ++g) fr[g] = ldsb128(kp0 + 16384 + kfrag_off(g));
    }
    WAIT_BAR4(); ROT();
    int t = 1;
    for (; t + 1 <= tl - 1; t += 2) {
        STAGE_ISSUE(t);     att_step<false>(B0, B1, A0, A1, o, negm, l_run, qf, fr, kp0 + sl_cur, vp0 + sl_prev, kp0 + ((sl_cur + 16384) & 65535), 0); WAITB(t); ROT();
        STAGE_ISSUE(t + 1); att_step<false>(A0, A1, B0, B1, o, negm, l_run, qf, fr, kp0 + sl_cur, vp0 + sl_prev, kp0 + ((sl_cur + 16384) & 65535), 0); WAITB(t + 1); ROT();
    }
    if (t <= tl - 1) {
        STAGE_ISSUE(t); att_step<false>(B0, B1, A0, A1, o, negm, l_run, qf, fr, kp0 + sl_cur, vp0 + sl_prev, kp0 + ((sl_cur + 16384) & 65535), 0); WAITB(t); ROT();
        A0 = B0; A1 = B1; ++t;
    }
    if (tl >= 1) {
        STAGE_ISSUE(t); att_step<true>(B0, B1, A0, A1, o, negm, l_run, qf, fr, kp0 + sl_cur, vp0 + sl_prev, kp0 + ((sl_cur + 16384) & 65535), qrow - t * 64 - 4 * hi); WAITB(t); ROT();
        ++t;
    } else { B0 = A0; B1 = A1; }
    if (t < NT) STAGE_ISSUE(t);
    {
        float sacc = 0.f;
#pragma unroll
        for (int r = 0; r < 16; ++r) { B1[r] = __builtin_amdgcn_exp2f(B1[r]); sacc += B0[r] + B1[r]; }
        l_run += sacc;
        pf0 = pack8(B0, 0); pf1 = pack8(B0, 1); pf2 = pack8(B1, 0); pf3 = pack8(B1, 1);
        const lds_cptr vp = vp0 + sl_prev;
#pragma unroll
        for (int d = 0; d < 4; ++d) {
            o[d] = MFMA(ldsb128(vp + (d * 4 + 0) * 1024), pf0, o[d]);
            o[d] = MFMA(ldsb128(vp + (d * 4 + 1) * 1024), pf1, o[d]);
            o[d] = MFMA(ldsb128(vp + (d * 4 + 2) * 1024), pf2, o[d]);
            o[d] = MFMA(ldsb128(vp + (d * 4 + 3) * 1024), pf3, o[d]);
        }
    }
    STAMP_AT(ws, 1, 3);
    {
        const float inv = 1.f / half_swap_sum(l_run);
        char* cb = ws + WS_CTX + ((size_t)(rb * 256 + head * 8) * 64 + l) * 16;
#pragma unroll
        for (int d = 0; d < 4; ++d) {
#pragma unroll
            for (int r = 0; r < 16; ++r) o[d][r] *= inv;
            *(bf16x8*)(cb + (2 * d) * 1024) = pack8(o[d], 0);
            *(bf16x8*)(cb + (2 * d + 1) * 1024) = pack8(o[d], 1);
        }
    }
    if (t < NT) {
        WAIT_ALL_BAR(); ROT(); ++t;
        for (; t < NT && t < tsw; ++t) { STAGE_ISSUE(t); WAIT_BAR4(); ROT(); }
        if (w < 4) { for (; t < NT; ++t) { STAGE_HELP(t); asm volatile("s_waitcnt vmcnt(8) lgkmcnt(0)\n\ts_barrier" ::: "memory"); ROT(); } }
        else { for (; t < NT; ++t) { WAIT_ALL_BAR(); ROT(); } }
    }
    asm volatile("s_waitcnt vmcnt(0)" ::: "memory");
    STAMP_AT(ws, 1, 4);
#undef DMA_K
#undef DMA_V
#undef ROT
#undef STAGE_ISSUE
#undef STAGE_HELP
#undef WAITB
}

template <int TQ, int TN, int KW, int BKS, bool APERM, bool CT>
struct G {
    static constexpr int NWAVE = TQ * TN * KW, RB = TN * 128, A_BYTES = 2 * TQ * BKS * 1024, W_BYTES = 16 * BKS * RB, STAGE = A_BYTES + W_BYTES;
    static constexpr int CPR = RB / 16;
    static_assert(STAGE == 32768 && (2 * TQ * BKS + W_BYTES / 1024) / NWAVE == 4, "4 glds per wave per stage, 4 x 32 KB ring");
    static DI int swz(int row) { return RB == 256 ? ((row & 3) << 2) : (((row >> 1) & 1) << 2); }
    static DI void stage(const char* AF, size_t a_rb_stride, const u16* Wp, int ldw, int kst, unsigned ldsaddr, int w, int l) {
#pragma unroll
        for (int p0 = 0; p0 < 2 * TQ * BKS; p0 += NWAVE) {
            const int p = p0 + w, rbi = p / BKS, ks = p % BKS;
            glds16(AF + rbi * a_rb_stride + (size_t)(kst * BKS + ks) * 1024 + l * 16, ldsaddr + p * 1024);
        }
#pragma unroll
        for (int p0 = 0; p0 < W_BYTES / 1024; p0 += NWAVE) {
            const int p = p0 + w, row = p * (1024 / RB) + l / CPR, c = (l % CPR) ^ swz(row);
            glds16(Wp + (size_t)(kst * BKS * 16 + row) * ldw + c * 8, ldsaddr + A_BYTES + p * 1024);
        }
    }
    static constexpr int NPA = 2 * TQ * BKS / NWAVE, NPW = W_BYTES / 1024 / NWAVE;
    static DI void stage_piece(int i, const char* AF, size_t a_rb_stride, const u16* Wp, int ldw, int kst, unsigned ldsaddr, int w, int l) {
        if (i < NPA) {
            const int p = i * NWAVE + w, rbi = p / BKS, ks = p % BKS;
            glds16(AF + rbi * a_rb_stride + (size_t)(kst * BKS + ks) * 1024 + l * 16, ldsaddr + p * 1024);
        } else {
            const int p = (i - NPA) * NWAVE + w, row = p * (1024 / RB) + l / CPR, c = (l % CPR) ^ swz(row);
            glds16(Wp + (size_t)(kst * BKS * 16 + row) * ldw + c * 8, ldsaddr + A_BYTES + p * 1024);
        }
    }
    static DI void run(const char* AF, size_t a_rb_stride, const u16* Wp, int ldw, int nstage, int rot, char* lds, float (&red)[64 / KW], int& tq, int& tn, int& kw, char* ws, int kid) {
        const int tid = threadIdx.x, l = tid & 63, hi = l >> 5;
        STAMP_AT(ws, kid, 0);
        const int w = __builtin_amdgcn_readfirstlane(tid >> 6);
        kw = w % KW; const int tt = w / KW; tq = tt / TN; tn = tt % TN;
        const unsigned lds0 = (unsigned)(uintptr_t)lds;
        const int q4 = (l & 15) >> 2, p4 = l & 3, blk = (l >> 4) & 1;
        f32x16 acc[2][2];
#pragma unroll
        for (int i = 0; i < 2; ++i) { acc[i][0] = f32x16{}; acc[i][1] = f32x16{}; }
        int kst = rot;
#define NEXTK() do { kst = (kst + 1 == nstage) ? 0 : kst + 1; } while (0)
        stage(AF, a_rb_stride, Wp, ldw, kst, lds0, w, l); NEXTK();
        stage(AF, a_rb_stride, Wp, ldw, kst, lds0 + STAGE, w, l); NEXTK();
        stage(AF, a_rb_stride, Wp, ldw, kst, lds0 + 2 * STAGE, w, l); NEXTK();
        for (int st = 0; st < nstage; ++st) {
            if (st + 2 < nstage) asm volatile("s_waitcnt vmcnt(8) lgkmcnt(0)\n\ts_barrier" ::: "memory");
            else if (st + 1 < nstage) asm volatile("s_waitcnt vmcnt(4) lgkmcnt(0)\n\ts_barrier" ::: "memory");
            else WAIT_ALL_BAR();
            if (st == 0) STAMP_AT(ws, kid, 1);
            const bool pre = st + 3 < nstage; const unsigned pdst = lds0 + ((st + 3) & 3) * STAGE;
            lds_cptr ab = (lds_cptr)lds + (st & 3) * STAGE;
            lds_cptr wb = ab + A_BYTES;
#pragma unroll
            for (int kk = 0; kk < BKS / KW; ++kk) {
                const int ks = kw * (BKS / KW) + kk;
                bf16x8 af[2], wf[2];
#pragma unroll
                for (int i = 0; i < 2; ++i) af[i] = ldsb128(ab + ((2 * tq + i) * BKS + ks) * 1024 + l * 16);
#pragma unroll
                for (int j = 0; j < 2; ++j) {
                    const int r0 = 16 * ks + (APERM ? 4 * hi : 8 * hi) + q4, r1 = r0 + (APERM ? 8 : 4);
                    const int ch = 4 * (2 * tn + j) + 2 * blk + (p4 >> 1);
                    wf[j] = wfrag(wb + r0 * RB + ((ch ^ swz(r0)) << 4) + 8 * (p4 & 1), wb + r1 * RB + ((ch ^ swz(r1)) << 4) + 8 * (p4 & 1));
                }
#pragma unroll
                for (int i = 0; i < 2; ++i)
#pragma unroll
                    for (int j = 0; j < 2; ++j) {
                        if (CT) acc[i][j] = MFMA(wf[j], af[i], acc[i][j]);
                        else acc[i][j] = MFMA(af[i], wf[j], acc[i][j]);
                        constexpr int PER = 4 / (BKS / KW);
                        const int m = i * 2 + j;
                        if (PER == 4) { SBAR(); if (pre) stage_piece(m, AF, a_rb_stride, Wp, ldw, kst, pdst, w, l); SBAR(); }
                        else if (m & 1) { SBAR(); if (pre) stage_piece(kk * 2 + (m >> 1), AF, a_rb_stride, Wp, ldw, kst, pdst, w, l); SBAR(); }
                    }
            }
            if (pre) NEXTK();
        }
        BAR_LGKM();
        STAMP_AT(ws, kid, 2);
        float* rb_ = (float*)lds;
#pragma unroll
        for (int i = 0; i < 2; ++i)
#pragma unroll
            for (int j = 0; j < 2; ++j)
#pragma unroll
                for (int r = 0; r < 16; ++r) rb_[(w * 64 + (i * 2 + j) * 16 + r) * 64 + l] = acc[i][j][r];
        BAR_LGKM();
#pragma unroll
        for (int e = 0; e < 64 / KW; ++e) {
            float s = 0.f;
#pragma unroll
            for (int k2 = 0; k2 < KW; ++k2) s += rb_[((tt * KW + k2) * 64 + kw * (64 / KW) + e) * 64 + l];
            red[e] = s;
        }
        STAMP_AT(ws, kid, 3);
    }
};

typedef G<2, 2, 2, 4, true, false> G3;
constexpr int K3_LDS = 131072;
__global__ __launch_bounds__(512, 2) void k3_oproj(const u16* __restrict__ wo, char* __restrict__ ws) {
    extern __shared__ __attribute__((aligned(16))) char lds[];
    const int b = blockIdx.x, xcd = b & 7, r = b >> 3, ksl = xcd & 3, rp = (xcd >> 2) * 8 + (r >> 2), ct = r & 3;
    const int l = threadIdx.x & 63, hi = l >> 5;
    float red[32]; int tq, tn, kw;
    G3::run(ws + WS_CTX + ((size_t)(rp * 4) * 256 + ksl * 64) * 1024, (size_t)256 * 1024, wo + (size_t)ksl * 1024 * 512 + ct * 128, 512, 16, (4 * ct + 2 * (r >> 2)) & 15, lds, red, tq, tn, kw, ws, 2);
    float* part = (float*)(ws + WS_PART) + (size_t)ksl * 2048 * 512;
#pragma unroll
    for (int e = 0; e < 32; ++e) {
        const int idx = kw * 32 + e, i = idx >> 5, j = (idx >> 4) & 1, rr = idx & 15;
        const int row = rp * 128 + 64 * tq + 32 * i + crow(rr, hi), col = ct * 128 + 64 * tn + 32 * j + (l & 31);
        part[(size_t)row * 512 + col] = red[e];
    }
    STAMP_AT(ws, 2, 4);
}

__global__ __launch_bounds__(512) void k3b_ln2(const u16* __restrict__ x, char* __restrict__ ws) {
    const int l = threadIdx.x & 63, row = blockIdx.x * 8 + (threadIdx.x >> 6);
    const float* part = (const float*)(ws + WS_PART) + (size_t)row * 512 + 8 * l;
    const float* mod = (const float*)(ws + WS_MOD);
    f32x4 pv[8];
#pragma unroll
    for (int s = 0; s < 4; ++s) { pv[2 * s] = *(const f32x4*)(part + (size_t)s * 2048 * 512); pv[2 * s + 1] = *(const f32x4*)(part + (size_t)s * 2048 * 512 + 4); }
    const u32x4 xv4 = *(const u32x4*)(x + (size_t)row * 512 + 8 * l);
    const f32x4 g0 = *(const f32x4*)(mod + 1024 + 8 * l), g1 = *(const f32x4*)(mod + 1024 + 8 * l + 4);
    const f32x4 c0 = *(const f32x4*)(mod + 2048 + 8 * l), c1 = *(const f32x4*)(mod + 2048 + 8 * l + 4);
    const f32x4 s0 = *(const f32x4*)(mod + 1536 + 8 * l), s1 = *(const f32x4*)(mod + 1536 + 8 * l + 4);
    float x1[8], xh[8];
#pragma unroll
    for (int j = 0; j < 4; ++j) {
        const float a0 = (pv[0][j] + pv[2][j]) + (pv[4][j] + pv[6][j]), a1 = (pv[1][j] + pv[3][j]) + (pv[5][j] + pv[7][j]);
        const float xa = (j & 1) ? bfhi(xv4[j >> 1]) : bflo(xv4[j >> 1]), xb = (j & 1) ? bfhi(xv4[2 + (j >> 1)]) : bflo(xv4[2 + (j >> 1)]);
        x1[j] = bfr(xa + bfr(g0[j] * bfr(a0)));
        x1[4 + j] = bfr(xb + bfr(g1[j] * bfr(a1)));
    }
    u32x4 o1 = {pk2(x1[0], x1[1]), pk2(x1[2], x1[3]), pk2(x1[4], x1[5]), pk2(x1[6], x1[7])};
    *(u32x4*)(ws + WS_X1 + ((size_t)row * 512 + 8 * l) * 2) = o1;
    ln_row(x1, xh);
    float h[8];
#pragma unroll
    for (int j = 0; j < 4; ++j) { h[j] = bfr(xh[j] * bfr(1.f + c0[j])) + s0[j]; h[4 + j] = bfr(xh[4 + j] * bfr(1.f + c1[j])) + s1[j]; }
    u32x4 oh = {pk2(h[0], h[1]), pk2(h[2], h[3]), pk2(h[4], h[5]), pk2(h[6], h[7])};
    *(u32x4*)(ws + WS_H2 + ((size_t)((row >> 5) * 32 + (l >> 1)) * 64 + (row & 31) + 32 * (l & 1)) * 16) = oh;
}

typedef G<2, 2, 2, 4, false, true> G4;
__global__ __launch_bounds__(512, 2) void k4_ffn1(const u16* __restrict__ w1, char* __restrict__ ws) {
    extern __shared__ __attribute__((aligned(16))) char lds[];
    const int b = blockIdx.x, xcd = b & 7, jj = b >> 3, rp = 4 * (xcd >> 1) + jj / 7, ct = 7 * (xcd & 1) + jj % 7;
    const int l = threadIdx.x & 63;
    float red[32]; int tq, tn, kw;
    G4::run(ws + WS_H2 + (size_t)(rp * 4) * 32 * 1024, (size_t)32 * 1024, w1 + ct * 128, DFF, 8, (jj % 7 + 2 * (jj / 7)) & 7, lds, red, tq, tn, kw, ws, 3);
#pragma unroll
    for (int j = 0; j < 2; ++j)
#pragma unroll
        for (int s = 0; s < 2; ++s) {
            const int e = j * 16 + 8 * s;
            u32x4 v = {pk2(fmaxf(red[e], 0.f), fmaxf(red[e + 1], 0.f)), pk2(fmaxf(red[e + 2], 0.f), fmaxf(red[e + 3], 0.f)),
                       pk2(fmaxf(red[e + 4], 0.f), fmaxf(red[e + 5], 0.f)), pk2(fmaxf(red[e + 6], 0.f), fmaxf(red[e + 7], 0.f))};
            const int rbq = rp * 4 + 2 * tq + kw, kstep = (ct * 128 + 64 * tn + 32 * j) / 16 + s;
            *(u32x4*)(ws + WS_H1 + ((size_t)(rbq * 112 + kstep) * 64 + l) * 16) = v;
        }
    STAMP_AT(ws, 3, 4);
}

typedef G<1, 1, 8, 8, true, false> G5;
__global__ __launch_bounds__(512, 2) void k5_ffn2(const u16* __restrict__ w2, char* __restrict__ ws, u16* __restrict__ out) {
    extern __shared__ __attribute__((aligned(16))) char lds[];
    const int b = blockIdx.x, xcd = b & 7, jj = b >> 3, rp = 8 * (xcd >> 1) + (jj >> 2), ct = 4 * (xcd & 1) + (jj & 3);
    const int l = threadIdx.x & 63, hi = l >> 5;
    float red[8]; int tq, tn, kw;
    G5::run(ws + WS_H1 + (size_t)(rp * 2) * 112 * 1024, (size_t)112 * 1024, w2 + ct * 64, 512, 14, (3 * (jj & 3) + 2 * (jj >> 2)) % 14, lds, red, tq, tn, kw, ws, 4);
    const float* mod = (const float*)(ws + WS_MOD);
    const u16* x1 = (const u16*)(ws + WS_X1);
#pragma unroll
    for (int e = 0; e < 8; ++e) {
        const int idx = kw * 8 + e, i = idx >> 5, j = (idx >> 4) & 1, rr = idx & 15;
        const int row = rp * 64 + 32 * i + crow(rr, hi), col = ct * 64 + 32 * j + (l & 31);
        const float v = bfr(bf2f(x1[(size_t)row * 512 + col]) + bfr(mod[2560 + col] * bfr(red[e])));
        out[(size_t)row * 512 + col] = (u16)(__float_as_uint(v) >> 16);
    }
    STAMP_AT(ws, 4, 4);
}

extern "C" void kernel_launch(void* const* d_in, const int* in_sizes, int n_in, void* d_out, int out_size, void* d_ws, size_t ws_size,
                              hipStream_t stream) {
    (void)in_sizes; (void)n_in; (void)out_size; (void)ws_size;
    const u16* x = (const u16*)d_in[0];
    const u16* ckv = (const u16*)d_in[1];
    const u16* wq = (const u16*)d_in[2];
    const u16* wuk = (const u16*)d_in[3];
    const u16* wuv = (const u16*)d_in[4];
    const u16* wo = (const u16*)d_in[5];
    const u16* w1 = (const u16*)d_in[7];
    const u16* w2 = (const u16*)d_in[8];
    const u16* cvec = (const u16*)d_in[9];
    const u16* wmod = (const u16*)d_in[10];
    char* ws = (char*)d_ws;
    hipFuncSetAttribute((const void*)k1_kvproj, hipFuncAttributeMaxDynamicSharedMemorySize, K1_LDS);
    hipFuncSetAttribute((const void*)k2_attn, hipFuncAttributeMaxDynamicSharedMemorySize, K2_LDS);
    hipFuncSetAttribute((const void*)k3_oproj, hipFuncAttributeMaxDynamicSharedMemorySize, K3_LDS);
    hipFuncSetAttribute((const void*)k4_ffn1, hipFuncAttributeMaxDynamicSharedMemorySize, K3_LDS);
    hipFuncSetAttribute((const void*)k5_ffn2, hipFuncAttributeMaxDynamicSharedMemorySize, K3_LDS);
    k1_kvproj<<<256, 512, K1_LDS, stream>>>(ckv, wuk, wuv, x, cvec, wmod, ws);
    k2_attn<<<256, 512, K2_LDS, stream>>>(wq, ws);
    k3_oproj<<<256, 512, K3_LDS, stream>>>(wo, ws);
    k3b_ln2<<<256, 512, 0, stream>>>(x, ws);
    k4_ffn1<<<224, 512, K3_LDS, stream>>>(w1, ws);
    k5_ffn2<<<256, 512, K3_LDS, stream>>>(w2, ws, (u16*)d_out);
}
```

```cpp
#include <hip/hip_runtime.h>
#include <hip/hip_bf16.h>
#include <stdint.h>

typedef unsigned short u16;
typedef short bf16x8 __attribute__((ext_vector_type(8)));
typedef short s16x4 __attribute__((ext_vector_type(4)));
typedef short v4i16_t __attribute__((ext_vector_type(4)));
typedef float f32x16 __attribute__((ext_vector_type(16)));
typedef float f32x4 __attribute__((ext_vector_type(4)));
typedef float f32x2 __attribute__((ext_vector_type(2)));
typedef unsigned u32x4 __attribute__((ext_vector_type(4)));
typedef __bf16 bf16x2_t __attribute__((ext_vector_type(2)));
typedef __attribute__((address_space(3))) const char* lds_cptr;
#define DI __device__ __forceinline__
#define MFMA(a, b, c) __builtin_amdgcn_mfma_f32_32x32x16_bf16((a), (b), (c), 0, 0, 0)
#define SBAR() __builtin_amdgcn_sched_barrier(0)
#define STAMPX(ws_, idx) do { } while (0)
#define STAMP_AT(ws_, kid, idx) do { } while (0)

constexpr int SEQ = 2048, DM = 512, NH = 32, HD = 128, DFF = 1792;

constexpr size_t WS_KV = 0;
constexpr size_t WS_XH = WS_KV + (size_t)32 * 32 * 32768;
constexpr size_t WS_MOD = WS_XH + (size_t)64 * 32 * 1024;
constexpr size_t WS_GS = WS_MOD + 12288;
constexpr size_t WS_CTX = WS_MOD + 16384;
constexpr size_t WS_PART = WS_CTX + (size_t)64 * 256 * 1024;
constexpr size_t WS_X1 = WS_PART + (size_t)4 * 2048 * 512 * 4;
constexpr size_t WS_H2 = WS_X1 + (size_t)2048 * 512 * 2;
constexpr size_t WS_H1 = WS_H2 + (size_t)64 * 32 * 1024;

DI unsigned pk2(float a, float b) { f32x2 v = {a, b}; bf16x2_t r = __builtin_convertvector(v, bf16x2_t); return __builtin_bit_cast(unsigned, r); }
DI float bf2f(u16 v) { return __uint_as_float(((unsigned)v) << 16); }
DI float bfr(float x) { return __uint_as_float(pk2(x, 0.f) << 16); }
DI float bflo(unsigned w) { return __uint_as_float(w << 16); }
DI float bfhi(unsigned w) { return __uint_as_float(w & 0xffff0000u); }
DI bf16x8 pack8(const f32x16& a, int s) {
    u32x4 w = {pk2(a[8 * s], a[8 * s + 1]), pk2(a[8 * s + 2], a[8 * s + 3]), pk2(a[8 * s + 4], a[8 * s + 5]), pk2(a[8 * s + 6], a[8 * s + 7])};
    return __builtin_bit_cast(bf16x8, w);
}
DI int crow(int r, int hi) { return (r & 3) + 8 * (r >> 2) + 4 * hi; }
DI void glds16(const void* g, unsigned lds_base) {
    unsigned sv;
    asm volatile("s_mov_b32 %0, m0\n\ts_mov_b32 m0, %2\n\ts_nop 0\n\tglobal_load_lds_dwordx4 %1, off\n\ts_mov_b32 m0, %0"
                 : "=&s"(sv) : "v"(g), "s"(lds_base) : "memory");
}
DI bf16x8 ldsb128(lds_cptr p) { return *(const __attribute__((address_space(3))) bf16x8*)p; }
DI s16x4 ldstr(lds_cptr p) { return __builtin_bit_cast(s16x4, __builtin_amdgcn_ds_read_tr16_b64_v4i16((__attribute__((address_space(3))) v4i16_t*)p)); }
DI bf16x8 wfrag(lds_cptr p0, lds_cptr p1) { s16x4 lo = ldstr(p0), hi = ldstr(p1); return __builtin_shufflevector(lo, hi, 0, 1, 2, 3, 4, 5, 6, 7); }
#define WAIT_ALL_BAR() asm volatile("s_waitcnt vmcnt(0) lgkmcnt(0)\n\ts_barrier" ::: "memory")
#define BAR_LGKM() asm volatile("s_waitcnt lgkmcnt(0)\n\ts_barrier" ::: "memory")
DI float wave_sum(float v) {
#pragma unroll
    for (int m = 1; m < 64; m <<= 1) v += __shfl_xor(v, m);
    return v;
}
DI float half_swap_max(float m) { auto rr = __builtin_amdgcn_permlane32_swap(__float_as_uint(m), __float_as_uint(m), false, false); return fmaxf(__uint_as_float(rr[0]), __uint_as_float(rr[1])); }
DI float half_swap_sum(float m) { auto rr = __builtin_amdgcn_permlane32_swap(__float_as_uint(m), __float_as_uint(m), false, false); return __uint_as_float(rr[0]) + __uint_as_float(rr[1]); }

DI void ln_row(const float (&xv)[8], float (&o)[8]) {
    float s = 0.f;
#pragma unroll
    for (int j = 0; j < 8; ++j) s += xv[j];
    const float mean = wave_sum(s) * (1.f / 512.f);
    float q = 0.f;
#pragma unroll
    for (int j = 0; j < 8; ++j) { const float d = xv[j] - mean; q += d * d; }
    const float var = wave_sum(q) * (1.f / 512.f);
    const float mb = bfr(mean), vb = bfr(var);
    const float den = bfr(sqrtf(bfr(vb + 1e-5f)));
#pragma unroll
    for (int j = 0; j < 8; ++j) o[j] = bfr(bfr(xv[j] - mb) / den);
}

constexpr int K1_LDS = 131072;
template <bool VT>
DI void k1_gemm(const u16* __restrict__ ckv, const u16* __restrict__ W, char* __restrict__ kvimg, char* lds, int key0, int h0, int h1, int rot,
                 const u16* __restrict__ x, const u16* __restrict__ cvec, const u16* __restrict__ wmod, char* __restrict__ ws) {
    const int tid = threadIdx.x, l = tid & 63, r32 = l & 31, hi = l >> 5;
    const int w = __builtin_amdgcn_readfirstlane(tid >> 6);
    const int wk = w >> 2, wc = w & 3;
    const unsigned lds0 = (unsigned)(uintptr_t)lds;
    const u16* asrc[4]; const u16* wsrc[4];
#pragma unroll
    for (int n = 0; n < 4; ++n) {
        const int P = n * 8 + w, row = 8 * P + (l >> 3), c = (l & 7) ^ ((row >> 1) & 7);
        asrc[n] = ckv + (size_t)(key0 + row) * 512 + c * 8;
        const int half = P >> 4, rw = 4 * (P & 15) + (l >> 4), cw = (l & 15) ^ ((rw & 3) << 2);
        wsrc[n] = W + (size_t)rw * 4096 + (half ? h1 : h0) * 128 + cw * 8;
    }
#define K1_PIECE(i, kt_, bb_) do { if ((i) < 4) glds16(asrc[i] + (kt_) * 64, (bb_) + ((i) * 8 + w) * 1024); \
        else glds16(wsrc[(i) - 4] + (size_t)(kt_) * 64 * 4096, (bb_) + 32768 + (((i) - 4) * 8 + w) * 1024); } while (0)
#pragma unroll
    for (int i = 0; i < 8; ++i) K1_PIECE(i, rot, lds0);
    {
        const int b = blockIdx.x, row = 8 * b + w, half = tid & 1, rowi = tid >> 1, bm = b < 192 ? b : 0;
        const u32x4 xv4 = *(const u32x4*)(x + (size_t)row * 512 + 8 * l);
        const u32x4 wv0 = *(const u32x4*)(wmod + (size_t)rowi * 3072 + 16 * bm + 8 * half), wv1 = *(const u32x4*)(wmod + (size_t)(rowi + 256) * 3072 + 16 * bm + 8 * half);
        const float c0 = bf2f(cvec[rowi]), c1 = bf2f(cvec[rowi + 256]);
        float* scr = (float*)(lds + 65536);
        {
            float xv[8], o[8];
#pragma unroll
            for (int j = 0; j < 4; ++j) { xv[2 * j] = bflo(xv4[j]); xv[2 * j + 1] = bfhi(xv4[j]); }
            ln_row(xv, o);
            u32x4 ov = {pk2(o[0], o[1]), pk2(o[2], o[3]), pk2(o[4], o[5]), pk2(o[6], o[7])};
            *(u32x4*)(ws + WS_XH + ((size_t)((row >> 5) * 32 + (l >> 1)) * 64 + (row & 31) + 32 * (l & 1)) * 16) = ov;
        }
        if (b < 192) {
            float a8[8];
            const float sc0 = bfr(c0 / (1.f + __expf(-c0))), sc1 = bfr(c1 / (1.f + __expf(-c1)));
#pragma unroll
            for (int j = 0; j < 4; ++j) { a8[2 * j] = sc0 * bflo(wv0[j]) + sc1 * bflo(wv1[j]); a8[2 * j + 1] = sc0 * bfhi(wv0[j]) + sc1 * bfhi(wv1[j]); }
#pragma unroll
            for (int m = 2; m < 64; m <<= 1)
#pragma unroll
                for (int j = 0; j < 8; ++j) a8[j] += __shfl_xor(a8[j], m);
            if (l < 2) {
#pragma unroll
                for (int j = 0; j < 8; ++j) scr[(w * 2 + l) * 8 + j] = a8[j];
            }
            BAR_LGKM();
            if (tid < 16) {
                float s = 0.f;
#pragma unroll
                for (int ww = 0; ww < 8; ++ww) s += scr[(ww * 2 + (tid >> 3)) * 8 + (tid & 7)];
                const int col = 16 * b + tid; const float mv = bfr(s);
                ((float*)(ws + WS_MOD))[col] = mv;
                if (col < 512) ((float*)(ws + WS_GS))[512 + col] = mv;
                else if (col < 1024) ((float*)(ws + WS_GS))[col - 512] = bfr(1.f + mv);
            }
        }
    }
    f32x16 acc[4][2];
#pragma unroll
    for (int a = 0; a < 4; ++a) { acc[a][0] = f32x16{}; acc[a][1] = f32x16{}; }
    const int q4 = (l & 15) >> 2, p4 = l & 3, blk = (l >> 4) & 1;
    const int xa = (l >> 1) & 7;
    STAMP_AT(kvimg, 0, 1);
    for (int kt = 0; kt < 8; ++kt) {
        WAIT_ALL_BAR();
        if (kt == 0) STAMP_AT(kvimg, 0, 2);
        const bool pre = kt + 1 < 8;
        const int ktn = (rot + kt + 1) & 7;
        const unsigned bb = lds0 + ((kt + 1) & 1) * 65536;
        lds_cptr ab = (lds_cptr)lds + (kt & 1) * 65536;
        lds_cptr wb = ab + 32768 + (wc >> 1) * 16384;
#pragma unroll
        for (int s = 0; s < 4; ++s) {
            bf16x8 af[4], wf[2];
#pragma unroll
            for (int kb = 0; kb < 4; ++kb) af[kb] = ldsb128(ab + (128 * wk + 32 * kb + r32) * 128 + (((2 * s + hi) ^ xa) << 4));
#pragma unroll
            for (int j = 0; j < 2; ++j) {
                const int db = 2 * (wc & 1) + j;
                lds_cptr p = wb + (16 * s + 8 * hi + q4) * 256 + 64 * (db ^ q4) + 32 * blk + 16 * (p4 >> 1) + 8 * (p4 & 1);
                wf[j] = wfrag(p, p + 1024);
            }
#pragma unroll
            for (int kb = 0; kb < 4; ++kb) {
#pragma unroll
                for (int j = 0; j < 2; ++j) {
                    if (VT) acc[kb][j] = MFMA(af[kb], wf[j], acc[kb][j]);
                    else acc[kb][j] = MFMA(wf[j], af[kb], acc[kb][j]);
                }
                if ((kb & 1) == 0) { if (pre) K1_PIECE(2 * s + (kb >> 1), ktn, bb); }
            }
        }
    }
#undef K1_PIECE
    STAMP_AT(kvimg, 0, 3);
    const int head = (wc >> 1) ? h1 : h0;
#pragma unroll
    for (int kb = 0; kb < 4; ++kb) {
        const int key = key0 + 128 * wk + 32 * kb, tile = key >> 6, kbl = kb & 1;
        char* tb = kvimg + ((size_t)(head * 32 + tile) << 15);
#pragma unroll
        for (int j = 0; j < 2; ++j) {
            const int db = 2 * (wc & 1) + j;
#pragma unroll
            for (int s2 = 0; s2 < 2; ++s2) {
                const int fo = VT ? (16384 + (db * 4 + kbl * 2 + s2) * 1024) : ((kbl * 8 + db * 2 + s2) * 1024);
                *(bf16x8*)(tb + fo + l * 16) = pack8(acc[kb][j], s2);
            }
        }
    }
}

__global__ __launch_bounds__(512, 2) void k1_kvproj(const u16* __restrict__ ckv, const u16* __restrict__ wuk, const u16* __restrict__ wuv,
                                                    const u16* __restrict__ x, const u16* __restrict__ cvec, const u16* __restrict__ wmod,
                                                    char* __restrict__ ws) {
    extern __shared__ __attribute__((aligned(16))) char lds[];
    const int b = blockIdx.x, tid = threadIdx.x, l = tid & 63;
    const int w = __builtin_amdgcn_readfirstlane(tid >> 6);
    const int xcd = b & 7, r = b >> 3, kb8 = r & 7, combo = r >> 3, type = combo & 1, hp = combo >> 1;
    const int h0 = xcd + 16 * hp, h1 = h0 + 8, key0 = kb8 * 256;
    const u16* W = type ? wuv : wuk;
    STAMP_AT(ws, 0, 0);
    const int rot = (kb8 + 2 * combo) & 7;
    if (type) k1_gemm<true>(ckv, W, ws + WS_KV, lds, key0, h0, h1, rot, x, cvec, wmod, ws);
    else k1_gemm<false>(ckv, W, ws + WS_KV, lds, key0, h0, h1, rot, x, cvec, wmod, ws);
    STAMP_AT(ws, 0, 4);
}

constexpr float ATT_THR = 8.f;
#define PIN(x) asm volatile("" : "+v"(x))
#define MX3(a, b, c) __builtin_fmaxf(__builtin_fmaxf((a), (b)), (c))
DI float att_rowmax(const f32x16& p0, const f32x16& p1) {
    float a = MX3(p0[0], p0[1], p1[0]), b = MX3(p0[2], p0[3], p1[1]); a = MX3(a, p1[2], p1[3]);
#pragma unroll
    for (int r = 4; r < 16; r += 4) { a = MX3(a, p0[r], p0[r + 1]); b = MX3(b, p0[r + 2], p0[r + 3]); a = MX3(a, p1[r], p1[r + 1]); b = MX3(b, p1[r + 2], p1[r + 3]); }
    return __builtin_fmaxf(a, b);
}
DI void att_mask(f32x16& s0, f32x16& s1, int dq) {
#pragma unroll
    for (int r = 0; r < 16; ++r) {
        const int c = (r & 3) + 8 * (r >> 2);
        if (c > dq) s0[r] = -INFINITY;
        if (c + 32 > dq) s1[r] = -INFINITY;
    }
}
constexpr int PD = 6;
DI int kfrag_off(int g) { return ((g & 1) * 8 + (g >> 1)) * 1024; }
DI int vfrag_off(int g) { return ((g & 3) * 4 + (g >> 2)) * 1024; }
template <bool MASK>
DI void att_step(f32x16& C0, f32x16& C1, const f32x16& P0, f32x16& P1, f32x16 (&o)[4], f32x16& negm, float& l_run,
                 const bf16x8 (&qf)[8], bf16x8 (&fr)[8], lds_cptr kp, lds_cptr vp, lds_cptr kpn, int dq) {
    unsigned pw[16];
    float sacc = 0.f, sacc2 = 0.f;
    SBAR();
#pragma unroll
    for (int g = 0; g < 16; ++g) {
        if ((g & 1) == 0) C0 = MFMA(fr[g & 7], qf[g >> 1], g < 2 ? negm : C0);
        else C1 = MFMA(fr[g & 7], qf[g >> 1], g < 2 ? negm : C1);
        if (g + PD < 16) fr[(g + PD) & 7] = ldsb128(kp + kfrag_off(g + PD));
        else fr[(g + PD) & 7] = ldsb128(vp + vfrag_off(g + PD - 16));
        P1[g] = __builtin_amdgcn_exp2f(P1[g]); PIN(P1[g]);
        sacc += P0[g]; PIN(sacc);
        if (g & 1) { pw[g >> 1] = pk2(P0[g - 1], P0[g]); PIN(pw[g >> 1]); }
        if (g >= 1) { sacc2 += P1[g - 1]; PIN(sacc2); }
        if (g >= 2 && (g & 1) == 0) { pw[8 + ((g - 2) >> 1)] = pk2(P1[g - 2], P1[g - 1]); PIN(pw[8 + ((g - 2) >> 1)]); }
        SBAR();
    }
    sacc2 += P1[15]; pw[15] = pk2(P1[14], P1[15]);
    l_run += sacc + sacc2;
    bool resc = false; float f = 1.f;
    float ma, mb;
#pragma unroll
    for (int g = 0; g < 16; ++g) {
        { const u32x4 pv = {pw[4 * (g >> 2)], pw[4 * (g >> 2) + 1], pw[4 * (g >> 2) + 2], pw[4 * (g >> 2) + 3]};
          o[g & 3] = MFMA(fr[g & 7], __builtin_bit_cast(bf16x8, pv), o[g & 3]); }
        if (g + PD < 16) fr[(g + PD) & 7] = ldsb128(vp + vfrag_off(g + PD));
        else fr[(g + PD) & 7] = ldsb128(kpn + kfrag_off(g + PD - 16));
        if (g == 1 && MASK) att_mask(C0, C1, dq);
        if (g == 1) { ma = MX3(C0[0], C0[1], C0[2]); mb = MX3(C0[3], C0[4], C0[5]); ma = MX3(ma, C0[6], C0[7]); mb = MX3(mb, C0[8], C0[9]); PIN(ma); PIN(mb); }
        if (g == 2) { ma = MX3(ma, C0[10], C0[11]); mb = MX3(mb, C0[12], C0[13]); ma = MX3(ma, C0[14], C0[15]); mb = MX3(mb, C1[0], C1[1]); PIN(ma); PIN(mb); }
        if (g == 3) { ma = MX3(ma, C1[2], C1[3]); mb = MX3(mb, C1[4], C1[5]); ma = MX3(ma, C1[6], C1[7]); mb = MX3(mb, C1[8], C1[9]); PIN(ma); PIN(mb); }
        if (g == 4) {
            ma = MX3(ma, C1[10], C1[11]); mb = MX3(mb, C1[12], C1[13]); ma = MX3(ma, C1[14], C1[15]);
            const float rm = __builtin_fmaxf(ma, mb);
            if (__builtin_expect(__any(rm > ATT_THR), 0)) {
                const float dl = __builtin_fmaxf(half_swap_max(rm), 0.f);
                f = __builtin_amdgcn_exp2f(-dl); l_run *= f; resc = true;
#pragma unroll
                for (int r = 0; r < 16; ++r) { C0[r] -= dl; C1[r] -= dl; negm[r] -= dl; }
            }
        }
        if (g >= 5) {
            const int e0 = g < 10 ? 2 * (g - 5) : 10 + (g - 10), e1 = g < 10 ? e0 + 2 : e0 + 1;
#pragma unroll
            for (int e = e0; e < e1; ++e) { C0[e] = __builtin_amdgcn_exp2f(C0[e]); PIN(C0[e]); }
        }
        SBAR();
    }
    if (__builtin_expect(resc, 0)) {
#pragma unroll
        for (int d = 0; d < 4; ++d)
#pragma unroll
            for (int r = 0; r < 16; ++r) o[d][r] *= f;
    }
}
constexpr int K2_LDS = 147456 + 4096;
constexpr float QSCALE = 0.08838834764831845f * 1.4426950408889634f;
__global__ __launch_bounds__(512, 2) void k2_attn(const u16* __restrict__ wq, char* __restrict__ ws) {
    extern __shared__ __attribute__((aligned(16))) char lds[];
    const int b = blockIdx.x, tid = threadIdx.x, l = tid & 63, r32 = l & 31, hi = l >> 5;
    const int w = __builtin_amdgcn_readfirstlane(tid >> 6);
    const int xcd = b & 7, kk = b >> 3, head = xcd + 8 * (kk >> 3), pi = kk & 7;
    const int qb = (w < 4) ? pi : 15 - pi, sub = w & 3, rb = qb * 4 + sub;
    const int tl = 2 * qb + (sub >> 1), NT = 32 - 2 * pi;
    const unsigned lds0 = (unsigned)(uintptr_t)lds;
    STAMP_AT(ws, 1, 0);
    const char* kvh = ws + WS_KV + ((size_t)head << 20);
    const char* ksrc = kvh + w * 2048 + l * 16;
    const unsigned kdst = lds0 + w * 2048, vdst = lds0 + 65536 + w * 2048;
#define DMA_K(t_, slot_) do { const char* g_ = ksrc + (size_t)(t_) * 32768; glds16(g_, kdst + (slot_)); glds16(g_ + 1024, kdst + (slot_) + 1024); } while (0)
#define DMA_V(t_, slot_) do { const char* g_ = ksrc + (size_t)(t_) * 32768 + 16384; glds16(g_, vdst + (slot_)); glds16(g_ + 1024, vdst + (slot_) + 1024); } while (0)
    DMA_K(0, 0);
    glds16(ws + WS_GS + (w & 3) * 1024 + l * 16, lds0 + 147456 + (w & 3) * 1024);
    u32x4 xf[16];
    const char* xsrc = ws + WS_XH + ((size_t)(rb * 32) * 64 + l) * 16;
#define XISSUE(c) do { _Pragma("unroll") for (int k8 = 0; k8 < 8; ++k8) asm volatile("global_load_dwordx4 %0, %1, off" : "=v"(xf[8 * ((c) & 1) + k8]) : "v"(xsrc + (8 * (c) + k8) * 1024) : "memory"); } while (0)
#define WISSUE(c) do { _Pragma("unroll") for (int n = 0; n < 4; ++n) { const int P = (4 * (c) + n) * 8 + w, row = 4 * P + (l >> 4), cc = (l & 15) ^ ((row & 3) << 2); \
            glds16(wq + (size_t)row * 4096 + head * 128 + cc * 8, lds0 + 16384 + P * 1024); } } while (0)
    XISSUE(0); WISSUE(0); XISSUE(1); WISSUE(1);
    const int q4 = (l & 15) >> 2, p4 = l & 3, blk = (l >> 4) & 1;
    const lds_cptr gsp = (lds_cptr)lds + 147456 + 32 * hi;
    lds_cptr wp[4];
#pragma unroll
    for (int db = 0; db < 4; ++db) wp[db] = (lds_cptr)lds + 16384 + (8 * hi + q4) * 256 + 64 * (db ^ q4) + 32 * blk + 16 * (p4 >> 1) + 8 * (p4 & 1);
    bf16x8 qf[8];
    {
        f32x16 qa[4];
#pragma unroll
        for (int d = 0; d < 4; ++d) qa[d] = f32x16{};
#define XWAIT(N, c) asm volatile("s_waitcnt vmcnt(" #N ") lgkmcnt(0)\n\ts_barrier" : "+v"(xf[8 * c]), "+v"(xf[8 * c + 1]), "+v"(xf[8 * c + 2]), "+v"(xf[8 * c + 3]), \
                                 "+v"(xf[8 * c + 4]), "+v"(xf[8 * c + 5]), "+v"(xf[8 * c + 6]), "+v"(xf[8 * c + 7]) :: "memory")
#define XONE(c, k8) asm volatile("global_load_dwordx4 %0, %1, off" : "=v"(xf[8 * ((c) & 1) + (k8)]) : "v"(xsrc + (8 * (c) + (k8)) * 1024) : "memory")
#define WONE(c, n) do { const int P = (4 * (c) + (n)) * 8 + w, row = 4 * P + (l >> 4), cc = (l & 15) ^ ((row & 3) << 2); \
            glds16(wq + (size_t)row * 4096 + head * 128 + cc * 8, lds0 + 16384 + P * 1024); } while (0)
#define KVONE(off_g, dst_) glds16(ksrc + (off_g), (dst_))
#define LDF4(p_) (*(const __attribute__((address_space(3))) f32x4*)(p_))
#define HPART(j_, G_, S_, e_) pk2(fmaf(bflo(xw[j_]), G_[e_], S_[e_]), fmaf(bfhi(xw[j_]), G_[e_ + 1], S_[e_ + 1]))
#pragma unroll
        for (int c = 0; c < 4; ++c) {
            STAMPX(ws, 2 * c);
            if (c == 0) { XWAIT(12, 0); STAMP_AT(ws, 1, 1); }
            if (c == 1) { XWAIT(4, 1); }
            if (c == 2) { XWAIT(8, 0); }
            if (c == 3) { XWAIT(2, 1); }
            STAMPX(ws, 2 * c + 1);
            bf16x8 wf[2][4]; f32x4 g0, g1, s0, s1; u32x4 hc;
            {
                const int ks = 8 * c;
                g0 = LDF4(gsp + 64 * ks); g1 = LDF4(gsp + 64 * ks + 16); s0 = LDF4(gsp + 2048 + 64 * ks); s1 = LDF4(gsp + 2048 + 64 * ks + 16);
#pragma unroll
                for (int db = 0; db < 4; ++db) wf[0][db] = wfrag(wp[db] + ks * 4096, wp[db] + ks * 4096 + 1024);
                const u32x4 xw = xf[8 * (c & 1)];
                hc = (u32x4){HPART(0, g0, s0, 0), HPART(1, g0, s0, 2), HPART(2, g1, s1, 0), HPART(3, g1, s1, 2)};
                g0 = LDF4(gsp + 64 * (ks + 1)); g1 = LDF4(gsp + 64 * (ks + 1) + 16); s0 = LDF4(gsp + 2048 + 64 * (ks + 1)); s1 = LDF4(gsp + 2048 + 64 * (ks + 1) + 16);
            }
            SBAR();
#pragma unroll
            for (int k8 = 0; k8 < 8; ++k8) {
                const int ks = 8 * c + k8;
                const bf16x8 hf = __builtin_bit_cast(bf16x8, hc);
                u32x4 hn = hc;
                const u32x4 xw = xf[8 * (c & 1) + (k8 < 7 ? k8 + 1 : k8)];
#pragma unroll
                for (int db = 0; db < 4; ++db) {
                    qa[db] = MFMA(wf[k8 & 1][db], hf, qa[db]);
                    {
                        const int ix = k8 * 4 + db;
                        if (c == 0) { if (ix < 4) WONE(2, ix); }
                        if (c == 1) { if (ix < 8) XONE(2, ix); else if (ix < 12) WONE(3, ix - 8);
                                      else if (ix < 16) KVONE((size_t)(1 + ((ix - 12) >> 1)) * 32768 + ((ix - 12) & 1) * 1024, kdst + 16384 * (1 + ((ix - 12) >> 1)) + ((ix - 12) & 1) * 1024); }
                        if (c == 2) { if (ix < 8) XONE(3, ix); else if (ix < 10) KVONE(16384 + (ix - 8) * 1024, vdst + (ix - 8) * 1024); }
                        if (c == 3) { if (ix < 2) KVONE(32768 + 16384 + ix * 1024, vdst + 16384 + ix * 1024); }
                    }
                    if (k8 < 7) {
                        wf[(k8 + 1) & 1][db] = wfrag(wp[db] + (ks + 1) * 4096, wp[db] + (ks + 1) * 4096 + 1024);
                        if (db == 0) hn[0] = HPART(0, g0, s0, 0);
                        if (db == 1) hn[1] = HPART(1, g0, s0, 2);
                        if (db == 2) hn[2] = HPART(2, g1, s1, 0);
                        if (db == 3) { hn[3] = HPART(3, g1, s1, 2); }
                        PIN(hn);
                        if (db == 1 && k8 < 6) { g0 = LDF4(gsp + 64 * (ks + 2)); s0 = LDF4(gsp + 2048 + 64 * (ks + 2)); }
                        if (db == 3 && k8 < 6) { g1 = LDF4(gsp + 64 * (ks + 2) + 16); s1 = LDF4(gsp + 2048 + 64 * (ks + 2) + 16); }
                    }
                    SBAR();
                }
                hc = hn;
            }
        }
#undef XWAIT
#undef XISSUE
#undef WISSUE
#undef HPART
#undef XONE
#undef WONE
#undef KVONE
#pragma unroll
        for (int db = 0; db < 4; ++db) {
#pragma unroll
            for (int r = 0; r < 16; ++r) qa[db][r] *= QSCALE;
            qf[2 * db] = pack8(qa[db], 0); qf[2 * db + 1] = pack8(qa[db], 1);
        }
    }
    STAMP_AT(ws, 1, 2); STAMPX(ws, 8);
#define WAIT_BAR4() asm volatile("s_waitcnt vmcnt(4)\n\ts_barrier" ::: "memory")
#define WAITB(t_) do { if ((t_) < tsw) WAIT_BAR4(); else asm volatile("s_waitcnt vmcnt(0)\n\ts_barrier" ::: "memory"); } while (0)
    float l_run = 0.f;
    f32x16 o[4], negm;
#pragma unroll
    for (int d = 0; d < 4; ++d) o[d] = f32x16{};
    const int qrow = rb * 32 + r32;
    const lds_cptr kp0 = (lds_cptr)lds + l * 16, vp0 = (lds_cptr)lds + 65536 + l * 16;
    f32x16 A0, A1, B0, B1;
    bf16x8 pf0, pf1, pf2, pf3, fr[8];
    int sl_prev = 49152, sl_cur = 0;
#define ROT() do { sl_prev = sl_cur; sl_cur = (sl_cur + 16384) & 65535; } while (0)
    const int tsw = 2 * pi + 3;
    const char* hsrc = kvh + (w & 3) * 4096 + l * 16;
    const unsigned hk = lds0 + (w & 3) * 4096, hv = lds0 + 65536 + (w & 3) * 4096;
#define STAGE_ISSUE(t_) do { if ((t_) < tsw) { const int tk_ = (t_) + 3 < NT ? (t_) + 3 : NT - 1, tv_ = (t_) + 2 < NT ? (t_) + 2 : NT - 1; DMA_K(tk_, sl_prev); DMA_V(tv_, (sl_cur + 32768) & 65535); } } while (0)
#define DMA_ARGS(t_) (t_) < tsw, ksrc + (size_t)((t_) + 3 < NT ? (t_) + 3 : NT - 1) * 32768, kdst + sl_prev, ksrc + (size_t)((t_) + 2 < NT ? (t_) + 2 : NT - 1) * 32768 + 16384, vdst + ((sl_cur + 32768) & 65535)
#define STAGE_HELP(t_) do { const int tk_ = (t_) + 3 < NT ? (t_) + 3 : NT - 1, tv_ = (t_) + 2 < NT ? (t_) + 2 : NT - 1; \
        const char* gk_ = hsrc + (size_t)tk_ * 32768; const char* gv_ = hsrc + (size_t)tv_ * 32768 + 16384; const unsigned sk_ = hk + sl_prev, sv_ = hv + ((sl_cur + 32768) & 65535); \
        glds16(gk_, sk_); glds16(gk_ + 1024, sk_ + 1024); glds16(gk_ + 2048, sk_ + 2048); glds16(gk_ + 3072, sk_ + 3072); \
        glds16(gv_, sv_); glds16(gv_ + 1024, sv_ + 1024); glds16(gv_ + 2048, sv_ + 2048); glds16(gv_ + 3072, sv_ + 3072); } while (0)
    asm volatile("s_waitcnt vmcnt(2) lgkmcnt(0)\n\ts_barrier" ::: "memory");
    STAGE_ISSUE(0);
    {
        A0 = f32x16{}; A1 = f32x16{};
#pragma unroll
        for (int ds = 0; ds < 8; ++ds) {
            A0 = MFMA(ldsb128(kp0 + ds * 1024), qf[ds], A0);
            A1 = MFMA(ldsb128(kp0 + 8192 + ds * 1024), qf[ds], A1);
        }
        if (tl == 0) att_mask(A0, A1, qrow - 4 * hi);
        const float mx = half_swap_max(att_rowmax(A0, A1));
#pragma unroll
        for (int r = 0; r < 16; ++r) { A0[r] = __builtin_amdgcn_exp2f(A0[r] - mx); A1[r] = A1[r] - mx; negm[r] = -mx; }
#pragma unroll
        for (int g = 0; g < PD; ++g) fr[g] = ldsb128(kp0 + 16384 + kfrag_off(g));
    }
    WAIT_BAR4(); ROT();
    int t = 1;
    for (; t + 1 <= tl - 1; t += 2) {
        STAGE_ISSUE(t); att_step<false>(B0, B1, A0, A1, o, negm, l_run, qf, fr, kp0 + sl_cur, vp0 + sl_prev, kp0 + ((sl_cur + 16384) & 65535), 0); WAITB(t); ROT();
        STAGE_ISSUE(t + 1); att_step<false>(A0, A1, B0, B1, o, negm, l_run, qf, fr, kp0 + sl_cur, vp0 + sl_prev, kp0 + ((sl_cur + 16384) & 65535), 0); WAITB(t + 1); ROT();
    }
    if (t <= tl - 1) {
        STAGE_ISSUE(t); att_step<false>(B0, B1, A0, A1, o, negm, l_run, qf, fr, kp0 + sl_cur, vp0 + sl_prev, kp0 + ((sl_cur + 16384) & 65535), 0); WAITB(t); ROT();
        A0 = B0; A1 = B1; ++t;
    }
    if (tl >= 1) {
        STAGE_ISSUE(t); att_step<true>(B0, B1, A0, A1, o, negm, l_run, qf, fr, kp0 + sl_cur, vp0 + sl_prev, kp0 + ((sl_cur + 16384) & 65535), qrow - t * 64 - 4 * hi); WAITB(t); ROT();
        ++t;
    } else { B0 = A0; B1 = A1; }
    if (t < NT) STAGE_ISSUE(t);
    {
        float sacc = 0.f;
#pragma unroll
        for (int r = 0; r < 16; ++r) { B1[r] = __builtin_amdgcn_exp2f(B1[r]); sacc += B0[r] + B1[r]; }
        l_run += sacc;
        pf0 = pack8(B0, 0); pf1 = pack8(B0, 1); pf2 = pack8(B1, 0); pf3 = pack8(B1, 1);
        const lds_cptr vp = vp0 + sl_prev;
#pragma unroll
        for (int d = 0; d < 4; ++d) {
            o[d] = MFMA(ldsb128(vp + (d * 4 + 0) * 1024), pf0, o[d]);
            o[d] = MFMA(ldsb128(vp + (d * 4 + 1) * 1024), pf1, o[d]);
            o[d] = MFMA(ldsb128(vp + (d * 4 + 2) * 1024), pf2, o[d]);
            o[d] = MFMA(ldsb128(vp + (d * 4 + 3) * 1024), pf3, o[d]);
        }
    }
    STAMP_AT(ws, 1, 3);
    {
        const float inv = 1.f / half_swap_sum(l_run);
        char* cb = ws + WS_CTX + ((size_t)(rb * 256 + head * 8) * 64 + l) * 16;
#pragma unroll
        for (int d = 0; d < 4; ++d) {
#pragma unroll
            for (int r = 0; r < 16; ++r) o[d][r] *= inv;
            *(bf16x8*)(cb + (2 * d) * 1024) = pack8(o[d], 0);
            *(bf16x8*)(cb + (2 * d + 1) * 1024) = pack8(o[d], 1);
        }
    }
    if (t < NT) {
        WAIT_ALL_BAR(); ROT(); ++t;
        for (; t < NT && t < tsw; ++t) { STAGE_ISSUE(t); WAIT_BAR4(); ROT(); }
        if (w < 4) { for (; t < NT; ++t) { STAGE_HELP(t); asm volatile("s_waitcnt vmcnt(8) lgkmcnt(0)\n\ts_barrier" ::: "memory"); ROT(); } }
        else { for (; t < NT; ++t) { WAIT_ALL_BAR(); ROT(); } }
    }
    asm volatile("s_waitcnt vmcnt(0)" ::: "memory");
    STAMP_AT(ws, 1, 4);
#undef DMA_K
#undef DMA_V
#undef ROT
#undef STAGE_ISSUE
#undef STAGE_HELP
#undef DMA_ARGS
#undef WAITB
}

template <int TQ, int TN, int KW, int BKS, bool APERM, bool CT>
struct G {
    static constexpr int NWAVE = TQ * TN * KW, RB = TN * 128, A_BYTES = 2 * TQ * BKS * 1024, W_BYTES = 16 * BKS * RB, STAGE = A_BYTES + W_BYTES;
    static constexpr int CPR = RB / 16;
    static_assert(STAGE == 32768 && (2 * TQ * BKS + W_BYTES / 1024) / NWAVE == 4, "4 glds per wave per stage, 4 x 32 KB ring");
    static DI int swz(int row) { return RB == 256 ? ((row & 3) << 2) : (((row >> 1) & 1) << 2); }
    static DI void stage(const char* AF, size_t a_rb_stride, const u16* Wp, int ldw, int kst, unsigned ldsaddr, int w, int l) {
#pragma unroll
        for (int p0 = 0; p0 < 2 * TQ * BKS; p0 += NWAVE) {
            const int p = p0 + w, rbi = p / BKS, ks = p % BKS;
            glds16(AF + rbi * a_rb_stride + (size_t)(kst * BKS + ks) * 1024 + l * 16, ldsaddr + p * 1024);
        }
#pragma unroll
        for (int p0 = 0; p0 < W_BYTES / 1024; p0 += NWAVE) {
            const int p = p0 + w, row = p * (1024 / RB) + l / CPR, c = (l % CPR) ^ swz(row);
            glds16(Wp + (size_t)(kst * BKS * 16 + row) * ldw + c * 8, ldsaddr + A_BYTES + p * 1024);
        }
    }
    static constexpr int NPA = 2 * TQ * BKS / NWAVE, NPW = W_BYTES / 1024 / NWAVE;
    static DI void stage_piece(int i, const char* AF, size_t a_rb_stride, const u16* Wp, int ldw, int kst, unsigned ldsaddr, int w, int l) {
        if (i < NPA) {
            const int p = i * NWAVE + w, rbi = p / BKS, ks = p % BKS;
            glds16(AF + rbi * a_rb_stride + (size_t)(kst * BKS + ks) * 1024 + l * 16, ldsaddr + p * 1024);
        } else {
            const int p = (i - NPA) * NWAVE + w, row = p * (1024 / RB) + l / CPR, c = (l % CPR) ^ swz(row);
            glds16(Wp + (size_t)(kst * BKS * 16 + row) * ldw + c * 8, ldsaddr + A_BYTES + p * 1024);
        }
    }
    template <bool RM, class EPI>
    static DI void run(EPI epi, const char* AF, size_t a_rb_stride, const u16* Wp, int ldw, int nstage, int rot, char* lds, float (&red)[64 / KW], int& tq, int& tn, int& kw, char* ws, int kid) {
        const int tid = threadIdx.x, l = tid & 63, hi = l >> 5;
        STAMP_AT(ws, kid, 0);
        const int w = __builtin_amdgcn_readfirstlane(tid >> 6);
        kw = w % KW; const int tt = w / KW; tq = tt / TN; tn = tt % TN;
        const unsigned lds0 = (unsigned)(uintptr_t)lds;
        const int q4 = (l & 15) >> 2, p4 = l & 3, blk = (l >> 4) & 1;
        f32x16 acc[2][2];
#pragma unroll
        for (int i = 0; i < 2; ++i) { acc[i][0] = f32x16{}; acc[i][1] = f32x16{}; }
        int kst = rot;
#define NEXTK() do { kst = (kst + 1 == nstage) ? 0 : kst + 1; } while (0)
        stage(AF, a_rb_stride, Wp, ldw, kst, lds0, w, l); NEXTK();
        stage(AF, a_rb_stride, Wp, ldw, kst, lds0 + STAGE, w, l); NEXTK();
        stage(AF, a_rb_stride, Wp, ldw, kst, lds0 + 2 * STAGE, w, l); NEXTK();
        for (int st = 0; st < nstage; ++st) {
            if (st + 2 < nstage) asm volatile("s_waitcnt vmcnt(8) lgkmcnt(0)\n\ts_barrier" ::: "memory");
            else if (st + 1 < nstage) asm volatile("s_waitcnt vmcnt(4) lgkmcnt(0)\n\ts_barrier" ::: "memory");
            else WAIT_ALL_BAR();
            if (st == 0) STAMP_AT(ws, kid, 1);
            const bool pre = st + 3 < nstage; const unsigned pdst = lds0 + ((st + 3) & 3) * STAGE;
            lds_cptr ab = (lds_cptr)lds + (st & 3) * STAGE;
            lds_cptr wb = ab + A_BYTES;
#pragma unroll
            for (int kk = 0; kk < BKS / KW; ++kk) {
                const int ks = kw * (BKS / KW) + kk;
                bf16x8 af[2], wf[2];
#pragma unroll
                for (int i = 0; i < 2; ++i) af[i] = ldsb128(ab + ((2 * tq + i) * BKS + ks) * 1024 + l * 16);
#pragma unroll
                for (int j = 0; j < 2; ++j) {
                    const int r0 = 16 * ks + (APERM ? 4 * hi : 8 * hi) + q4, r1 = r0 + (APERM ? 8 : 4);
                    const int ch = 4 * (2 * tn + j) + 2 * blk + (p4 >> 1);
                    wf[j] = wfrag(wb + r0 * RB + ((ch ^ swz(r0)) << 4) + 8 * (p4 & 1), wb + r1 * RB + ((ch ^ swz(r1)) << 4) + 8 * (p4 & 1));
                }
#pragma unroll
                for (int i = 0; i < 2; ++i)
#pragma unroll
                    for (int j = 0; j < 2; ++j) {
                        if (CT) acc[i][j] = MFMA(wf[j], af[i], acc[i][j]);
                        else acc[i][j] = MFMA(af[i], wf[j], acc[i][j]);
                        constexpr int PER = 4 / (BKS / KW);
                        const int m = i * 2 + j;
                        if (PER == 4) { SBAR(); if (pre) stage_piece(m, AF, a_rb_stride, Wp, ldw, kst, pdst, w, l); SBAR(); }
                        else if (m & 1) { SBAR(); if (pre) stage_piece(kk * 2 + (m >> 1), AF, a_rb_stride, Wp, ldw, kst, pdst, w, l); SBAR(); }
                    }
            }
            if (pre) NEXTK();
        }
        BAR_LGKM();
        STAMP_AT(ws, kid, 2);
        float* rb_ = (float*)lds;
        constexpr int RS = RM ? 68 : 64;
#pragma unroll
        for (int i = 0; i < 2; ++i)
#pragma unroll
            for (int j = 0; j < 2; ++j)
#pragma unroll
                for (int r = 0; r < 16; ++r) rb_[(w * 64 + (i * 2 + j) * 16 + r) * RS + l] = acc[i][j][r];
        BAR_LGKM();
        if constexpr (RM) {
            constexpr int C8 = TN * 8, NITEM = 64 * TQ * C8 / (64 * NWAVE);
#pragma unroll
            for (int it = 0; it < NITEM; ++it) {
                const int id = it * (64 * NWAVE) + tid, row = id / C8, c8 = id % C8;
                const int rr = row & 31, r = (rr & 3) + 4 * (rr >> 3), h = (rr >> 2) & 1;
                const int t2 = (row >> 6) * TN + (c8 >> 3), reg = (((row >> 5) & 1) * 2 + ((c8 >> 2) & 1)) * 16 + r, ln = (c8 & 3) * 8 + 32 * h;
                f32x4 a0 = {0.f, 0.f, 0.f, 0.f}, a1 = a0;
#pragma unroll
                for (int k2 = 0; k2 < KW; ++k2) {
                    const float* q_ = rb_ + ((t2 * KW + k2) * 64 + reg) * RS + ln;
                    a0 += *(const f32x4*)q_; a1 += *(const f32x4*)(q_ + 4);
                }
                epi(row, c8 * 8, a0, a1);
            }
        } else {
#pragma unroll
            for (int e = 0; e < 64 / KW; ++e) {
                float s = 0.f;
#pragma unroll
                for (int k2 = 0; k2 < KW; ++k2) s += rb_[((tt * KW + k2) * 64 + kw * (64 / KW) + e) * RS + l];
                red[e] = s;
            }
        }
        STAMP_AT(ws, kid, 3);
    }
};

typedef G<2, 2, 2, 4, true, false> G3;
constexpr int K3_LDS = 139264;
__global__ __launch_bounds__(512, 2) void k3_oproj(const u16* __restrict__ wo, char* __restrict__ ws) {
    extern __shared__ __attribute__((aligned(16))) char lds[];
    const int b = blockIdx.x, xcd = b & 7, r = b >> 3, ksl = xcd & 3, rp = (xcd >> 2) * 8 + (r >> 2), ct = r & 3;
    float red[32]; int tq, tn, kw;
    u16* part = (u16*)(ws + WS_PART) + (size_t)ksl * 2048 * 512 + (size_t)(rp * 128) * 512 + ct * 128;
    G3::run<true>([&](int row, int col, f32x4 a0, f32x4 a1) {
                      u32x4 v = {pk2(a0[0], a0[1]), pk2(a0[2], a0[3]), pk2(a1[0], a1[1]), pk2(a1[2], a1[3])};
                      *(u32x4*)(part + (size_t)row * 512 + col) = v; },
                  ws + WS_CTX + ((size_t)(rp * 4) * 256 + ksl * 64) * 1024, (size_t)256 * 1024, wo + (size_t)ksl * 1024 * 512 + ct * 128, 512, 16, (4 * ct + 2 * (r >> 2)) & 15, lds, red, tq, tn, kw, ws, 2);
    STAMP_AT(ws, 2, 4);
}

__global__ __launch_bounds__(512) void k3b_ln2(const u16* __restrict__ x, char* __restrict__ ws) {
    const int l = threadIdx.x & 63, row = blockIdx.x * 8 + (threadIdx.x >> 6);
    const u16* part = (const u16*)(ws + WS_PART) + (size_t)row * 512 + 8 * l;
    const float* mod = (const float*)(ws + WS_MOD);
    u32x4 pv[4];
#pragma unroll
    for (int s = 0; s < 4; ++s) pv[s] = *(const u32x4*)(part + (size_t)s * 2048 * 512);
    const u32x4 xv4 = *(const u32x4*)(x + (size_t)row * 512 + 8 * l);
    const f32x4 g0 = *(const f32x4*)(mod + 1024 + 8 * l), g1 = *(const f32x4*)(mod + 1024 + 8 * l + 4);
    const f32x4 c0 = *(const f32x4*)(mod + 2048 + 8 * l), c1 = *(const f32x4*)(mod + 2048 + 8 * l + 4);
    const f32x4 s0 = *(const f32x4*)(mod + 1536 + 8 * l), s1 = *(const f32x4*)(mod + 1536 + 8 * l + 4);
    float x1[8], xh[8];
#pragma unroll
    for (int j = 0; j < 4; ++j) {
        const int w0 = j >> 1, w1 = 2 + (j >> 1);
        const float a0 = (j & 1) ? (bfhi(pv[0][w0]) + bfhi(pv[1][w0])) + (bfhi(pv[2][w0]) + bfhi(pv[3][w0])) : (bflo(pv[0][w0]) + bflo(pv[1][w0])) + (bflo(pv[2][w0]) + bflo(pv[3][w0]));
        const float a1 = (j & 1) ? (bfhi(pv[0][w1]) + bfhi(pv[1][w1])) + (bfhi(pv[2][w1]) + bfhi(pv[3][w1])) : (bflo(pv[0][w1]) + bflo(pv[1][w1])) + (bflo(pv[2][w1]) + bflo(pv[3][w1]));
        const float xa = (j & 1) ? bfhi(xv4[j >> 1]) : bflo(xv4[j >> 1]), xb = (j & 1) ? bfhi(xv4[2 + (j >> 1)]) : bflo(xv4[2 + (j >> 1)]);
        x1[j] = bfr(xa + bfr(g0[j] * bfr(a0)));
        x1[4 + j] = bfr(xb + bfr(g1[j] * bfr(a1)));
    }
    u32x4 o1 = {pk2(x1[0], x1[1]), pk2(x1[2], x1[3]), pk2(x1[4], x1[5]), pk2(x1[6], x1[7])};
    *(u32x4*)(ws + WS_X1 + ((size_t)row * 512 + 8 * l) * 2) = o1;
    ln_row(x1, xh);
    float h[8];
#pragma unroll
    for (int j = 0; j < 4; ++j) { h[j] = bfr(xh[j] * bfr(1.f + c0[j])) + s0[j]; h[4 + j] = bfr(xh[4 + j] * bfr(1.f + c1[j])) + s1[j]; }
    u32x4 oh = {pk2(h[0], h[1]), pk2(h[2], h[3]), pk2(h[4], h[5]), pk2(h[6], h[7])};
    *(u32x4*)(ws + WS_H2 + ((size_t)((row >> 5) * 32 + (l >> 1)) * 64 + (row & 31) + 32 * (l & 1)) * 16) = oh;
}

typedef G<2, 2, 2, 4, false, true> G4;
__global__ __launch_bounds__(512, 2) void k4_ffn1(const u16* __restrict__ w1, char* __restrict__ ws) {
    extern __shared__ __attribute__((aligned(16))) char lds[];
    const int b = blockIdx.x, xcd = b & 7, jj = b >> 3, rp = 4 * (xcd >> 1) + jj / 7, ct = 7 * (xcd & 1) + jj % 7;
    const int l = threadIdx.x & 63;
    float red[32]; int tq, tn, kw;
    G4::run<false>(0, ws + WS_H2 + (size_t)(rp * 4) * 32 * 1024, (size_t)32 * 1024, w1 + ct * 128, DFF, 8, (jj % 7 + 2 * (jj / 7)) & 7, lds, red, tq, tn, kw, ws, 3);
#pragma unroll
    for (int j = 0; j < 2; ++j)
#pragma unroll
        for (int s = 0; s < 2; ++s) {
            const int e = j * 16 + 8 * s;
            u32x4 v = {pk2(fmaxf(red[e], 0.f), fmaxf(red[e + 1], 0.f)), pk2(fmaxf(red[e + 2], 0.f), fmaxf(red[e + 3], 0.f)),
                       pk2(fmaxf(red[e + 4], 0.f), fmaxf(red[e + 5], 0.f)), pk2(fmaxf(red[e + 6], 0.f), fmaxf(red[e + 7], 0.f))};
            const int rbq = rp * 4 + 2 * tq + kw, kstep = (ct * 128 + 64 * tn + 32 * j) / 16 + s;
            *(u32x4*)(ws + WS_H1 + ((size_t)(rbq * 112 + kstep) * 64 + l) * 16) = v;
        }
    STAMP_AT(ws, 3, 4);
}

typedef G<1, 1, 8, 8, true, false> G5;
__global__ __launch_bounds__(512, 2) void k5_ffn2(const u16* __restrict__ w2, char* __restrict__ ws, u16* __restrict__ out) {
    extern __shared__ __attribute__((aligned(16))) char lds[];
    const int b = blockIdx.x, xcd = b & 7, jj = b >> 3, rp = 8 * (xcd >> 1) + (jj >> 2), ct = 4 * (xcd & 1) + (jj & 3);
    float red[8]; int tq, tn, kw;
    const float* gb = (const float*)(ws + WS_MOD) + 2560 + ct * 64;
    const u16* x1 = (const u16*)(ws + WS_X1) + (size_t)(rp * 64) * 512 + ct * 64;
    u16* op = out + (size_t)(rp * 64) * 512 + ct * 64;
    G5::run<true>([&](int row, int col, f32x4 a0, f32x4 a1) {
                      const u32x4 xv = *(const u32x4*)(x1 + (size_t)row * 512 + col);
                      const f32x4 g0 = *(const f32x4*)(gb + col), g1 = *(const f32x4*)(gb + col + 4);
                      u32x4 v = {pk2(bflo(xv[0]) + bfr(g0[0] * bfr(a0[0])), bfhi(xv[0]) + bfr(g0[1] * bfr(a0[1]))),
                                 pk2(bflo(xv[1]) + bfr(g0[2] * bfr(a0[2])), bfhi(xv[1]) + bfr(g0[3] * bfr(a0[3]))),
                                 pk2(bflo(xv[2]) + bfr(g1[0] * bfr(a1[0])), bfhi(xv[2]) + bfr(g1[1] * bfr(a1[1]))),
                                 pk2(bflo(xv[3]) + bfr(g1[2] * bfr(a1[2])), bfhi(xv[3]) + bfr(g1[3] * bfr(a1[3])))};
                      *(u32x4*)(op + (size_t)row * 512 + col) = v; },
                  ws + WS_H1 + (size_t)(rp * 2) * 112 * 1024, (size_t)112 * 1024, w2 + ct * 64, 512, 14, (3 * (jj & 3) + 2 * (jj >> 2)) % 14, lds, red, tq, tn, kw, ws, 4);
    STAMP_AT(ws, 4, 4);
}

extern "C" void kernel_launch(void* const* d_in, const int* in_sizes, int n_in, void* d_out, int out_size, void* d_ws, size_t ws_size,
                              hipStream_t stream) {
    (void)in_sizes; (void)n_in; (void)out_size; (void)ws_size;
    const u16* x = (const u16*)d_in[0];
    const u16* ckv = (const u16*)d_in[1];
    const u16* wq = (const u16*)d_in[2];
    const u16* wuk = (const u16*)d_in[3];
    const u16* wuv = (const u16*)d_in[4];
    const u16* wo = (const u16*)d_in[5];
    const u16* w1 = (const u16*)d_in[7];
    const u16* w2 = (const u16*)d_in[8];
    const u16* cvec = (const u16*)d_in[9];
    const u16* wmod = (const u16*)d_in[10];
    char* ws = (char*)d_ws;
    hipFuncSetAttribute((const void*)k1_kvproj, hipFuncAttributeMaxDynamicSharedMemorySize, K1_LDS);
    hipFuncSetAttribute((const void*)k2_attn, hipFuncAttributeMaxDynamicSharedMemorySize, K2_LDS);
    hipFuncSetAttribute((const void*)k3_oproj, hipFuncAttributeMaxDynamicSharedMemorySize, K3_LDS);
    hipFuncSetAttribute((const void*)k4_ffn1, hipFuncAttributeMaxDynamicSharedMemorySize, K3_LDS);
    hipFuncSetAttribute((const void*)k5_ffn2, hipFuncAttributeMaxDynamicSharedMemorySize, K3_LDS);
    k1_kvproj<<<256, 512, K1_LDS, stream>>>(ckv, wuk, wuv, x, cvec, wmod, ws);
    k2_attn<<<256, 512, K2_LDS, stream>>>(wq, ws);
    k3_oproj<<<256, 512, K3_LDS, stream>>>(wo, ws);
    k3b_ln2<<<256, 512, 0, stream>>>(x, ws);
    k4_ffn1<<<224, 512, K3_LDS, stream>>>(w1, ws);
    k5_ffn2<<<256, 512, K3_LDS, stream>>>(w2, ws, (u16*)d_out);
}
```

```cpp
#include <hip/hip_runtime.h>
#include <hip/hip_bf16.h>
#include <stdint.h>

typedef unsigned short u16;
typedef short bf16x8 __attribute__((ext_vector_type(8)));
typedef short s16x4 __attribute__((ext_vector_type(4)));
typedef short v4i16_t __attribute__((ext_vector_type(4)));
typedef float f32x16 __attribute__((ext_vector_type(16)));
typedef float f32x4 __attribute__((ext_vector_type(4)));
typedef float f32x2 __attribute__((ext_vector_type(2)));
typedef unsigned u32x4 __attribute__((ext_vector_type(4)));
typedef __bf16 bf16x2_t __attribute__((ext_vector_type(2)));
typedef __attribute__((address_space(3))) const char* lds_cptr;
#define DI __device__ __forceinline__
#define MFMA(a, b, c) __builtin_amdgcn_mfma_f32_32x32x16_bf16((a), (b), (c), 0, 0, 0)
#define SBAR() __builtin_amdgcn_sched_barrier(0)
#define STAMPX(ws_, idx) do { } while (0)
#define STAMP_AT(ws_, kid, idx) do { } while (0)

constexpr int SEQ = 2048, DM = 512, NH = 32, HD = 128, DFF = 1792;

constexpr size_t WS_KV = 0;
constexpr size_t WS_XH = WS_KV + (size_t)32 * 32 * 32768;
constexpr size_t WS_MOD = WS_XH + (size_t)64 * 32 * 1024;
constexpr size_t WS_GS = WS_MOD + 12288;
constexpr size_t WS_CTX = WS_MOD + 16384;
constexpr size_t WS_PART = WS_CTX + (size_t)64 * 256 * 1024;
constexpr size_t WS_X1 = WS_PART + (size_t)4 * 2048 * 512 * 4;
constexpr size_t WS_H2 = WS_X1 + (size_t)2048 * 512 * 2;
constexpr size_t WS_H1 = WS_H2 + (size_t)64 * 32 * 1024;

DI unsigned pk2(float a, float b) { f32x2 v = {a, b}; bf16x2_t r = __builtin_convertvector(v, bf16x2_t); return __builtin_bit_cast(unsigned, r); }
DI float bf2f(u16 v) { return __uint_as_float(((unsigned)v) << 16); }
DI float bfr(float x) { return __uint_as_float(pk2(x, 0.f) << 16); }
DI float bflo(unsigned w) { return __uint_as_float(w << 16); }
DI float bfhi(unsigned w) { return __uint_as_float(w & 0xffff0000u); }
DI bf16x8 pack8(const f32x16& a, int s) {
    u32x4 w = {pk2(a[8 * s], a[8 * s + 1]), pk2(a[8 * s + 2], a[8 * s + 3]), pk2(a[8 * s + 4], a[8 * s + 5]), pk2(a[8 * s + 6], a[8 * s + 7])};
    return __builtin_bit_cast(bf16x8, w);
}
DI int crow(int r, int hi) { return (r & 3) + 8 * (r >> 2) + 4 * hi; }
DI void glds16(const void* g, unsigned lds_base) {
    unsigned sv;
    asm volatile("s_mov_b32 %0, m0\n\ts_mov_b32 m0, %2\n\ts_nop 0\n\tglobal_load_lds_dwordx4 %1, off\n\ts_mov_b32 m0, %0"
                 : "=&s"(sv) : "v"(g), "s"(lds_base) : "memory");
}
DI bf16x8 ldsb128(lds_cptr p) { return *(const __attribute__((address_space(3))) bf16x8*)p; }
DI s16x4 ldstr(lds_cptr p) { return __builtin_bit_cast(s16x4, __builtin_amdgcn_ds_read_tr16_b64_v4i16((__attribute__((address_space(3))) v4i16_t*)p)); }
DI bf16x8 wfrag(lds_cptr p0, lds_cptr p1) { s16x4 lo = ldstr(p0), hi = ldstr(p1); return __builtin_shufflevector(lo, hi, 0, 1, 2, 3, 4, 5, 6, 7); }
#define WAIT_ALL_BAR() asm volatile("s_waitcnt vmcnt(0) lgkmcnt(0)\n\ts_barrier" ::: "memory")
#define BAR_LGKM() asm volatile("s_waitcnt lgkmcnt(0)\n\ts_barrier" ::: "memory")
DI float wave_sum(float v) {
#pragma unroll
    for (int m = 1; m < 64; m <<= 1) v += __shfl_xor(v, m);
    return v;
}
DI float half_swap_max(float m) { auto rr = __builtin_amdgcn_permlane32_swap(__float_as_uint(m), __float_as_uint(m), false, false); return fmaxf(__uint_as_float(rr[0]), __uint_as_float(rr[1])); }
DI float half_swap_sum(float m) { auto rr = __builtin_amdgcn_permlane32_swap(__float_as_uint(m), __float_as_uint(m), false, false); return __uint_as_float(rr[0]) + __uint_as_float(rr[1]); }

DI void ln_row(const float (&xv)[8], float (&o)[8]) {
    float s = 0.f;
#pragma unroll
    for (int j = 0; j < 8; ++j) s += xv[j];
    const float mean = wave_sum(s) * (1.f / 512.f);
    float q = 0.f;
#pragma unroll
    for (int j = 0; j < 8; ++j) { const float d = xv[j] - mean; q += d * d; }
    const float var = wave_sum(q) * (1.f / 512.f);
    const float mb = bfr(mean), vb = bfr(var);
    const float den = bfr(sqrtf(bfr(vb + 1e-5f)));
#pragma unroll
    for (int j = 0; j < 8; ++j) o[j] = bfr(bfr(xv[j] - mb) / den);
}

constexpr int K1_LDS = 147456;
template <bool VT>
DI void k1_gemm(const u16* __restrict__ ckv, const u16* __restrict__ W, char* __restrict__ kvimg, char* lds, int key0, int h0, int h1, int rot,
                 const u16* __restrict__ x, const u16* __restrict__ cvec, const u16* __restrict__ wmod, char* __restrict__ ws) {
    const int tid = threadIdx.x, l = tid & 63, r32 = l & 31, hi = l >> 5;
    const int w = __builtin_amdgcn_readfirstlane(tid >> 6);
    const int wk = w >> 1, wc = w & 1;
    const unsigned lds0 = (unsigned)(uintptr_t)lds;
    constexpr int SLOT = 49152;
    const u16* asrc[4]; const u16* wsrc[2];
#pragma unroll
    for (int n = 0; n < 4; ++n) {
        const int P = n * 8 + w, row = 8 * P + (l >> 3), c = (l & 7) ^ ((row >> 1) & 7);
        asrc[n] = ckv + (size_t)(key0 + row) * 512 + c * 8;
    }
#pragma unroll
    for (int n = 0; n < 2; ++n) {
        const int P = n * 8 + w, rw = 4 * P + (l >> 4), cw = (l & 15) ^ ((rw & 3) << 2);
        wsrc[n] = W + (size_t)rw * 4096 + cw * 8;
    }
#define K1_PIECE(i, g_, bb_) do { const int kt_ = (rot + (g_)) & 7; if ((i) < 4) glds16(asrc[i] + kt_ * 64, (bb_) + ((i) * 8 + w) * 1024); \
        else glds16(wsrc[(i) - 4] + (size_t)kt_ * 64 * 4096 + (((g_) >> 3) ? h1 : h0) * 128, (bb_) + 32768 + (((i) - 4) * 8 + w) * 1024); } while (0)
#pragma unroll
    for (int i = 0; i < 6; ++i) K1_PIECE(i, 0, lds0);
#pragma unroll
    for (int i = 0; i < 6; ++i) K1_PIECE(i, 1, lds0 + SLOT);
    {
        const int b = blockIdx.x, row = 8 * b + w, half = tid & 1, rowi = tid >> 1, bm = b < 192 ? b : 0;
        const u32x4 xv4 = *(const u32x4*)(x + (size_t)row * 512 + 8 * l);
        const u32x4 wv0 = *(const u32x4*)(wmod + (size_t)rowi * 3072 + 16 * bm + 8 * half), wv1 = *(const u32x4*)(wmod + (size_t)(rowi + 256) * 3072 + 16 * bm + 8 * half);
        const float c0 = bf2f(cvec[rowi]), c1 = bf2f(cvec[rowi + 256]);
        float* scr = (float*)(lds + 2 * SLOT);
        {
            float xv[8], o[8];
#pragma unroll
            for (int j = 0; j < 4; ++j) { xv[2 * j] = bflo(xv4[j]); xv[2 * j + 1] = bfhi(xv4[j]); }
            ln_row(xv, o);
            u32x4 ov = {pk2(o[0], o[1]), pk2(o[2], o[3]), pk2(o[4], o[5]), pk2(o[6], o[7])};
            *(u32x4*)(ws + WS_XH + ((size_t)((row >> 5) * 32 + (l >> 1)) * 64 + (row & 31) + 32 * (l & 1)) * 16) = ov;
        }
        if (b < 192) {
            float a8[8];
            const float sc0 = bfr(c0 / (1.f + __expf(-c0))), sc1 = bfr(c1 / (1.f + __expf(-c1)));
#pragma unroll
            for (int j = 0; j < 4; ++j) { a8[2 * j] = sc0 * bflo(wv0[j]) + sc1 * bflo(wv1[j]); a8[2 * j + 1] = sc0 * bfhi(wv0[j]) + sc1 * bfhi(wv1[j]); }
#pragma unroll
            for (int m = 2; m < 64; m <<= 1)
#pragma unroll
                for (int j = 0; j < 8; ++j) a8[j] += __shfl_xor(a8[j], m);
            if (l < 2) {
#pragma unroll
                for (int j = 0; j < 8; ++j) scr[(w * 2 + l) * 8 + j] = a8[j];
            }
            BAR_LGKM();
            if (tid < 16) {
                float s = 0.f;
#pragma unroll
                for (int ww = 0; ww < 8; ++ww) s += scr[(ww * 2 + (tid >> 3)) * 8 + (tid & 7)];
                const int col = 16 * b + tid; const float mv = bfr(s);
                ((float*)(ws + WS_MOD))[col] = mv;
                if (col < 512) ((float*)(ws + WS_GS))[512 + col] = mv;
                else if (col < 1024) ((float*)(ws + WS_GS))[col - 512] = bfr(1.f + mv);
            }
        }
    }
    f32x16 acc[2][2];
#pragma unroll
    for (int a = 0; a < 2; ++a) { acc[a][0] = f32x16{}; acc[a][1] = f32x16{}; }
    const int q4 = (l & 15) >> 2, p4 = l & 3, blk = (l >> 4) & 1;
    const int xa = (l >> 1) & 7;
    int aoff[4], woff[2];
#pragma unroll
    for (int s = 0; s < 4; ++s) aoff[s] = (64 * wk + r32) * 128 + (((2 * s + hi) ^ xa) << 4);
#pragma unroll
    for (int j = 0; j < 2; ++j) woff[j] = 32768 + (8 * hi + q4) * 256 + 64 * ((2 * wc + j) ^ q4) + 32 * blk + 16 * (p4 >> 1) + 8 * (p4 & 1);
    STAMP_AT(kvimg, 0, 1);
    int sl = 0;
    for (int g = 0; g < 16; ++g) {
        if (g == 0 || g == 15) WAIT_ALL_BAR();
        else if (g == 8 || g == 9) asm volatile("s_waitcnt vmcnt(14) lgkmcnt(0)\n\ts_barrier" ::: "memory");
        else asm volatile("s_waitcnt vmcnt(6) lgkmcnt(0)\n\ts_barrier" ::: "memory");
        if (g == 0) STAMP_AT(kvimg, 0, 2);
        const bool pre = g + 2 < 16;
        const int sln = sl == 0 ? 2 * SLOT : sl - SLOT;
        const unsigned bb = lds0 + sln;
        lds_cptr sb = (lds_cptr)lds + sl;
        bf16x8 af[2][2], wf[2][2];
        af[0][0] = ldsb128(sb + aoff[0]); af[0][1] = ldsb128(sb + aoff[0] + 4096);
        wf[0][0] = wfrag(sb + woff[0], sb + woff[0] + 1024); wf[0][1] = wfrag(sb + woff[1], sb + woff[1] + 1024);
#pragma unroll
        for (int s = 0; s < 4; ++s) {
            if (s < 3) {
                af[(s + 1) & 1][0] = ldsb128(sb + aoff[s + 1]); af[(s + 1) & 1][1] = ldsb128(sb + aoff[s + 1] + 4096);
                wf[(s + 1) & 1][0] = wfrag(sb + woff[0] + (s + 1) * 4096, sb + woff[0] + (s + 1) * 4096 + 1024);
                wf[(s + 1) & 1][1] = wfrag(sb + woff[1] + (s + 1) * 4096, sb + woff[1] + (s + 1) * 4096 + 1024);
            }
            SBAR();
#pragma unroll
            for (int kb = 0; kb < 2; ++kb)
#pragma unroll
                for (int j = 0; j < 2; ++j) {
                    if (VT) acc[kb][j] = MFMA(af[s & 1][kb], wf[s & 1][j], acc[kb][j]);
                    else acc[kb][j] = MFMA(wf[s & 1][j], af[s & 1][kb], acc[kb][j]);
                    const int ix = s * 4 + kb * 2 + j;
                    if ((ix & 1) && ix < 12) { SBAR(); if (pre) K1_PIECE(ix >> 1, g + 2, bb); SBAR(); }
                }
        }
        if ((g & 7) == 7) {
            const int head = g >> 3 ? h1 : h0;
#pragma unroll
            for (int kb = 0; kb < 2; ++kb) {
                const int tile = (key0 >> 6) + wk;
                char* tb = kvimg + ((size_t)(head * 32 + tile) << 15);
#pragma unroll
                for (int j = 0; j < 2; ++j) {
                    const int db = 2 * wc + j;
#pragma unroll
                    for (int s2 = 0; s2 < 2; ++s2) {
                        const int fo = VT ? (16384 + (db * 4 + kb * 2 + s2) * 1024) : ((kb * 8 + db * 2 + s2) * 1024);
                        *(bf16x8*)(tb + fo + l * 16) = pack8(acc[kb][j], s2);
                    }
                    acc[kb][j] = f32x16{};
                }
            }
        }
        sl = sl == 2 * SLOT ? 0 : sl + SLOT;
    }
#undef K1_PIECE
    STAMP_AT(kvimg, 0, 3);
}

__global__ __launch_bounds__(512, 2) void k1_kvproj(const u16* __restrict__ ckv, const u16* __restrict__ wuk, const u16* __restrict__ wuv,
                                                    const u16* __restrict__ x, const u16* __restrict__ cvec, const u16* __restrict__ wmod,
                                                    char* __restrict__ ws) {
    extern __shared__ __attribute__((aligned(16))) char lds[];
    const int b = blockIdx.x, tid = threadIdx.x, l = tid & 63;
    const int w = __builtin_amdgcn_readfirstlane(tid >> 6);
    const int xcd = b & 7, r = b >> 3, kb8 = r & 7, combo = r >> 3, type = combo & 1, hp = combo >> 1;
    const int h0 = xcd + 16 * hp, h1 = h0 + 8, key0 = kb8 * 256;
    const u16* W = type ? wuv : wuk;
    STAMP_AT(ws, 0, 0);
    const int rot = (kb8 + 2 * combo) & 7;
    if (type) k1_gemm<true>(ckv, W, ws + WS_KV, lds, key0, h0, h1, rot, x, cvec, wmod, ws);
    else k1_gemm<false>(ckv, W, ws + WS_KV, lds, key0, h0, h1, rot, x, cvec, wmod, ws);
    STAMP_AT(ws, 0, 4);
}

constexpr float ATT_THR = 8.f;
#define PIN(x) asm volatile("" : "+v"(x))
#define MX3(a, b, c) __builtin_fmaxf(__builtin_fmaxf((a), (b)), (c))
DI float att_rowmax(const f32x16& p0, const f32x16& p1) {
    float a = MX3(p0[0], p0[1], p1[0]), b = MX3(p0[2], p0[3], p1[1]); a = MX3(a, p1[2], p1[3]);
#pragma unroll
    for (int r = 4; r < 16; r += 4) { a = MX3(a, p0[r], p0[r + 1]); b = MX3(b, p0[r + 2], p0[r + 3]); a = MX3(a, p1[r], p1[r + 1]); b = MX3(b, p1[r + 2], p1[r + 3]); }
    return __builtin_fmaxf(a, b);
}
DI void att_mask(f32x16& s0, f32x16& s1, int dq) {
#pragma unroll
    for (int r = 0; r < 16; ++r) {
        const int c = (r & 3) + 8 * (r >> 2);
        if (c > dq) s0[r] = -INFINITY;
        if (c + 32 > dq) s1[r] = -INFINITY;
    }
}
constexpr int PD = 6;
DI int kfrag_off(int g) { return ((g & 1) * 8 + (g >> 1)) * 1024; }
DI int vfrag_off(int g) { return ((g & 3) * 4 + (g >> 2)) * 1024; }
template <bool MASK>
DI void att_step(f32x16& C0, f32x16& C1, const f32x16& P0, f32x16& P1, f32x16 (&o)[4], f32x16& negm, float& l_run,
                 const bf16x8 (&qf)[8], bf16x8 (&fr)[8], lds_cptr kp, lds_cptr vp, lds_cptr kpn, int dq) {
    unsigned pw[16];
    float sacc = 0.f, sacc2 = 0.f;
    SBAR();
#pragma unroll
    for (int g = 0; g < 16; ++g) {
        if ((g & 1) == 0) C0 = MFMA(fr[g & 7], qf[g >> 1], g < 2 ? negm : C0);
        else C1 = MFMA(fr[g & 7], qf[g >> 1], g < 2 ? negm : C1);
        if (g + PD < 16) fr[(g + PD) & 7] = ldsb128(kp + kfrag_off(g + PD));
        else fr[(g + PD) & 7] = ldsb128(vp + vfrag_off(g + PD - 16));
        P1[g] = __builtin_amdgcn_exp2f(P1[g]); PIN(P1[g]);
        sacc += P0[g]; PIN(sacc);
        if (g & 1) { pw[g >> 1] = pk2(P0[g - 1], P0[g]); PIN(pw[g >> 1]); }
        if (g >= 1) { sacc2 += P1[g - 1]; PIN(sacc2); }
        if (g >= 2 && (g & 1) == 0) { pw[8 + ((g - 2) >> 1)] = pk2(P1[g - 2], P1[g - 1]); PIN(pw[8 + ((g - 2) >> 1)]); }
        SBAR();
    }
    sacc2 += P1[15]; pw[15] = pk2(P1[14], P1[15]);
    l_run += sacc + sacc2;
    bool resc = false; float f = 1.f;
    float ma, mb;
#pragma unroll
    for (int g = 0; g < 16; ++g) {
        { const u32x4 pv = {pw[4 * (g >> 2)], pw[4 * (g >> 2) + 1], pw[4 * (g >> 2) + 2], pw[4 * (g >> 2) + 3]};
          o[g & 3] = MFMA(fr[g & 7], __builtin_bit_cast(bf16x8, pv), o[g & 3]); }
        if (g + PD < 16) fr[(g + PD) & 7] = ldsb128(vp + vfrag_off(g + PD));
        else fr[(g + PD) & 7] = ldsb128(kpn + kfrag_off(g + PD - 16));
        if (g == 1 && MASK) att_mask(C0, C1, dq);
        if (g == 1) { ma = MX3(C0[0], C0[1], C0[2]); mb = MX3(C0[3], C0[4], C0[5]); ma = MX3(ma, C0[6], C0[7]); mb = MX3(mb, C0[8], C0[9]); PIN(ma); PIN(mb); }
        if (g == 2) { ma = MX3(ma, C0[10], C0[11]); mb = MX3(mb, C0[12], C0[13]); ma = MX3(ma, C0[14], C0[15]); mb = MX3(mb, C1[0], C1[1]); PIN(ma); PIN(mb); }
        if (g == 3) { ma = MX3(ma, C1[2], C1[3]); mb = MX3(mb, C1[4], C1[5]); ma = MX3(ma, C1[6], C1[7]); mb = MX3(mb, C1[8], C1[9]); PIN(ma); PIN(mb); }
        if (g == 4) {
            ma = MX3(ma, C1[10], C1[11]); mb = MX3(mb, C1[12], C1[13]); ma = MX3(ma, C1[14], C1[15]);
            const float rm = __builtin_fmaxf(ma, mb);
            if (__builtin_expect(__any(rm > ATT_THR), 0)) {
                const float dl = __builtin_fmaxf(half_swap_max(rm), 0.f);
                f = __builtin_amdgcn_exp2f(-dl); l_run *= f; resc = true;
#pragma unroll
                for (int r = 0; r < 16; ++r) { C0[r] -= dl; C1[r] -= dl; negm[r] -= dl; }
            }
        }
        if (g >= 5) {
            const int e0 = g < 10 ? 2 * (g - 5) : 10 + (g - 10), e1 = g < 10 ? e0 + 2 : e0 + 1;
#pragma unroll
            for (int e = e0; e < e1; ++e) { C0[e] = __builtin_amdgcn_exp2f(C0[e]); PIN(C0[e]); }
        }
        SBAR();
    }
    if (__builtin_expect(resc, 0)) {
#pragma unroll
        for (int d = 0; d < 4; ++d)
#pragma unroll
            for (int r = 0; r < 16; ++r) o[d][r] *= f;
    }
}
constexpr int K2_LDS = 147456 + 4096;
constexpr float QSCALE = 0.08838834764831845f * 1.4426950408889634f;
__global__ __launch_bounds__(512, 2) void k2_attn(const u16* __restrict__ wq, char* __restrict__ ws) {
    extern __shared__ __attribute__((aligned(16))) char lds[];
    const int b = blockIdx.x, tid = threadIdx.x, l = tid & 63, r32 = l & 31, hi = l >> 5;
    const int w = __builtin_amdgcn_readfirstlane(tid >> 6);
    const int xcd = b & 7, kk = b >> 3, head = xcd + 8 * (kk >> 3), pi = kk & 7;
    const int qb = (w < 4) ? pi : 15 - pi, sub = w & 3, rb = qb * 4 + sub;
    const int tl = 2 * qb + (sub >> 1), NT = 32 - 2 * pi;
    const unsigned lds0 = (unsigned)(uintptr_t)lds;
    STAMP_AT(ws, 1, 0);
    const char* kvh = ws + WS_KV + ((size_t)head << 20);
    const char* ksrc = kvh + w * 2048 + l * 16;
    const unsigned kdst = lds0 + w * 2048, vdst = lds0 + 65536 + w * 2048;
#define DMA_K(t_, slot_) do { const char* g_ = ksrc + (size_t)(t_) * 32768; glds16(g_, kdst + (slot_)); glds16(g_ + 1024, kdst + (slot_) + 1024); } while (0)
#define DMA_V(t_, slot_) do { const char* g_ = ksrc + (size_t)(t_) * 32768 + 16384; glds16(g_, vdst + (slot_)); glds16(g_ + 1024, vdst + (slot_) + 1024); } while (0)
    DMA_K(0, 0);
    glds16(ws + WS_GS + (w & 3) * 1024 + l * 16, lds0 + 147456 + (w & 3) * 1024);
    u32x4 xf[16];
    const char* xsrc = ws + WS_XH + ((size_t)(rb * 32) * 64 + l) * 16;
#define XISSUE(c) do { _Pragma("unroll") for (int k8 = 0; k8 < 8; ++k8) asm volatile("global_load_dwordx4 %0, %1, off" : "=v"(xf[8 * ((c) & 1) + k8]) : "v"(xsrc + (8 * (c) + k8) * 1024) : "memory"); } while (0)
#define WISSUE(c) do { _Pragma("unroll") for (int n = 0; n < 4; ++n) { const int P = (4 * (c) + n) * 8 + w, row = 4 * P + (l >> 4), cc = (l & 15) ^ ((row & 3) << 2); \
            glds16(wq + (size_t)row * 4096 + head * 128 + cc * 8, lds0 + 16384 + P * 1024); } } while (0)
    XISSUE(0); WISSUE(0); XISSUE(1); WISSUE(1);
    const int q4 = (l & 15) >> 2, p4 = l & 3, blk = (l >> 4) & 1;
    const lds_cptr gsp = (lds_cptr)lds + 147456 + 32 * hi;
    lds_cptr wp[4];
#pragma unroll
    for (int db = 0; db < 4; ++db) wp[db] = (lds_cptr)lds + 16384 + (8 * hi + q4) * 256 + 64 * (db ^ q4) + 32 * blk + 16 * (p4 >> 1) + 8 * (p4 & 1);
    bf16x8 qf[8];
    {
        f32x16 qa[4];
#pragma unroll
        for (int d = 0; d < 4; ++d) qa[d] = f32x16{};
#define XWAIT(N, c) asm volatile("s_waitcnt vmcnt(" #N ") lgkmcnt(0)\n\ts_barrier" : "+v"(xf[8 * c]), "+v"(xf[8 * c + 1]), "+v"(xf[8 * c + 2]), "+v"(xf[8 * c + 3]), \
                                 "+v"(xf[8 * c + 4]), "+v"(xf[8 * c + 5]), "+v"(xf[8 * c + 6]), "+v"(xf[8 * c + 7]) :: "memory")
#define XONE(c, k8) asm volatile("global_load_dwordx4 %0, %1, off" : "=v"(xf[8 * ((c) & 1) + (k8)]) : "v"(xsrc + (8 * (c) + (k8)) * 1024) : "memory")
#define WONE(c, n) do { const int P = (4 * (c) + (n)) * 8 + w, row = 4 * P + (l >> 4), cc = (l & 15) ^ ((row & 3) << 2); \
            glds16(wq + (size_t)row * 4096 + head * 128 + cc * 8, lds0 + 16384 + P * 1024); } while (0)
#define KVONE(off_g, dst_) glds16(ksrc + (off_g), (dst_))
#define LDF4(p_) (*(const __attribute__((address_space(3))) f32x4*)(p_))
#define HPART(j_, G_, S_, e_) pk2(fmaf(bflo(xw[j_]), G_[e_], S_[e_]), fmaf(bfhi(xw[j_]), G_[e_ + 1], S_[e_ + 1]))
#pragma unroll
        for (int c = 0; c < 4; ++c) {
            STAMPX(ws, 2 * c);
            if (c == 0) { XWAIT(12, 0); STAMP_AT(ws, 1, 1); }
            if (c == 1) { XWAIT(4, 1); }
            if (c == 2) { XWAIT(8, 0); }
            if (c == 3) { XWAIT(2, 1); }
            STAMPX(ws, 2 * c + 1);
            bf16x8 wf[2][4]; f32x4 g0, g1, s0, s1; u32x4 hc;
            {
                const int ks = 8 * c;
                g0 = LDF4(gsp + 64 * ks); g1 = LDF4(gsp + 64 * ks + 16); s0 = LDF4(gsp + 2048 + 64 * ks); s1 = LDF4(gsp + 2048 + 64 * ks + 16);
#pragma unroll
                for (int db = 0; db < 4; ++db) wf[0][db] = wfrag(wp[db] + ks * 4096, wp[db] + ks * 4096 + 1024);
                const u32x4 xw = xf[8 * (c & 1)];
                hc = (u32x4){HPART(0, g0, s0, 0), HPART(1, g0, s0, 2), HPART(2, g1, s1, 0), HPART(3, g1, s1, 2)};
                g0 = LDF4(gsp + 64 * (ks + 1)); g1 = LDF4(gsp + 64 * (ks + 1) + 16); s0 = LDF4(gsp + 2048 + 64 * (ks + 1)); s1 = LDF4(gsp + 2048 + 64 * (ks + 1) + 16);
            }
            SBAR();
#pragma unroll
            for (int k8 = 0; k8 < 8; ++k8) {
                const int ks = 8 * c + k8;
                const bf16x8 hf = __builtin_bit_cast(bf16x8, hc);
                u32x4 hn = hc;
                const u32x4 xw = xf[8 * (c & 1) + (k8 < 7 ? k8 + 1 : k8)];
#pragma unroll
                for (int db = 0; db < 4; ++db) {
                    qa[db] = MFMA(wf[k8 & 1][db], hf, qa[db]);
                    {
                        const int ix = k8 * 4 + db;
                        if (c == 0) { if (ix < 4) WONE(2, ix); }
                        if (c == 1) { if (ix < 8) XONE(2, ix); else if (ix < 12) WONE(3, ix - 8);
                                      else if (ix < 16) KVONE((size_t)(1 + ((ix - 12) >> 1)) * 32768 + ((ix - 12) & 1) * 1024, kdst + 16384 * (1 + ((ix - 12) >> 1)) + ((ix - 12) & 1) * 1024); }
                        if (c == 2) { if (ix < 8) XONE(3, ix); else if (ix < 10) KVONE(16384 + (ix - 8) * 1024, vdst + (ix - 8) * 1024); }
                        if (c == 3) { if (ix < 2) KVONE(32768 + 16384 + ix * 1024, vdst + 16384 + ix * 1024); }
                    }
                    if (k8 < 7) {
                        wf[(k8 + 1) & 1][db] = wfrag(wp[db] + (ks + 1) * 4096, wp[db] + (ks + 1) * 4096 + 1024);
                        if (db == 0) hn[0] = HPART(0, g0, s0, 0);
                        if (db == 1) hn[1] = HPART(1, g0, s0, 2);
                        if (db == 2) hn[2] = HPART(2, g1, s1, 0);
                        if (db == 3) { hn[3] = HPART(3, g1, s1, 2); }
                        PIN(hn);
                        if (db == 1 && k8 < 6) { g0 = LDF4(gsp + 64 * (ks + 2)); s0 = LDF4(gsp + 2048 + 64 * (ks + 2)); }
                        if (db == 3 && k8 < 6) { g1 = LDF4(gsp + 64 * (ks + 2) + 16); s1 = LDF4(gsp + 2048 + 64 * (ks + 2) + 16); }
                    }
                    SBAR();
                }
                hc = hn;
            }
        }
#undef XWAIT
#undef XISSUE
#undef WISSUE
#undef HPART
#undef XONE
#undef WONE
#undef KVONE
#pragma unroll
        for (int db = 0; db < 4; ++db) {
#pragma unroll
            for (int r = 0; r < 16; ++r) qa[db][r] *= QSCALE;
            qf[2 * db] = pack8(qa[db], 0); qf[2 * db + 1] = pack8(qa[db], 1);
        }
    }
    STAMP_AT(ws, 1, 2); STAMPX(ws, 8);
#define WAIT_BAR4() asm volatile("s_waitcnt vmcnt(4)\n\ts_barrier" ::: "memory")
#define WAITB(t_) do { if ((t_) < tsw) WAIT_BAR4(); else asm volatile("s_waitcnt vmcnt(0)\n\ts_barrier" ::: "memory"); } while (0)
    float l_run = 0.f;
    f32x16 o[4], negm;
#pragma unroll
    for (int d = 0; d < 4; ++d) o[d] = f32x16{};
    const int qrow = rb * 32 + r32;
    const lds_cptr kp0 = (lds_cptr)lds + l * 16, vp0 = (lds_cptr)lds + 65536 + l * 16;
    f32x16 A0, A1, B0, B1;
    bf16x8 pf0, pf1, pf2, pf3, fr[8];
    int sl_prev = 49152, sl_cur = 0;
#define ROT() do { sl_prev = sl_cur; sl_cur = (sl_cur + 16384) & 65535; } while (0)
    const int tsw = 2 * pi + 3;
    const char* hsrc = kvh + (w & 3) * 4096 + l * 16;
    const unsigned hk = lds0 + (w & 3) * 4096, hv = lds0 + 65536 + (w & 3) * 4096;
#define STAGE_ISSUE(t_) do { if ((t_) < tsw) { const int tk_ = (t_) + 3 < NT ? (t_) + 3 : NT - 1, tv_ = (t_) + 2 < NT ? (t_) + 2 : NT - 1; DMA_K(tk_, sl_prev); DMA_V(tv_, (sl_cur + 32768) & 65535); } } while (0)
#define DMA_ARGS(t_) (t_) < tsw, ksrc + (size_t)((t_) + 3 < NT ? (t_) + 3 : NT - 1) * 32768, kdst + sl_prev, ksrc + (size_t)((t_) + 2 < NT ? (t_) + 2 : NT - 1) * 32768 + 16384, vdst + ((sl_cur + 32768) & 65535)
#define STAGE_HELP(t_) do { const int tk_ = (t_) + 3 < NT ? (t_) + 3 : NT - 1, tv_ = (t_) + 2 < NT ? (t_) + 2 : NT - 1; \
        const char* gk_ = hsrc + (size_t)tk_ * 32768; const char* gv_ = hsrc + (size_t)tv_ * 32768 + 16384; const unsigned sk_ = hk + sl_prev, sv_ = hv + ((sl_cur + 32768) & 65535); \
        glds16(gk_, sk_); glds16(gk_ + 1024, sk_ + 1024); glds16(gk_ + 2048, sk_ + 2048); glds16(gk_ + 3072, sk_ + 3072); \
        glds16(gv_, sv_); glds16(gv_ + 1024, sv_ + 1024); glds16(gv_ + 2048, sv_ + 2048); glds16(gv_ + 3072, sv_ + 3072); } while (0)
    asm volatile("s_waitcnt vmcnt(2) lgkmcnt(0)\n\ts_barrier" ::: "memory");
    STAGE_ISSUE(0);
    {
        A0 = f32x16{}; A1 = f32x16{};
#pragma unroll
        for (int ds = 0; ds < 8; ++ds) {
            A0 = MFMA(ldsb128(kp0 + ds * 1024), qf[ds], A0);
            A1 = MFMA(ldsb128(kp0 + 8192 + ds * 1024), qf[ds], A1);
        }
        if (tl == 0) att_mask(A0, A1, qrow - 4 * hi);
        const float mx = half_swap_max(att_rowmax(A0, A1));
#pragma unroll
        for (int r = 0; r < 16; ++r) { A0[r] = __builtin_amdgcn_exp2f(A0[r] - mx); A1[r] = A1[r] - mx; negm[r] = -mx; }
#pragma unroll
        for (int g = 0; g < PD; ++g) fr[g] = ldsb128(kp0 + 16384 + kfrag_off(g));
    }
    WAIT_BAR4(); ROT();
    int t = 1;
    for (; t + 1 <= tl - 1; t += 2) {
        STAGE_ISSUE(t); att_step<false>(B0, B1, A0, A1, o, negm, l_run, qf, fr, kp0 + sl_cur, vp0 + sl_prev, kp0 + ((sl_cur + 16384) & 65535), 0); WAITB(t); ROT();
        STAGE_ISSUE(t + 1); att_step<false>(A0, A1, B0, B1, o, negm, l_run, qf, fr, kp0 + sl_cur, vp0 + sl_prev, kp0 + ((sl_cur + 16384) & 65535), 0); WAITB(t + 1); ROT();
    }
    if (t <= tl - 1) {
        STAGE_ISSUE(t); att_step<false>(B0, B1, A0, A1, o, negm, l_run, qf, fr, kp0 + sl_cur, vp0 + sl_prev, kp0 + ((sl_cur + 16384) & 65535), 0); WAITB(t); ROT();
        A0 = B0; A1 = B1; ++t;
    }
    if (tl >= 1) {
        STAGE_ISSUE(t); att_step<true>(B0, B1, A0, A1, o, negm, l_run, qf, fr, kp0 + sl_cur, vp0 + sl_prev, kp0 + ((sl_cur + 16384) & 65535), qrow - t * 64 - 4 * hi); WAITB(t); ROT();
        ++t;
    } else { B0 = A0; B1 = A1; }
    if (t < NT) STAGE_ISSUE(t);
    {
        float sacc = 0.f;
#pragma unroll
        for (int r = 0; r < 16; ++r) { B1[r] = __builtin_amdgcn_exp2f(B1[r]); sacc += B0[r] + B1[r]; }
        l_run += sacc;
        pf0 = pack8(B0, 0); pf1 = pack8(B0, 1); pf2 = pack8(B1, 0); pf3 = pack8(B1, 1);
        const lds_cptr vp = vp0 + sl_prev;
#pragma unroll
        for (int d = 0; d < 4; ++d) {
            o[d] = MFMA(ldsb128(vp + (d * 4 + 0) * 1024), pf0, o[d]);
            o[d] = MFMA(ldsb128(vp + (d * 4 + 1) * 1024), pf1, o[d]);
            o[d] = MFMA(ldsb128(vp + (d * 4 + 2) * 1024), pf2, o[d]);
            o[d] = MFMA(ldsb128(vp + (d * 4 + 3) * 1024), pf3, o[d]);
        }
    }
    STAMP_AT(ws, 1, 3);
    {
        const float inv = 1.f / half_swap_sum(l_run);
        char* cb = ws + WS_CTX + ((size_t)(rb * 256 + head * 8) * 64 + l) * 16;
#pragma unroll
        for (int d = 0; d < 4; ++d) {
#pragma unroll
            for (int r = 0; r < 16; ++r) o[d][r] *= inv;
            *(bf16x8*)(cb + (2 * d) * 1024) = pack8(o[d], 0);
            *(bf16x8*)(cb + (2 * d + 1) * 1024) = pack8(o[d], 1);
        }
    }
    if (t < NT) {
        WAIT_ALL_BAR(); ROT(); ++t;
        for (; t < NT && t < tsw; ++t) { STAGE_ISSUE(t); WAIT_BAR4(); ROT(); }
        if (w < 4) { for (; t < NT; ++t) { STAGE_HELP(t); asm volatile("s_waitcnt vmcnt(8) lgkmcnt(0)\n\ts_barrier" ::: "memory"); ROT(); } }
        else { for (; t < NT; ++t) { WAIT_ALL_BAR(); ROT(); } }
    }
    asm volatile("s_waitcnt vmcnt(0)" ::: "memory");
    STAMP_AT(ws, 1, 4);
#undef DMA_K
#undef DMA_V
#undef ROT
#undef STAGE_ISSUE
#undef STAGE_HELP
#undef DMA_ARGS
#undef WAITB
}

template <int TQ, int TN, int KW, int BKS, bool APERM, bool CT>
struct G {
    static constexpr int NWAVE = TQ * TN * KW, RB = TN * 128, A_BYTES = 2 * TQ * BKS * 1024, W_BYTES = 16 * BKS * RB, STAGE = A_BYTES + W_BYTES;
    static constexpr int CPR = RB / 16;
    static_assert(STAGE == 32768 && (2 * TQ * BKS + W_BYTES / 1024) / NWAVE == 4, "4 glds per wave per stage, 4 x 32 KB ring");
    static DI int swz(int row) { return RB == 256 ? ((row & 3) << 2) : (((row >> 1) & 1) << 2); }
    static DI void stage(const char* AF, size_t a_rb_stride, const u16* Wp, int ldw, int kst, unsigned ldsaddr, int w, int l) {
#pragma unroll
        for (int p0 = 0; p0 < 2 * TQ * BKS; p0 += NWAVE) {
            const int p = p0 + w, rbi = p / BKS, ks = p % BKS;
            glds16(AF + rbi * a_rb_stride + (size_t)(kst * BKS + ks) * 1024 + l * 16, ldsaddr + p * 1024);
        }
#pragma unroll
        for (int p0 = 0; p0 < W_BYTES / 1024; p0 += NWAVE) {
            const int p = p0 + w, row = p * (1024 / RB) + l / CPR, c = (l % CPR) ^ swz(row);
            glds16(Wp + (size_t)(kst * BKS * 16 + row) * ldw + c * 8, ldsaddr + A_BYTES + p * 1024);
        }
    }
    static constexpr int NPA = 2 * TQ * BKS / NWAVE, NPW = W_BYTES / 1024 / NWAVE;
    static DI void stage_piece(int i, const char* AF, size_t a_rb_stride, const u16* Wp, int ldw, int kst, unsigned ldsaddr, int w, int l) {
        if (i < NPA) {
            const int p = i * NWAVE + w, rbi = p / BKS, ks = p % BKS;
            glds16(AF + rbi * a_rb_stride + (size_t)(kst * BKS + ks) * 1024 + l * 16, ldsaddr + p * 1024);
        } else {
            const int p = (i - NPA) * NWAVE + w, row = p * (1024 / RB) + l / CPR, c = (l % CPR) ^ swz(row);
            glds16(Wp + (size_t)(kst * BKS * 16 + row) * ldw + c * 8, ldsaddr + A_BYTES + p * 1024);
        }
    }
    template <bool RM, class EPI>
    static DI void run(EPI epi, const char* AF, size_t a_rb_stride, const u16* Wp, int ldw, int nstage, int rot, char* lds, float (&red)[64 / KW], int& tq, int& tn, int& kw, char* ws, int kid) {
        const int tid = threadIdx.x, l = tid & 63, hi = l >> 5;
        STAMP_AT(ws, kid, 0);
        const int w = __builtin_amdgcn_readfirstlane(tid >> 6);
        kw = w % KW; const int tt = w / KW; tq = tt / TN; tn = tt % TN;
        const unsigned lds0 = (unsigned)(uintptr_t)lds;
        const int q4 = (l & 15) >> 2, p4 = l & 3, blk = (l >> 4) & 1;
        f32x16 acc[2][2];
#pragma unroll
        for (int i = 0; i < 2; ++i) { acc[i][0] = f32x16{}; acc[i][1] = f32x16{}; }
        int kst = rot;
#define NEXTK() do { kst = (kst + 1 == nstage) ? 0 : kst + 1; } while (0)
        stage(AF, a_rb_stride, Wp, ldw, kst, lds0, w, l); NEXTK();
        stage(AF, a_rb_stride, Wp, ldw, kst, lds0 + STAGE, w, l); NEXTK();
        stage(AF, a_rb_stride, Wp, ldw, kst, lds0 + 2 * STAGE, w, l); NEXTK();
        for (int st = 0; st < nstage; ++st) {
            if (st + 2 < nstage) asm volatile("s_waitcnt vmcnt(8) lgkmcnt(0)\n\ts_barrier" ::: "memory");
            else if (st + 1 < nstage) asm volatile("s_waitcnt vmcnt(4) lgkmcnt(0)\n\ts_barrier" ::: "memory");
            else WAIT_ALL_BAR();
            if (st == 0) STAMP_AT(ws, kid, 1);
            const bool pre = st + 3 < nstage; const unsigned pdst = lds0 + ((st + 3) & 3) * STAGE;
            lds_cptr ab = (lds_cptr)lds + (st & 3) * STAGE;
            lds_cptr wb = ab + A_BYTES;
#pragma unroll
            for (int kk = 0; kk < BKS / KW; ++kk) {
                const int ks = kw * (BKS / KW) + kk;
                bf16x8 af[2], wf[2];
#pragma unroll
                for (int i = 0; i < 2; ++i) af[i] = ldsb128(ab + ((2 * tq + i) * BKS + ks) * 1024 + l * 16);
#pragma unroll
                for (int j = 0; j < 2; ++j) {
                    const int r0 = 16 * ks + (APERM ? 4 * hi : 8 * hi) + q4, r1 = r0 + (APERM ? 8 : 4);
                    const int ch = 4 * (2 * tn + j) + 2 * blk + (p4 >> 1);
                    wf[j] = wfrag(wb + r0 * RB + ((ch ^ swz(r0)) << 4) + 8 * (p4 & 1), wb + r1 * RB + ((ch ^ swz(r1)) << 4) + 8 * (p4 & 1));
                }
#pragma unroll
                for (int i = 0; i < 2; ++i)
#pragma unroll
                    for (int j = 0; j < 2; ++j) {
                        if (CT) acc[i][j] = MFMA(wf[j], af[i], acc[i][j]);
                        else acc[i][j] = MFMA(af[i], wf[j], acc[i][j]);
                        constexpr int PER = 4 / (BKS / KW);
                        const int m = i * 2 + j;
                        if (PER == 4) { SBAR(); if (pre) stage_piece(m, AF, a_rb_stride, Wp, ldw, kst, pdst, w, l); SBAR(); }
                        else if (m & 1) { SBAR(); if (pre) stage_piece(kk * 2 + (m >> 1), AF, a_rb_stride, Wp, ldw, kst, pdst, w, l); SBAR(); }
                    }
            }
            if (pre) NEXTK();
        }
        BAR_LGKM();
        STAMP_AT(ws, kid, 2);
        float* rb_ = (float*)lds;
        constexpr int RS = RM ? 68 : 64;
#pragma unroll
        for (int i = 0; i < 2; ++i)
#pragma unroll
            for (int j = 0; j < 2; ++j)
#pragma unroll
                for (int r = 0; r < 16; ++r) rb_[(w * 64 + (i * 2 + j) * 16 + r) * RS + l] = acc[i][j][r];
        BAR_LGKM();
        if constexpr (RM) {
            constexpr int C8 = TN * 8, NITEM = 64 * TQ * C8 / (64 * NWAVE);
#pragma unroll
            for (int it = 0; it < NITEM; ++it) {
                const int id = it * (64 * NWAVE) + tid, row = id / C8, c8 = id % C8;
                const int rr = row & 31, r = (rr & 3) + 4 * (rr >> 3), h = (rr >> 2) & 1;
                const int t2 = (row >> 6) * TN + (c8 >> 3), reg = (((row >> 5) & 1) * 2 + ((c8 >> 2) & 1)) * 16 + r, ln = (c8 & 3) * 8 + 32 * h;
                f32x4 a0 = {0.f, 0.f, 0.f, 0.f}, a1 = a0;
#pragma unroll
                for (int k2 = 0; k2 < KW; ++k2) {
                    const float* q_ = rb_ + ((t2 * KW + k2) * 64 + reg) * RS + ln;
                    a0 += *(const f32x4*)q_; a1 += *(const f32x4*)(q_ + 4);
                }
                epi(row, c8 * 8, a0, a1);
            }
        } else {
#pragma unroll
            for (int e = 0; e < 64 / KW; ++e) {
                float s = 0.f;
#pragma unroll
                for (int k2 = 0; k2 < KW; ++k2) s += rb_[((tt * KW + k2) * 64 + kw * (64 / KW) + e) * RS + l];
                red[e] = s;
            }
        }
        STAMP_AT(ws, kid, 3);
    }
};

typedef G<2, 2, 2, 4, true, false> G3;
constexpr int K3_LDS = 139264;
__global__ __launch_bounds__(512, 2) void k3_oproj(const u16* __restrict__ wo, char* __restrict__ ws) {
    extern __shared__ __attribute__((aligned(16))) char lds[];
    const int b = blockIdx.x, xcd = b & 7, r = b >> 3, ksl = xcd & 3, rp = (xcd >> 2) * 8 + (r >> 2), ct = r & 3;
    float red[32]; int tq, tn, kw;
    u16* part = (u16*)(ws + WS_PART) + (size_t)ksl * 2048 * 512 + (size_t)(rp * 128) * 512 + ct * 128;
    G3::run<true>([&](int row, int col, f32x4 a0, f32x4 a1) {
                      u32x4 v = {pk2(a0[0], a0[1]), pk2(a0[2], a0[3]), pk2(a1[0], a1[1]), pk2(a1[2], a1[3])};
                      *(u32x4*)(part + (size_t)row * 512 + col) = v; },
                  ws + WS_CTX + ((size_t)(rp * 4) * 256 + ksl * 64) * 1024, (size_t)256 * 1024, wo + (size_t)ksl * 1024 * 512 + ct * 128, 512, 16, (4 * ct + 2 * (r >> 2)) & 15, lds, red, tq, tn, kw, ws, 2);
    STAMP_AT(ws, 2, 4);
}

__global__ __launch_bounds__(512) void k3b_ln2(const u16* __restrict__ x, char* __restrict__ ws) {
    const int l = threadIdx.x & 63, row = blockIdx.x * 8 + (threadIdx.x >> 6);
    const u16* part = (const u16*)(ws + WS_PART) + (size_t)row * 512 + 8 * l;
    const float* mod = (const float*)(ws + WS_MOD);
    u32x4 pv[4];
#pragma unroll
    for (int s = 0; s < 4; ++s) pv[s] = *(const u32x4*)(part + (size_t)s * 2048 * 512);
    const u32x4 xv4 = *(const u32x4*)(x + (size_t)row * 512 + 8 * l);
    const f32x4 g0 = *(const f32x4*)(mod + 1024 + 8 * l), g1 = *(const f32x4*)(mod + 1024 + 8 * l + 4);
    const f32x4 c0 = *(const f32x4*)(mod + 2048 + 8 * l), c1 = *(const f32x4*)(mod + 2048 + 8 * l + 4);
    const f32x4 s0 = *(const f32x4*)(mod + 1536 + 8 * l), s1 = *(const f32x4*)(mod + 1536 + 8 * l + 4);
    float x1[8], xh[8];
#pragma unroll
    for (int j = 0; j < 4; ++j) {
        const int w0 = j >> 1, w1 = 2 + (j >> 1);
        const float a0 = (j & 1) ? (bfhi(pv[0][w0]) + bfhi(pv[1][w0])) + (bfhi(pv[2][w0]) + bfhi(pv[3][w0])) : (bflo(pv[0][w0]) + bflo(pv[1][w0])) + (bflo(pv[2][w0]) + bflo(pv[3][w0]));
        const float a1 = (j & 1) ? (bfhi(pv[0][w1]) + bfhi(pv[1][w1])) + (bfhi(pv[2][w1]) + bfhi(pv[3][w1])) : (bflo(pv[0][w1]) + bflo(pv[1][w1])) + (bflo(pv[2][w1]) + bflo(pv[3][w1]));
        const float xa = (j & 1) ? bfhi(xv4[j >> 1]) : bflo(xv4[j >> 1]), xb = (j & 1) ? bfhi(xv4[2 + (j >> 1)]) : bflo(xv4[2 + (j >> 1)]);
        x1[j] = bfr(xa + bfr(g0[j] * bfr(a0)));
        x1[4 + j] = bfr(xb + bfr(g1[j] * bfr(a1)));
    }
    u32x4 o1 = {pk2(x1[0], x1[1]), pk2(x1[2], x1[3]), pk2(x1[4], x1[5]), pk2(x1[6], x1[7])};
    *(u32x4*)(ws + WS_X1 + ((size_t)row * 512 + 8 * l) * 2) = o1;
    ln_row(x1, xh);
    float h[8];
#pragma unroll
    for (int j = 0; j < 4; ++j) { h[j] = bfr(xh[j] * bfr(1.f + c0[j])) + s0[j]; h[4 + j] = bfr(xh[4 + j] * bfr(1.f + c1[j])) + s1[j]; }
    u32x4 oh = {pk2(h[0], h[1]), pk2(h[2], h[3]), pk2(h[4], h[5]), pk2(h[6], h[7])};
    *(u32x4*)(ws + WS_H2 + ((size_t)((row >> 5) * 32 + (l >> 1)) * 64 + (row & 31) + 32 * (l & 1)) * 16) = oh;
}

typedef G<2, 2, 2, 4, false, true> G4;
__global__ __launch_bounds__(512, 2) void k4_ffn1(const u16* __restrict__ w1, char* __restrict__ ws) {
    extern __shared__ __attribute__((aligned(16))) char lds[];
    const int b = blockIdx.x, xcd = b & 7, jj = b >> 3, rp = 4 * (xcd >> 1) + jj / 7, ct = 7 * (xcd & 1) + jj % 7;
    const int l = threadIdx.x & 63;
    float red[32]; int tq, tn, kw;
    G4::run<false>(0, ws + WS_H2 + (size_t)(rp * 4) * 32 * 1024, (size_t)32 * 1024, w1 + ct * 128, DFF, 8, (jj % 7 + 2 * (jj / 7)) & 7, lds, red, tq, tn, kw, ws, 3);
#pragma unroll
    for (int j = 0; j < 2; ++j)
#pragma unroll
        for (int s = 0; s < 2; ++s) {
            const int e = j * 16 + 8 * s;
            u32x4 v = {pk2(fmaxf(red[e], 0.f), fmaxf(red[e + 1], 0.f)), pk2(fmaxf(red[e + 2], 0.f), fmaxf(red[e + 3], 0.f)),
                       pk2(fmaxf(red[e + 4], 0.f), fmaxf(red[e + 5], 0.f)), pk2(fmaxf(red[e + 6], 0.f), fmaxf(red[e + 7], 0.f))};
            const int rbq = rp * 4 + 2 * tq + kw, kstep = (ct * 128 + 64 * tn + 32 * j) / 16 + s;
            *(u32x4*)(ws + WS_H1 + ((size_t)(rbq * 112 + kstep) * 64 + l) * 16) = v;
        }
    STAMP_AT(ws, 3, 4);
}

typedef G<1, 1, 8, 8, true, false> G5;
__global__ __launch_bounds__(512, 2) void k5_ffn2(const u16* __restrict__ w2, char* __restrict__ ws, u16* __restrict__ out) {
    extern __shared__ __attribute__((aligned(16))) char lds[];
    const int b = blockIdx.x, xcd = b & 7, jj = b >> 3, rp = 8 * (xcd >> 1) + (jj >> 2), ct = 4 * (xcd & 1) + (jj & 3);
    float red[8]; int tq, tn, kw;
    const float* gb = (const float*)(ws + WS_MOD) + 2560 + ct * 64;
    const u16* x1 = (const u16*)(ws + WS_X1) + (size_t)(rp * 64) * 512 + ct * 64;
    u16* op = out + (size_t)(rp * 64) * 512 + ct * 64;
    G5::run<true>([&](int row, int col, f32x4 a0, f32x4 a1) {
                      const u32x4 xv = *(const u32x4*)(x1 + (size_t)row * 512 + col);
                      const f32x4 g0 = *(const f32x4*)(gb + col), g1 = *(const f32x4*)(gb + col + 4);
                      u32x4 v = {pk2(bflo(xv[0]) + bfr(g0[0] * bfr(a0[0])), bfhi(xv[0]) + bfr(g0[1] * bfr(a0[1]))),
                                 pk2(bflo(xv[1]) + bfr(g0[2] * bfr(a0[2])), bfhi(xv[1]) + bfr(g0[3] * bfr(a0[3]))),
                                 pk2(bflo(xv[2]) + bfr(g1[0] * bfr(a1[0])), bfhi(xv[2]) + bfr(g1[1] * bfr(a1[1]))),
                                 pk2(bflo(xv[3]) + bfr(g1[2] * bfr(a1[2])), bfhi(xv[3]) + bfr(g1[3] * bfr(a1[3])))};
                      *(u32x4*)(op + (size_t)row * 512 + col) = v; },
                  ws + WS_H1 + (size_t)(rp * 2) * 112 * 1024, (size_t)112 * 1024, w2 + ct * 64, 512, 14, (3 * (jj & 3) + 2 * (jj >> 2)) % 14, lds, red, tq, tn, kw, ws, 4);
    STAMP_AT(ws, 4, 4);
}

extern "C" void kernel_launch(void* const* d_in, const int* in_sizes, int n_in, void* d_out, int out_size, void* d_ws, size_t ws_size,
                              hipStream_t stream) {
    (void)in_sizes; (void)n_in; (void)out_size; (void)ws_size;
    const u16* x = (const u16*)d_in[0];
    const u16* ckv = (const u16*)d_in[1];
    const u16* wq = (const u16*)d_in[2];
    const u16* wuk = (const u16*)d_in[3];
    const u16* wuv = (const u16*)d_in[4];
    const u16* wo = (const u16*)d_in[5];
    const u16* w1 = (const u16*)d_in[7];
    const u16* w2 = (const u16*)d_in[8];
    const u16* cvec = (const u16*)d_in[9];
    const u16* wmod = (const u16*)d_in[10];
    char* ws = (char*)d_ws;
    hipFuncSetAttribute((const void*)k1_kvproj, hipFuncAttributeMaxDynamicSharedMemorySize, K1_LDS);
    hipFuncSetAttribute((const void*)k2_attn, hipFuncAttributeMaxDynamicSharedMemorySize, K2_LDS);
    hipFuncSetAttribute((const void*)k3_oproj, hipFuncAttributeMaxDynamicSharedMemorySize, K3_LDS);
    hipFuncSetAttribute((const void*)k4_ffn1, hipFuncAttributeMaxDynamicSharedMemorySize, K3_LDS);
    hipFuncSetAttribute((const void*)k5_ffn2, hipFuncAttributeMaxDynamicSharedMemorySize, K3_LDS);
    k1_kvproj<<<256, 512, K1_LDS, stream>>>(ckv, wuk, wuv, x, cvec, wmod, ws);
    k2_attn<<<256, 512, K2_LDS, stream>>>(wq, ws);
    k3_oproj<<<256, 512, K3_LDS, stream>>>(wo, ws);
    k3b_ln2<<<256, 512, 0, stream>>>(x, ws);
    k4_ffn1<<<224, 512, K3_LDS, stream>>>(w1, ws);
    k5_ffn2<<<256, 512, K3_LDS, stream>>>(w2, ws, (u16*)d_out);
}
```

```cpp
#include <hip/hip_runtime.h>
#include <hip/hip_bf16.h>
#include <stdint.h>

typedef unsigned short u16;
typedef short bf16x8 __attribute__((ext_vector_type(8)));
typedef short s16x4 __attribute__((ext_vector_type(4)));
typedef short v4i16_t __attribute__((ext_vector_type(4)));
typedef float f32x16 __attribute__((ext_vector_type(16)));
typedef float f32x4 __attribute__((ext_vector_type(4)));
typedef float f32x2 __attribute__((ext_vector_type(2)));
typedef unsigned u32x4 __attribute__((ext_vector_type(4)));
typedef __bf16 bf16x2_t __attribute__((ext_vector_type(2)));
typedef __attribute__((address_space(3))) const char* lds_cptr;
#define DI __device__ __forceinline__
#define MFMA(a, b, c) __builtin_amdgcn_mfma_f32_32x32x16_bf16((a), (b), (c), 0, 0, 0)
#define SBAR() __builtin_amdgcn_sched_barrier(0)
#define STAMPX(ws_, idx) do { } while (0)
#define STAMP_AT(ws_, kid, idx) do { } while (0)

constexpr int SEQ = 2048, DM = 512, NH = 32, HD = 128, DFF = 1792;

constexpr size_t WS_KV = 0;
constexpr size_t WS_XH = WS_KV + (size_t)32 * 32 * 32768;
constexpr size_t WS_MOD = WS_XH + (size_t)64 * 32 * 1024;
constexpr size_t WS_GS = WS_MOD + 12288;
constexpr size_t WS_CTX = WS_MOD + 16384;
constexpr size_t WS_PART = WS_CTX + (size_t)64 * 256 * 1024;
constexpr size_t WS_X1 = WS_PART + (size_t)4 * 2048 * 512 * 4;
constexpr size_t WS_H2 = WS_X1 + (size_t)2048 * 512 * 2;
constexpr size_t WS_H1 = WS_H2 + (size_t)64 * 32 * 1024;

DI unsigned pk2(float a, float b) { f32x2 v = {a, b}; bf16x2_t r = __builtin_convertvector(v, bf16x2_t); return __builtin_bit_cast(unsigned, r); }
DI float bf2f(u16 v) { return __uint_as_float(((unsigned)v) << 16); }
DI float bfr(float x) { return __uint_as_float(pk2(x, 0.f) << 16); }
DI float bflo(unsigned w) { return __uint_as_float(w << 16); }
DI float bfhi(unsigned w) { return __uint_as_float(w & 0xffff0000u); }
DI bf16x8 pack8(const f32x16& a, int s) {
    u32x4 w = {pk2(a[8 * s], a[8 * s + 1]), pk2(a[8 * s + 2], a[8 * s + 3]), pk2(a[8 * s + 4], a[8 * s + 5]), pk2(a[8 * s + 6], a[8 * s + 7])};
    return __builtin_bit_cast(bf16x8, w);
}
DI int crow(int r, int hi) { return (r & 3) + 8 * (r >> 2) + 4 * hi; }
DI void glds16(const void* g, unsigned lds_base) {
    unsigned sv;
    asm volatile("s_mov_b32 %0, m0\n\ts_mov_b32 m0, %2\n\ts_nop 0\n\tglobal_load_lds_dwordx4 %1, off\n\ts_mov_b32 m0, %0"
                 : "=&s"(sv) : "v"(g), "s"(lds_base) : "memory");
}
DI bf16x8 ldsb128(lds_cptr p) { return *(const __attribute__((address_space(3))) bf16x8*)p; }
DI s16x4 ldstr(lds_cptr p) { return __builtin_bit_cast(s16x4, __builtin_amdgcn_ds_read_tr16_b64_v4i16((__attribute__((address_space(3))) v4i16_t*)p)); }
DI bf16x8 wfrag(lds_cptr p0, lds_cptr p1) { s16x4 lo = ldstr(p0), hi = ldstr(p1); return __builtin_shufflevector(lo, hi, 0, 1, 2, 3, 4, 5, 6, 7); }
#define WAIT_ALL_BAR() asm volatile("s_waitcnt vmcnt(0) lgkmcnt(0)\n\ts_barrier" ::: "memory")
#define BAR_LGKM() asm volatile("s_waitcnt lgkmcnt(0)\n\ts_barrier" ::: "memory")
DI float wave_sum(float v) {
#pragma unroll
    for (int m = 1; m < 64; m <<= 1) v += __shfl_xor(v, m);
    return v;
}
DI float half_swap_max(float m) { auto rr = __builtin_amdgcn_permlane32_swap(__float_as_uint(m), __float_as_uint(m), false, false); return fmaxf(__uint_as_float(rr[0]), __uint_as_float(rr[1])); }
DI float half_swap_sum(float m) { auto rr = __builtin_amdgcn_permlane32_swap(__float_as_uint(m), __float_as_uint(m), false, false); return __uint_as_float(rr[0]) + __uint_as_float(rr[1]); }

DI void ln_row(const float (&xv)[8], float (&o)[8]) {
    float s = 0.f;
#pragma unroll
    for (int j = 0; j < 8; ++j) s += xv[j];
    const float mean = wave_sum(s) * (1.f / 512.f);
    float q = 0.f;
#pragma unroll
    for (int j = 0; j < 8; ++j) { const float d = xv[j] - mean; q += d * d; }
    const float var = wave_sum(q) * (1.f / 512.f);
    const float mb = bfr(mean), vb = bfr(var);
    const float den = bfr(sqrtf(bfr(vb + 1e-5f)));
#pragma unroll
    for (int j = 0; j < 8; ++j) o[j] = bfr(bfr(xv[j] - mb) / den);
}

constexpr int K1_LDS = 131072;
template <bool VT>
DI void k1_gemm(const u16* __restrict__ ckv, const u16* __restrict__ W, char* __restrict__ kvimg, char* lds, int key0, int h0, int h1, int rot,
                 const u16* __restrict__ x, const u16* __restrict__ cvec, const u16* __restrict__ wmod, char* __restrict__ ws) {
    const int tid = threadIdx.x, l = tid & 63, r32 = l & 31, hi = l >> 5;
    const int w = __builtin_amdgcn_readfirstlane(tid >> 6);
    const int wk = w >> 2, wc = w & 3;
    const unsigned lds0 = (unsigned)(uintptr_t)lds;
    const u16* asrc[4]; const u16* wsrc[4];
#pragma unroll
    for (int n = 0; n < 4; ++n) {
        const int P = n * 8 + w, row = 8 * P + (l >> 3), c = (l & 7) ^ ((row >> 1) & 7);
        asrc[n] = ckv + (size_t)(key0 + row) * 512 + c * 8;
        const int half = P >> 4, rw = 4 * (P & 15) + (l >> 4), cw = (l & 15) ^ ((rw & 3) << 2);
        wsrc[n] = W + (size_t)rw * 4096 + (half ? h1 : h0) * 128 + cw * 8;
    }
#define K1_PIECE(i, kt_, bb_) do { if ((i) < 4) glds16(asrc[i] + (kt_) * 64, (bb_) + ((i) * 8 + w) * 1024); \
        else glds16(wsrc[(i) - 4] + (size_t)(kt_) * 64 * 4096, (bb_) + 32768 + (((i) - 4) * 8 + w) * 1024); } while (0)
#pragma unroll
    for (int i = 0; i < 8; ++i) K1_PIECE(i, rot, lds0);
    {
        const int b = blockIdx.x, row = 8 * b + w, half = tid & 1, rowi = tid >> 1, bm = b < 192 ? b : 0;
        const u32x4 xv4 = *(const u32x4*)(x + (size_t)row * 512 + 8 * l);
        const u32x4 wv0 = *(const u32x4*)(wmod + (size_t)rowi * 3072 + 16 * bm + 8 * half), wv1 = *(const u32x4*)(wmod + (size_t)(rowi + 256) * 3072 + 16 * bm + 8 * half);
        const float c0 = bf2f(cvec[rowi]), c1 = bf2f(cvec[rowi + 256]);
        float* scr = (float*)(lds + 65536);
        {
            float xv[8], o[8];
#pragma unroll
            for (int j = 0; j < 4; ++j) { xv[2 * j] = bflo(xv4[j]); xv[2 * j + 1] = bfhi(xv4[j]); }
            ln_row(xv, o);
            u32x4 ov = {pk2(o[0], o[1]), pk2(o[2], o[3]), pk2(o[4], o[5]), pk2(o[6], o[7])};
            *(u32x4*)(ws + WS_XH + ((size_t)((row >> 5) * 32 + (l >> 1)) * 64 + (row & 31) + 32 * (l & 1)) * 16) = ov;
        }
        if (b < 192) {
            float a8[8];
            const float sc0 = bfr(c0 / (1.f + __expf(-c0))), sc1 = bfr(c1 / (1.f + __expf(-c1)));
#pragma unroll
            for (int j = 0; j < 4; ++j) { a8[2 * j] = sc0 * bflo(wv0[j]) + sc1 * bflo(wv1[j]); a8[2 * j + 1] = sc0 * bfhi(wv0[j]) + sc1 * bfhi(wv1[j]); }
#pragma unroll
            for (int m = 2; m < 64; m <<= 1)
#pragma unroll
                for (int j = 0; j < 8; ++j) a8[j] += __shfl_xor(a8[j], m);
            if (l < 2) {
#pragma unroll
                for (int j = 0; j < 8; ++j) scr[(w * 2 + l) * 8 + j] = a8[j];
            }
            BAR_LGKM();
            if (tid < 16) {
                float s = 0.f;
#pragma unroll
                for (int ww = 0; ww < 8; ++ww) s += scr[(ww * 2 + (tid >> 3)) * 8 + (tid & 7)];
                const int col = 16 * b + tid; const float mv = bfr(s);
                ((float*)(ws + WS_MOD))[col] = mv;
                if (col < 512) ((float*)(ws + WS_GS))[512 + col] = mv;
                else if (col < 1024) ((float*)(ws + WS_GS))[col - 512] = bfr(1.f + mv);
            }
        }
    }
    f32x16 acc[4][2];
#pragma unroll
    for (int a = 0; a < 4; ++a) { acc[a][0] = f32x16{}; acc[a][1] = f32x16{}; }
    const int q4 = (l & 15) >> 2, p4 = l & 3, blk = (l >> 4) & 1;
    const int xa = (l >> 1) & 7;
    STAMP_AT(kvimg, 0, 1);
    for (int kt = 0; kt < 8; ++kt) {
        WAIT_ALL_BAR();
        if (kt == 0) STAMP_AT(kvimg, 0, 2);
        const bool pre = kt + 1 < 8;
        const int ktn = (rot + kt + 1) & 7;
        const unsigned bb = lds0 + ((kt + 1) & 1) * 65536;
        lds_cptr ab = (lds_cptr)lds + (kt & 1) * 65536;
        lds_cptr wb = ab + 32768 + (wc >> 1) * 16384;
#pragma unroll
        for (int s = 0; s < 4; ++s) {
            bf16x8 af[4], wf[2];
#pragma unroll
            for (int kb = 0; kb < 4; ++kb) af[kb] = ldsb128(ab + (128 * wk + 32 * kb + r32) * 128 + (((2 * s + hi) ^ xa) << 4));
#pragma unroll
            for (int j = 0; j < 2; ++j) {
                const int db = 2 * (wc & 1) + j;
                lds_cptr p = wb + (16 * s + 8 * hi + q4) * 256 + 64 * (db ^ q4) + 32 * blk + 16 * (p4 >> 1) + 8 * (p4 & 1);
                wf[j] = wfrag(p, p + 1024);
            }
#pragma unroll
            for (int kb = 0; kb < 4; ++kb) {
#pragma unroll
                for (int j = 0; j < 2; ++j) {
                    if (VT) acc[kb][j] = MFMA(af[kb], wf[j], acc[kb][j]);
                    else acc[kb][j] = MFMA(wf[j], af[kb], acc[kb][j]);
                }
                if ((kb & 1) == 0) { if (pre) K1_PIECE(2 * s + (kb >> 1), ktn, bb); }
            }
        }
    }
#undef K1_PIECE
    STAMP_AT(kvimg, 0, 3);
    const int head = (wc >> 1) ? h1 : h0;
#pragma unroll
    for (int kb = 0; kb < 4; ++kb) {
        const int key = key0 + 128 * wk + 32 * kb, tile = key >> 6, kbl = kb & 1;
        char* tb = kvimg + ((size_t)(head * 32 + tile) << 15);
#pragma unroll
        for (int j = 0; j < 2; ++j) {
            const int db = 2 * (wc & 1) + j;
#pragma unroll
            for (int s2 = 0; s2 < 2; ++s2) {
                const int fo = VT ? (16384 + (db * 4 + kbl * 2 + s2) * 1024) : ((kbl * 8 + db * 2 + s2) * 1024);
                *(bf16x8*)(tb + fo + l * 16) = pack8(acc[kb][j], s2);
            }
        }
    }
}

__global__ __launch_bounds__(512, 2) void k1_kvproj(const u16* __restrict__ ckv, const u16* __restrict__ wuk, const u16* __restrict__ wuv,
                                                    const u16* __restrict__ x, const u16* __restrict__ cvec, const u16* __restrict__ wmod,
                                                    char* __restrict__ ws) {
    extern __shared__ __attribute__((aligned(16))) char lds[];
    const int b = blockIdx.x, tid = threadIdx.x, l = tid & 63;
    const int w = __builtin_amdgcn_readfirstlane(tid >> 6);
    const int xcd = b & 7, r = b >> 3, kb8 = r & 7, combo = r >> 3, type = combo & 1, hp = combo >> 1;
    const int h0 = xcd + 16 * hp, h1 = h0 + 8, key0 = kb8 * 256;
    const u16* W = type ? wuv : wuk;
    STAMP_AT(ws, 0, 0);
    const int rot = (kb8 + 2 * combo) & 7;
    if (type) k1_gemm<true>(ckv, W, ws + WS_KV, lds, key0, h0, h1, rot, x, cvec, wmod, ws);
    else k1_gemm<false>(ckv, W, ws + WS_KV, lds, key0, h0, h1, rot, x, cvec, wmod, ws);
    STAMP_AT(ws, 0, 4);
}

constexpr float ATT_THR = 8.f;
#define PIN(x) asm volatile("" : "+v"(x))
#define MX3(a, b, c) __builtin_fmaxf(__builtin_fmaxf((a), (b)), (c))
DI float att_rowmax(const f32x16& p0, const f32x16& p1) {
    float a = MX3(p0[0], p0[1], p1[0]), b = MX3(p0[2], p0[3], p1[1]); a = MX3(a, p1[2], p1[3]);
#pragma unroll
    for (int r = 4; r < 16; r += 4) { a = MX3(a, p0[r], p0[r + 1]); b = MX3(b, p0[r + 2], p0[r + 3]); a = MX3(a, p1[r], p1[r + 1]); b = MX3(b, p1[r + 2], p1[r + 3]); }
    return __builtin_fmaxf(a, b);
}
DI void att_mask(f32x16& s0, f32x16& s1, int dq) {
#pragma unroll
    for (int r = 0; r < 16; ++r) {
        const int c = (r & 3) + 8 * (r >> 2);
        if (c > dq) s0[r] = -INFINITY;
        if (c + 32 > dq) s1[r] = -INFINITY;
    }
}
constexpr int PD = 6;
DI int kfrag_off(int g) { return ((g & 1) * 8 + (g >> 1)) * 1024; }
DI int vfrag_off(int g) { return ((g & 3) * 4 + (g >> 2)) * 1024; }
template <bool MASK>
DI void att_step(f32x16& C0, f32x16& C1, const f32x16& P0, f32x16& P1, f32x16 (&o)[4], f32x16& negm, float& l_run,
                 const bf16x8 (&qf)[8], bf16x8 (&fr)[8], lds_cptr kp, lds_cptr vp, lds_cptr kpn, int dq) {
    unsigned pw[16];
    float sacc = 0.f, sacc2 = 0.f;
    SBAR();
#pragma unroll
    for (int g = 0; g < 16; ++g) {
        if ((g & 1) == 0) C0 = MFMA(fr[g & 7], qf[g >> 1], g < 2 ? negm : C0);
        else C1 = MFMA(fr[g & 7], qf[g >> 1], g < 2 ? negm : C1);
        if (g + PD < 16) fr[(g + PD) & 7] = ldsb128(kp + kfrag_off(g + PD));
        else fr[(g + PD) & 7] = ldsb128(vp + vfrag_off(g + PD - 16));
        P1[g] = __builtin_amdgcn_exp2f(P1[g]); PIN(P1[g]);
        sacc += P0[g]; PIN(sacc);
        if (g & 1) { pw[g >> 1] = pk2(P0[g - 1], P0[g]); PIN(pw[g >> 1]); }
        if (g >= 1) { sacc2 += P1[g - 1]; PIN(sacc2); }
        if (g >= 2 && (g & 1) == 0) { pw[8 + ((g - 2) >> 1)] = pk2(P1[g - 2], P1[g - 1]); PIN(pw[8 + ((g - 2) >> 1)]); }
        SBAR();
    }
    sacc2 += P1[15]; pw[15] = pk2(P1[14], P1[15]);
    l_run += sacc + sacc2;
    bool resc = false; float f = 1.f;
    float ma, mb;
#pragma unroll
    for (int g = 0; g < 16; ++g) {
        { const u32x4 pv = {pw[4 * (g >> 2)], pw[4 * (g >> 2) + 1], pw[4 * (g >> 2) + 2], pw[4 * (g >> 2) + 3]};
          o[g & 3] = MFMA(fr[g & 7], __builtin_bit_cast(bf16x8, pv), o[g & 3]); }
        if (g + PD < 16) fr[(g + PD) & 7] = ldsb128(vp + vfrag_off(g + PD));
        else fr[(g + PD) & 7] = ldsb128(kpn + kfrag_off(g + PD - 16));
        if (g == 1 && MASK) att_mask(C0, C1, dq);
        if (g == 1) { ma = MX3(C0[0], C0[1], C0[2]); mb = MX3(C0[3], C0[4], C0[5]); ma = MX3(ma, C0[6], C0[7]); mb = MX3(mb, C0[8], C0[9]); PIN(ma); PIN(mb); }
        if (g == 2) { ma = MX3(ma, C0[10], C0[11]); mb = MX3(mb, C0[12], C0[13]); ma = MX3(ma, C0[14], C0[15]); mb = MX3(mb, C1[0], C1[1]); PIN(ma); PIN(mb); }
        if (g == 3) { ma = MX3(ma, C1[2], C1[3]); mb = MX3(mb, C1[4], C1[5]); ma = MX3(ma, C1[6], C1[7]); mb = MX3(mb, C1[8], C1[9]); PIN(ma); PIN(mb); }
        if (g == 4) {
            ma = MX3(ma, C1[10], C1[11]); mb = MX3(mb, C1[12], C1[13]); ma = MX3(ma, C1[14], C1[15]);
            const float rm = __builtin_fmaxf(ma, mb);
            if (__builtin_expect(__any(rm > ATT_THR), 0)) {
                const float dl = __builtin_fmaxf(half_swap_max(rm), 0.f);
                f = __builtin_amdgcn_exp2f(-dl); l_run *= f; resc = true;
#pragma unroll
                for (int r = 0; r < 16; ++r) { C0[r] -= dl; C1[r] -= dl; negm[r] -= dl; }
            }
        }
        if (g >= 5) {
            const int e0 = g < 10 ? 2 * (g - 5) : 10 + (g - 10), e1 = g < 10 ? e0 + 2 : e0 + 1;
#pragma unroll
            for (int e = e0; e < e1; ++e) { C0[e] = __builtin_amdgcn_exp2f(C0[e]); PIN(C0[e]); }
        }
        SBAR();
    }
    if (__builtin_expect(resc, 0)) {
#pragma unroll
        for (int d = 0; d < 4; ++d)
#pragma unroll
            for (int r = 0; r < 16; ++r) o[d][r] *= f;
    }
}
template <bool MASK>
DI void att_hstep(f32x16& C, f32x16& P, f32x16 (&o)[4], f32x16& negm, float& l_run, const bf16x8 (&qf)[8], bf16x8 (&fr)[8],
                  lds_cptr kp, lds_cptr vp, lds_cptr kpn, int dq) {
    unsigned pw[8];
    float sacc = 0.f, sacc2 = 0.f;
    SBAR();
#pragma unroll
    for (int g = 0; g < 8; ++g) {
        C = MFMA(fr[g], qf[g], g == 0 ? negm : C);
        if (g + PD < 8) fr[(g + PD) & 7] = ldsb128(kp + (g + PD) * 1024);
        else fr[(g + PD) & 7] = ldsb128(vp + (((g + PD - 8) & 3) * 4 + ((g + PD - 8) >> 2)) * 1024);
        P[8 + g] = __builtin_amdgcn_exp2f(P[8 + g]); PIN(P[8 + g]);
        sacc += P[g]; PIN(sacc);
        if (g & 1) { pw[g >> 1] = pk2(P[g - 1], P[g]); PIN(pw[g >> 1]); }
        if (g >= 1) { sacc2 += P[7 + g]; PIN(sacc2); }
        if (g >= 2 && (g & 1) == 0) { pw[4 + ((g - 2) >> 1)] = pk2(P[6 + g], P[7 + g]); PIN(pw[4 + ((g - 2) >> 1)]); }
        SBAR();
    }
    sacc2 += P[15]; pw[7] = pk2(P[14], P[15]);
    l_run += sacc + sacc2;
    bool resc = false; float f = 1.f;
    float ma, mb;
#pragma unroll
    for (int g = 0; g < 8; ++g) {
        { const u32x4 pv = {pw[4 * (g >> 2)], pw[4 * (g >> 2) + 1], pw[4 * (g >> 2) + 2], pw[4 * (g >> 2) + 3]};
          o[g & 3] = MFMA(fr[g], __builtin_bit_cast(bf16x8, pv), o[g & 3]); }
        if (g + PD < 8) fr[(g + PD) & 7] = ldsb128(vp + (((g + PD) & 3) * 4 + ((g + PD) >> 2)) * 1024);
        else fr[(g + PD) & 7] = ldsb128(kpn + (g + PD - 8) * 1024);
        if (g == 1 && MASK) {
#pragma unroll
            for (int r = 0; r < 16; ++r) if ((r & 3) + 8 * (r >> 2) > dq) C[r] = -INFINITY;
        }
        if (g == 1) { ma = MX3(C[0], C[1], C[2]); mb = MX3(C[3], C[4], C[5]); ma = MX3(ma, C[6], C[7]); mb = MX3(mb, C[8], C[9]); PIN(ma); PIN(mb); }
        if (g == 2) {
            ma = MX3(ma, C[10], C[11]); mb = MX3(mb, C[12], C[13]); ma = MX3(ma, C[14], C[15]);
            const float rm = __builtin_fmaxf(ma, mb);
            if (__builtin_expect(__any(rm > ATT_THR), 0)) {
                const float dl = __builtin_fmaxf(half_swap_max(rm), 0.f);
                f = __builtin_amdgcn_exp2f(-dl); l_run *= f; resc = true;
#pragma unroll
                for (int r = 0; r < 16; ++r) { C[r] -= dl; negm[r] -= dl; }
            }
        }
        if (g >= 3) {
            const int e0 = g < 6 ? 2 * (g - 3) : 6 + (g - 6), e1 = g < 6 ? e0 + 2 : e0 + 1;
#pragma unroll
            for (int e = e0; e < e1; ++e) { C[e] = __builtin_amdgcn_exp2f(C[e]); PIN(C[e]); }
        }
        SBAR();
    }
    if (__builtin_expect(resc, 0)) {
#pragma unroll
        for (int d = 0; d < 4; ++d)
#pragma unroll
            for (int r = 0; r < 16; ++r) o[d][r] *= f;
    }
}
constexpr int K2_LDS = 163840;
constexpr float QSCALE = 0.08838834764831845f * 1.4426950408889634f;
__global__ __launch_bounds__(512, 2) void k2_attn(const u16* __restrict__ wq, char* __restrict__ ws) {
    extern __shared__ __attribute__((aligned(16))) char lds[];
    const int b = blockIdx.x, tid = threadIdx.x, l = tid & 63, r32 = l & 31, hi = l >> 5;
    const int w = __builtin_amdgcn_readfirstlane(tid >> 6);
    const int xcd = b & 7, kk = b >> 3, head = xcd + 8 * (kk >> 3), pi = kk & 7;
    const int qb = (w < 4) ? pi : 15 - pi, sub = w & 3, rb = qb * 4 + sub;
    const int tl = 2 * qb + (sub >> 1), NT = 32 - 2 * pi;
    const unsigned lds0 = (unsigned)(uintptr_t)lds;
    STAMP_AT(ws, 1, 0);
    const char* kvh = ws + WS_KV + ((size_t)head << 20);
    const char* ksrc = kvh + w * 2048 + l * 16;
    const unsigned kdst = lds0 + w * 2048, vdst = lds0 + 65536 + w * 2048;
#define DMA_K(t_, slot_) do { const char* g_ = ksrc + (size_t)(t_) * 32768; glds16(g_, kdst + (slot_)); glds16(g_ + 1024, kdst + (slot_) + 1024); } while (0)
#define DMA_V(t_, slot_) do { const char* g_ = ksrc + (size_t)(t_) * 32768 + 16384; glds16(g_, vdst + (slot_)); glds16(g_ + 1024, vdst + (slot_) + 1024); } while (0)
    DMA_K(0, 0);
    glds16(ws + WS_GS + (w & 3) * 1024 + l * 16, lds0 + 147456 + (w & 3) * 1024);
    u32x4 xf[16];
    const char* xsrc = ws + WS_XH + ((size_t)(rb * 32) * 64 + l) * 16;
#define XISSUE(c) do { _Pragma("unroll") for (int k8 = 0; k8 < 8; ++k8) asm volatile("global_load_dwordx4 %0, %1, off" : "=v"(xf[8 * ((c) & 1) + k8]) : "v"(xsrc + (8 * (c) + k8) * 1024) : "memory"); } while (0)
#define WISSUE(c) do { _Pragma("unroll") for (int n = 0; n < 4; ++n) { const int P = (4 * (c) + n) * 8 + w, row = 4 * P + (l >> 4), cc = (l & 15) ^ ((row & 3) << 2); \
            glds16(wq + (size_t)row * 4096 + head * 128 + cc * 8, lds0 + 16384 + P * 1024); } } while (0)
    XISSUE(0); WISSUE(0); XISSUE(1); WISSUE(1);
    const int q4 = (l & 15) >> 2, p4 = l & 3, blk = (l >> 4) & 1;
    const lds_cptr gsp = (lds_cptr)lds + 147456 + 32 * hi;
    lds_cptr wp[4];
#pragma unroll
    for (int db = 0; db < 4; ++db) wp[db] = (lds_cptr)lds + 16384 + (8 * hi + q4) * 256 + 64 * (db ^ q4) + 32 * blk + 16 * (p4 >> 1) + 8 * (p4 & 1);
    bf16x8 qf[8];
    {
        f32x16 qa[4];
#pragma unroll
        for (int d = 0; d < 4; ++d) qa[d] = f32x16{};
#define XWAIT(N, c) asm volatile("s_waitcnt vmcnt(" #N ") lgkmcnt(0)\n\ts_barrier" : "+v"(xf[8 * c]), "+v"(xf[8 * c + 1]), "+v"(xf[8 * c + 2]), "+v"(xf[8 * c + 3]), \
                                 "+v"(xf[8 * c + 4]), "+v"(xf[8 * c + 5]), "+v"(xf[8 * c + 6]), "+v"(xf[8 * c + 7]) :: "memory")
#define XONE(c, k8) asm volatile("global_load_dwordx4 %0, %1, off" : "=v"(xf[8 * ((c) & 1) + (k8)]) : "v"(xsrc + (8 * (c) + (k8)) * 1024) : "memory")
#define WONE(c, n) do { const int P = (4 * (c) + (n)) * 8 + w, row = 4 * P + (l >> 4), cc = (l & 15) ^ ((row & 3) << 2); \
            glds16(wq + (size_t)row * 4096 + head * 128 + cc * 8, lds0 + 16384 + P * 1024); } while (0)
#define KVONE(off_g, dst_) glds16(ksrc + (off_g), (dst_))
#define LDF4(p_) (*(const __attribute__((address_space(3))) f32x4*)(p_))
#define HPART(j_, G_, S_, e_) pk2(fmaf(bflo(xw[j_]), G_[e_], S_[e_]), fmaf(bfhi(xw[j_]), G_[e_ + 1], S_[e_ + 1]))
#pragma unroll
        for (int c = 0; c < 4; ++c) {
            STAMPX(ws, 2 * c);
            if (c == 0) { XWAIT(12, 0); STAMP_AT(ws, 1, 1); }
            if (c == 1) { XWAIT(4, 1); }
            if (c == 2) { XWAIT(8, 0); }
            if (c == 3) { XWAIT(2, 1); }
            STAMPX(ws, 2 * c + 1);
            bf16x8 wf[2][4]; f32x4 g0, g1, s0, s1; u32x4 hc;
            {
                const int ks = 8 * c;
                g0 = LDF4(gsp + 64 * ks); g1 = LDF4(gsp + 64 * ks + 16); s0 = LDF4(gsp + 2048 + 64 * ks); s1 = LDF4(gsp + 2048 + 64 * ks + 16);
#pragma unroll
                for (int db = 0; db < 4; ++db) wf[0][db] = wfrag(wp[db] + ks * 4096, wp[db] + ks * 4096 + 1024);
                const u32x4 xw = xf[8 * (c & 1)];
                hc = (u32x4){HPART(0, g0, s0, 0), HPART(1, g0, s0, 2), HPART(2, g1, s1, 0), HPART(3, g1, s1, 2)};
                g0 = LDF4(gsp + 64 * (ks + 1)); g1 = LDF4(gsp + 64 * (ks + 1) + 16); s0 = LDF4(gsp + 2048 + 64 * (ks + 1)); s1 = LDF4(gsp + 2048 + 64 * (ks + 1) + 16);
            }
            SBAR();
#pragma unroll
            for (int k8 = 0; k8 < 8; ++k8) {
                const int ks = 8 * c + k8;
                const bf16x8 hf = __builtin_bit_cast(bf16x8, hc);
                u32x4 hn = hc;
                const u32x4 xw = xf[8 * (c & 1) + (k8 < 7 ? k8 + 1 : k8)];
#pragma unroll
                for (int db = 0; db < 4; ++db) {
                    qa[db] = MFMA(wf[k8 & 1][db], hf, qa[db]);
                    {
                        const int ix = k8 * 4 + db;
                        if (c == 0) { if (ix < 4) WONE(2, ix); }
                        if (c == 1) { if (ix < 8) XONE(2, ix); else if (ix < 12) WONE(3, ix - 8);
                                      else if (ix < 16) KVONE((size_t)(1 + ((ix - 12) >> 1)) * 32768 + ((ix - 12) & 1) * 1024, kdst + 16384 * (1 + ((ix - 12) >> 1)) + ((ix - 12) & 1) * 1024); }
                        if (c == 2) { if (ix < 8) XONE(3, ix); else if (ix < 10) KVONE(16384 + (ix - 8) * 1024, vdst + (ix - 8) * 1024); }
                        if (c == 3) { if (ix < 2) KVONE(32768 + 16384 + ix * 1024, vdst + 16384 + ix * 1024); }
                    }
                    if (k8 < 7) {
                        wf[(k8 + 1) & 1][db] = wfrag(wp[db] + (ks + 1) * 4096, wp[db] + (ks + 1) * 4096 + 1024);
                        if (db == 0) hn[0] = HPART(0, g0, s0, 0);
                        if (db == 1) hn[1] = HPART(1, g0, s0, 2);
                        if (db == 2) hn[2] = HPART(2, g1, s1, 0);
                        if (db == 3) { hn[3] = HPART(3, g1, s1, 2); }
                        PIN(hn);
                        if (db == 1 && k8 < 6) { g0 = LDF4(gsp + 64 * (ks + 2)); s0 = LDF4(gsp + 2048 + 64 * (ks + 2)); }
                        if (db == 3 && k8 < 6) { g1 = LDF4(gsp + 64 * (ks + 2) + 16); s1 = LDF4(gsp + 2048 + 64 * (ks + 2) + 16); }
                    }
                    SBAR();
                }
                hc = hn;
            }
        }
#undef XWAIT
#undef XISSUE
#undef WISSUE
#undef HPART
#undef XONE
#undef WONE
#undef KVONE
#pragma unroll
        for (int db = 0; db < 4; ++db) {
#pragma unroll
            for (int r = 0; r < 16; ++r) qa[db][r] *= QSCALE;
            qf[2 * db] = pack8(qa[db], 0); qf[2 * db + 1] = pack8(qa[db], 1);
        }
    }
    STAMP_AT(ws, 1, 2); STAMPX(ws, 8);
#define WAIT_BAR4() asm volatile("s_waitcnt vmcnt(4)\n\ts_barrier" ::: "memory")
    float l_run = 0.f;
    f32x16 o[4], negm;
#pragma unroll
    for (int d = 0; d < 4; ++d) o[d] = f32x16{};
    const int qrow = rb * 32 + r32;
    const lds_cptr kp0 = (lds_cptr)lds + l * 16, vp0 = (lds_cptr)lds + 65536 + l * 16;
    f32x16 A0, A1, B0, B1;
    bf16x8 pf0, pf1, pf2, pf3, fr[8];
    int sl_prev = 49152, sl_cur = 0;
#define ROT() do { sl_prev = sl_cur; sl_cur = (sl_cur + 16384) & 65535; } while (0)
    const int tsw = 2 * pi + 3;
    const bool split = pi <= 6;
    const int tdma = split ? NT : tsw;
    const char* hsrc = kvh + (w & 3) * 4096 + l * 16;
    const unsigned hk = lds0 + (w & 3) * 4096, hv = lds0 + 65536 + (w & 3) * 4096;
#define WAITB(t_) do { if ((t_) < tdma) WAIT_BAR4(); else asm volatile("s_waitcnt vmcnt(0)\n\ts_barrier" ::: "memory"); } while (0)
#define STAGE_ISSUE(t_) do { if ((t_) < tdma) { const int tk_ = (t_) + 3 < NT ? (t_) + 3 : NT - 1, tv_ = (t_) + 2 < NT ? (t_) + 2 : NT - 1; DMA_K(tk_, sl_prev); DMA_V(tv_, (sl_cur + 32768) & 65535); } } while (0)
#define STAGE_HELP(t_) do { const int tk_ = (t_) + 3 < NT ? (t_) + 3 : NT - 1, tv_ = (t_) + 2 < NT ? (t_) + 2 : NT - 1; \
        const char* gk_ = hsrc + (size_t)tk_ * 32768; const char* gv_ = hsrc + (size_t)tv_ * 32768 + 16384; const unsigned sk_ = hk + sl_prev, sv_ = hv + ((sl_cur + 32768) & 65535); \
        glds16(gk_, sk_); glds16(gk_ + 1024, sk_ + 1024); glds16(gk_ + 2048, sk_ + 2048); glds16(gk_ + 3072, sk_ + 3072); \
        glds16(gv_, sv_); glds16(gv_ + 1024, sv_ + 1024); glds16(gv_ + 2048, sv_ + 2048); glds16(gv_ + 3072, sv_ + 3072); } while (0)
#define KPN() (kp0 + ((sl_cur + 16384) & 65535))
#define DRAIN_FULL(X0, X1) do { float sacc_ = 0.f; \
        _Pragma("unroll") for (int r = 0; r < 16; ++r) { X1[r] = __builtin_amdgcn_exp2f(X1[r]); sacc_ += X0[r] + X1[r]; } \
        l_run += sacc_; pf0 = pack8(X0, 0); pf1 = pack8(X0, 1); pf2 = pack8(X1, 0); pf3 = pack8(X1, 1); const lds_cptr vp_ = vp0 + sl_prev; \
        _Pragma("unroll") for (int d = 0; d < 4; ++d) { o[d] = MFMA(ldsb128(vp_ + (d * 4 + 0) * 1024), pf0, o[d]); o[d] = MFMA(ldsb128(vp_ + (d * 4 + 1) * 1024), pf1, o[d]); \
            o[d] = MFMA(ldsb128(vp_ + (d * 4 + 2) * 1024), pf2, o[d]); o[d] = MFMA(ldsb128(vp_ + (d * 4 + 3) * 1024), pf3, o[d]); } } while (0)
#define STORE_O(rb_, inv_) do { char* cb_ = ws + WS_CTX + ((size_t)((rb_) * 256 + head * 8) * 64 + l) * 16; \
        _Pragma("unroll") for (int d = 0; d < 4; ++d) { _Pragma("unroll") for (int r = 0; r < 16; ++r) o[d][r] *= (inv_); \
            *(bf16x8*)(cb_ + (2 * d) * 1024) = pack8(o[d], 0); *(bf16x8*)(cb_ + (2 * d + 1) * 1024) = pack8(o[d], 1); } } while (0)
    asm volatile("s_waitcnt vmcnt(2) lgkmcnt(0)\n\ts_barrier" ::: "memory");
    if (split && w >= 4) {
#pragma unroll
        for (int ds = 0; ds < 8; ++ds) *(bf16x8*)(lds + 131072 + sub * 8192 + ds * 1024 + l * 16) = qf[ds];
    }
    STAGE_ISSUE(0);
    {
        A0 = f32x16{}; A1 = f32x16{};
#pragma unroll
        for (int ds = 0; ds < 8; ++ds) {
            A0 = MFMA(ldsb128(kp0 + ds * 1024), qf[ds], A0);
            A1 = MFMA(ldsb128(kp0 + 8192 + ds * 1024), qf[ds], A1);
        }
        if (tl == 0) att_mask(A0, A1, qrow - 4 * hi);
        const float mx = half_swap_max(att_rowmax(A0, A1));
#pragma unroll
        for (int r = 0; r < 16; ++r) { A0[r] = __builtin_amdgcn_exp2f(A0[r] - mx); A1[r] = A1[r] - mx; negm[r] = -mx; }
#pragma unroll
        for (int g = 0; g < PD; ++g) fr[g] = ldsb128(kp0 + 16384 + kfrag_off(g));
    }
    asm volatile("s_waitcnt vmcnt(4) lgkmcnt(0)\n\ts_barrier" ::: "memory"); ROT();
    const bool lsplit = split && w >= 4;
    const int tfull = lsplit ? tsw : tl;
    int t = 1;
    for (; t + 1 < tfull; t += 2) {
        STAGE_ISSUE(t);     att_step<false>(B0, B1, A0, A1, o, negm, l_run, qf, fr, kp0 + sl_cur, vp0 + sl_prev, KPN(), 0); WAITB(t); ROT();
        STAGE_ISSUE(t + 1); att_step<false>(A0, A1, B0, B1, o, negm, l_run, qf, fr, kp0 + sl_cur, vp0 + sl_prev, KPN(), 0); WAITB(t + 1); ROT();
    }
    if (t < tfull) {
        STAGE_ISSUE(t); att_step<false>(B0, B1, A0, A1, o, negm, l_run, qf, fr, kp0 + sl_cur, vp0 + sl_prev, KPN(), 0); WAITB(t); ROT();
        A0 = B0; A1 = B1; ++t;
    }
    if (!lsplit) {
        if (tl >= 1) {
            STAGE_ISSUE(t); att_step<true>(B0, B1, A0, A1, o, negm, l_run, qf, fr, kp0 + sl_cur, vp0 + sl_prev, KPN(), qrow - t * 64 - 4 * hi); WAITB(t); ROT();
            ++t;
        } else { B0 = A0; B1 = A1; }
        if (t < NT) STAGE_ISSUE(t);
        DRAIN_FULL(B0, B1);
        STAMP_AT(ws, 1, 3);
        { const float inv = 1.f / half_swap_sum(l_run); STORE_O(rb, inv); }
        if (t < NT) { WAIT_ALL_BAR(); ROT(); ++t; }
        const int tstop = split ? tsw : NT;
        for (; t < tstop && t < tdma; ++t) { STAGE_ISSUE(t); WAIT_BAR4(); ROT(); }
        if (!split) {
            if (w < 4) { for (; t < NT; ++t) { STAGE_HELP(t); asm volatile("s_waitcnt vmcnt(8)\n\ts_barrier" ::: "memory"); ROT(); } }
            else { for (; t < NT; ++t) { WAIT_ALL_BAR(); ROT(); } }
        }
    }
    if (split) {
        const int kb = w >= 4 ? 0 : 1, rbl = (15 - pi) * 4 + sub, tlL = 2 * (15 - pi) + (sub >> 1);
        const bool msk = (sub & 1) == kb;
        const int tlh = (kb == 1 && !(sub & 1)) ? tlL - 1 : tlL;
        const int dqb = rbl * 32 + r32 - 32 * kb - 4 * hi;
        const lds_cptr kph = kp0 + kb * 8192, vph = vp0 + kb * 2048;
        STAGE_ISSUE(t);
        if (kb == 0) { DRAIN_FULL(A0, A1); }
        else {
#pragma unroll
            for (int ds = 0; ds < 8; ++ds) qf[ds] = ldsb128((lds_cptr)lds + 131072 + sub * 8192 + ds * 1024 + l * 16);
#pragma unroll
            for (int d = 0; d < 4; ++d) o[d] = f32x16{};
            l_run = 0.f; negm = f32x16{};
        }
        {
            A0 = negm;
#pragma unroll
            for (int ds = 0; ds < 8; ++ds) A0 = MFMA(ldsb128(kph + sl_cur + ds * 1024), qf[ds], A0);
            float rm = A0[0];
#pragma unroll
            for (int r = 1; r < 16; ++r) rm = __builtin_fmaxf(rm, A0[r]);
            rm = half_swap_max(rm);
            const float dl = kb == 1 ? rm : (rm > ATT_THR ? rm : 0.f);
            const float f = kb == 1 ? 1.f : __builtin_amdgcn_exp2f(-dl);
            l_run *= f;
#pragma unroll
            for (int r = 0; r < 16; ++r) { A0[r] -= dl; negm[r] -= dl; }
#pragma unroll
            for (int d = 0; d < 4; ++d)
#pragma unroll
                for (int r = 0; r < 16; ++r) o[d][r] *= f;
#pragma unroll
            for (int r = 0; r < 8; ++r) A0[r] = __builtin_amdgcn_exp2f(A0[r]);
#pragma unroll
            for (int g = 0; g < PD; ++g) fr[g] = ldsb128(kph + ((sl_cur + 16384) & 65535) + g * 1024);
        }
        WAIT_BAR4(); ROT(); ++t;
        const int thf = msk ? tlh : tlh + 1;
#define KPNH() (kph + ((sl_cur + 16384) & 65535))
        for (; t + 1 < thf; t += 2) {
            STAGE_ISSUE(t);     att_hstep<false>(B0, A0, o, negm, l_run, qf, fr, kph + sl_cur, vph + sl_prev, KPNH(), 0); WAIT_BAR4(); ROT();
            STAGE_ISSUE(t + 1); att_hstep<false>(A0, B0, o, negm, l_run, qf, fr, kph + sl_cur, vph + sl_prev, KPNH(), 0); WAIT_BAR4(); ROT();
        }
        if (t < thf) {
            STAGE_ISSUE(t); att_hstep<false>(B0, A0, o, negm, l_run, qf, fr, kph + sl_cur, vph + sl_prev, KPNH(), 0); WAIT_BAR4(); ROT();
            A0 = B0; ++t;
        }
        if (msk) {
            STAGE_ISSUE(t); att_hstep<true>(B0, A0, o, negm, l_run, qf, fr, kph + sl_cur, vph + sl_prev, KPNH(), dqb - t * 64); WAIT_BAR4(); ROT();
            A0 = B0; ++t;
        }
        if (t < NT) STAGE_ISSUE(t);
        {
            float sacc = 0.f;
#pragma unroll
            for (int r = 0; r < 16; ++r) { if (r >= 8) A0[r] = __builtin_amdgcn_exp2f(A0[r]); sacc += A0[r]; }
            l_run += sacc;
            pf0 = pack8(A0, 0); pf1 = pack8(A0, 1);
            const lds_cptr vp = vph + sl_prev;
#pragma unroll
            for (int d = 0; d < 4; ++d) { o[d] = MFMA(ldsb128(vp + (d * 4) * 1024), pf0, o[d]); o[d] = MFMA(ldsb128(vp + (d * 4 + 1) * 1024), pf1, o[d]); }
        }
        for (; t < NT; ++t) { if (t > tlh + 1) STAGE_ISSUE(t); WAIT_BAR4(); ROT(); }
        WAIT_ALL_BAR();
        float* mg = (float*)lds + sub * (66 * 64) + l;
        const float lsum = half_swap_sum(l_run);
        if (kb == 1) {
#pragma unroll
            for (int d = 0; d < 4; ++d)
#pragma unroll
                for (int r = 0; r < 16; ++r) mg[(d * 16 + r) * 64] = o[d][r];
            mg[64 * 64] = negm[0]; mg[65 * 64] = lsum;
        }
        BAR_LGKM();
        if (kb == 0) {
            const float nmh = mg[64 * 64], lh = mg[65 * 64], nml = negm[0];
            const float nm = __builtin_fminf(nmh, nml);
            const float fl = __builtin_amdgcn_exp2f(nm - nml), fh = __builtin_amdgcn_exp2f(nm - nmh);
            const float inv = 1.f / (lsum * fl + lh * fh);
            const float cl = fl * inv, ch = fh * inv;
#pragma unroll
            for (int d = 0; d < 4; ++d)
#pragma unroll
                for (int r = 0; r < 16; ++r) o[d][r] = o[d][r] * cl + mg[(d * 16 + r) * 64] * ch;
            STORE_O(rb, 1.f);
        }
    }
    asm volatile("s_waitcnt vmcnt(0)" ::: "memory");
    STAMP_AT(ws, 1, 4);
#undef DMA_K
#undef DMA_V
#undef ROT
#undef STAGE_ISSUE
#undef STAGE_HELP
#undef WAITB
#undef KPN
#undef KPNH
#undef DRAIN_FULL
#undef STORE_O
}

template <int TQ, int TN, int KW, int BKS, bool APERM, bool CT>
struct G {
    static constexpr int NWAVE = TQ * TN * KW, RB = TN * 128, A_BYTES = 2 * TQ * BKS * 1024, W_BYTES = 16 * BKS * RB, STAGE = A_BYTES + W_BYTES;
    static constexpr int CPR = RB / 16;
    static_assert(STAGE == 32768 && (2 * TQ * BKS + W_BYTES / 1024) / NWAVE == 4, "4 glds per wave per stage, 4 x 32 KB ring");
    static DI int swz(int row) { return RB == 256 ? ((row & 3) << 2) : (((row >> 1) & 1) << 2); }
    static DI void stage(const char* AF, size_t a_rb_stride, const u16* Wp, int ldw, int kst, unsigned ldsaddr, int w, int l) {
#pragma unroll
        for (int p0 = 0; p0 < 2 * TQ * BKS; p0 += NWAVE) {
            const int p = p0 + w, rbi = p / BKS, ks = p % BKS;
            glds16(AF + rbi * a_rb_stride + (size_t)(kst * BKS + ks) * 1024 + l * 16, ldsaddr + p * 1024);
        }
#pragma unroll
        for (int p0 = 0; p0 < W_BYTES / 1024; p0 += NWAVE) {
            const int p = p0 + w, row = p * (1024 / RB) + l / CPR, c = (l % CPR) ^ swz(row);
            glds16(Wp + (size_t)(kst * BKS * 16 + row) * ldw + c * 8, ldsaddr + A_BYTES + p * 1024);
        }
    }
    static constexpr int NPA = 2 * TQ * BKS / NWAVE, NPW = W_BYTES / 1024 / NWAVE;
    static DI void stage_piece(int i, const char* AF, size_t a_rb_stride, const u16* Wp, int ldw, int kst, unsigned ldsaddr, int w, int l) {
        if (i < NPA) {
            const int p = i * NWAVE + w, rbi = p / BKS, ks = p % BKS;
            glds16(AF + rbi * a_rb_stride + (size_t)(kst * BKS + ks) * 1024 + l * 16, ldsaddr + p * 1024);
        } else {
            const int p = (i - NPA) * NWAVE + w, row = p * (1024 / RB) + l / CPR, c = (l % CPR) ^ swz(row);
            glds16(Wp + (size_t)(kst * BKS * 16 + row) * ldw + c * 8, ldsaddr + A_BYTES + p * 1024);
        }
    }
    template <bool RM, class EPI>
    static DI void run(EPI epi, const char* AF, size_t a_rb_stride, const u16* Wp, int ldw, int nstage, int rot, char* lds, float (&red)[64 / KW], int& tq, int& tn, int& kw, char* ws, int kid) {
        const int tid = threadIdx.x, l = tid & 63, hi = l >> 5;
        STAMP_AT(ws, kid, 0);
        const int w = __builtin_amdgcn_readfirstlane(tid >> 6);
        kw = w % KW; const int tt = w / KW; tq = tt / TN; tn = tt % TN;
        const unsigned lds0 = (unsigned)(uintptr_t)lds;
        const int q4 = (l & 15) >> 2, p4 = l & 3, blk = (l >> 4) & 1;
        f32x16 acc[2][2];
#pragma unroll
        for (int i = 0; i < 2; ++i) { acc[i][0] = f32x16{}; acc[i][1] = f32x16{}; }
        int kst = rot;
#define NEXTK() do { kst = (kst + 1 == nstage) ? 0 : kst + 1; } while (0)
        stage(AF, a_rb_stride, Wp, ldw, kst, lds0, w, l); NEXTK();
        stage(AF, a_rb_stride, Wp, ldw, kst, lds0 + STAGE, w, l); NEXTK();
        stage(AF, a_rb_stride, Wp, ldw, kst, lds0 + 2 * STAGE, w, l); NEXTK();
        for (int st = 0; st < nstage; ++st) {
            if (st + 2 < nstage) asm volatile("s_waitcnt vmcnt(8) lgkmcnt(0)\n\ts_barrier" ::: "memory");
            else if (st + 1 < nstage) asm volatile("s_waitcnt vmcnt(4) lgkmcnt(0)\n\ts_barrier" ::: "memory");
            else WAIT_ALL_BAR();
            if (st == 0) STAMP_AT(ws, kid, 1);
            const bool pre = st + 3 < nstage; const unsigned pdst = lds0 + ((st + 3) & 3) * STAGE;
            lds_cptr ab = (lds_cptr)lds + (st & 3) * STAGE;
            lds_cptr wb = ab + A_BYTES;
#pragma unroll
            for (int kk = 0; kk < BKS / KW; ++kk) {
                const int ks = kw * (BKS / KW) + kk;
                bf16x8 af[2], wf[2];
#pragma unroll
                for (int i = 0; i < 2; ++i) af[i] = ldsb128(ab + ((2 * tq + i) * BKS + ks) * 1024 + l * 16);
#pragma unroll
                for (int j = 0; j < 2; ++j) {
                    const int r0 = 16 * ks + (APERM ? 4 * hi : 8 * hi) + q4, r1 = r0 + (APERM ? 8 : 4);
                    const int ch = 4 * (2 * tn + j) + 2 * blk + (p4 >> 1);
                    wf[j] = wfrag(wb + r0 * RB + ((ch ^ swz(r0)) << 4) + 8 * (p4 & 1), wb + r1 * RB + ((ch ^ swz(r1)) << 4) + 8 * (p4 & 1));
                }
#pragma unroll
                for (int i = 0; i < 2; ++i)
#pragma unroll
                    for (int j = 0; j < 2; ++j) {
                        if (CT) acc[i][j] = MFMA(wf[j], af[i], acc[i][j]);
                        else acc[i][j] = MFMA(af[i], wf[j], acc[i][j]);
                        constexpr int PER = 4 / (BKS / KW);
                        const int m = i * 2 + j;
                        if (PER == 4) { SBAR(); if (pre) stage_piece(m, AF, a_rb_stride, Wp, ldw, kst, pdst, w, l); SBAR(); }
                        else if (m & 1) { SBAR(); if (pre) stage_piece(kk * 2 + (m >> 1), AF, a_rb_stride, Wp, ldw, kst, pdst, w, l); SBAR(); }
                    }
            }
            if (pre) NEXTK();
        }
        BAR_LGKM();
        STAMP_AT(ws, kid, 2);
        float* rb_ = (float*)lds;
        constexpr int RS = RM ? 68 : 64;
#pragma unroll
        for (int i = 0; i < 2; ++i)
#pragma unroll
            for (int j = 0; j < 2; ++j)
#pragma unroll
                for (int r = 0; r < 16; ++r) rb_[(w * 64 + (i * 2 + j) * 16 + r) * RS + l] = acc[i][j][r];
        BAR_LGKM();
        if constexpr (RM) {
            constexpr int C8 = TN * 8, NITEM = 64 * TQ * C8 / (64 * NWAVE);
#pragma unroll
            for (int it = 0; it < NITEM; ++it) {
                const int id = it * (64 * NWAVE) + tid, row = id / C8, c8 = id % C8;
                const int rr = row & 31, r = (rr & 3) + 4 * (rr >> 3), h = (rr >> 2) & 1;
                const int t2 = (row >> 6) * TN + (c8 >> 3), reg = (((row >> 5) & 1) * 2 + ((c8 >> 2) & 1)) * 16 + r, ln = (c8 & 3) * 8 + 32 * h;
                f32x4 a0 = {0.f, 0.f, 0.f, 0.f}, a1 = a0;
#pragma unroll
                for (int k2 = 0; k2 < KW; ++k2) {
                    const float* q_ = rb_ + ((t2 * KW + k2) * 64 + reg) * RS + ln;
                    a0 += *(const f32x4*)q_; a1 += *(const f32x4*)(q_ + 4);
                }
                epi(row, c8 * 8, a0, a1);
            }
        } else {
#pragma unroll
            for (int e = 0; e < 64 / KW; ++e) {
                float s = 0.f;
#pragma unroll
                for (int k2 = 0; k2 < KW; ++k2) s += rb_[((tt * KW + k2) * 64 + kw * (64 / KW) + e) * RS + l];
                red[e] = s;
            }
        }
        STAMP_AT(ws, kid, 3);
    }
};

typedef G<2, 2, 2, 4, true, false> G3;
constexpr int K3_LDS = 139264;
__global__ __launch_bounds__(512, 2) void k3_oproj(const u16* __restrict__ wo, char* __restrict__ ws) {
    extern __shared__ __attribute__((aligned(16))) char lds[];
    const int b = blockIdx.x, xcd = b & 7, r = b >> 3, ksl = xcd & 3, rp = (xcd >> 2) * 8 + (r >> 2), ct = r & 3;
    float red[32]; int tq, tn, kw;
    u16* part = (u16*)(ws + WS_PART) + (size_t)ksl * 2048 * 512 + (size_t)(rp * 128) * 512 + ct * 128;
    G3::run<true>([&](int row, int col, f32x4 a0, f32x4 a1) {
                      u32x4 v = {pk2(a0[0], a0[1]), pk2(a0[2], a0[3]), pk2(a1[0], a1[1]), pk2(a1[2], a1[3])};
                      *(u32x4*)(part + (size_t)row * 512 + col) = v; },
                  ws + WS_CTX + ((size_t)(rp * 4) * 256 + ksl * 64) * 1024, (size_t)256 * 1024, wo + (size_t)ksl * 1024 * 512 + ct * 128, 512, 16, (4 * ct + 2 * (r >> 2)) & 15, lds, red, tq, tn, kw, ws, 2);
    STAMP_AT(ws, 2, 4);
}

__global__ __launch_bounds__(512) void k3b_ln2(const u16* __restrict__ x, char* __restrict__ ws) {
    const int l = threadIdx.x & 63, row = blockIdx.x * 8 + (threadIdx.x >> 6);
    const u16* part = (const u16*)(ws + WS_PART) + (size_t)row * 512 + 8 * l;
    const float* mod = (const float*)(ws + WS_MOD);
    u32x4 pv[4];
#pragma unroll
    for (int s = 0; s < 4; ++s) pv[s] = *(const u32x4*)(part + (size_t)s * 2048 * 512);
    const u32x4 xv4 = *(const u32x4*)(x + (size_t)row * 512 + 8 * l);
    const f32x4 g0 = *(const f32x4*)(mod + 1024 + 8 * l), g1 = *(const f32x4*)(mod + 1024 + 8 * l + 4);
    const f32x4 c0 = *(const f32x4*)(mod + 2048 + 8 * l), c1 = *(const f32x4*)(mod + 2048 + 8 * l + 4);
    const f32x4 s0 = *(const f32x4*)(mod + 1536 + 8 * l), s1 = *(const f32x4*)(mod + 1536 + 8 * l + 4);
    float x1[8], xh[8];
#pragma unroll
    for (int j = 0; j < 4; ++j) {
        const int w0 = j >> 1, w1 = 2 + (j >> 1);
        const float a0 = (j & 1) ? (bfhi(pv[0][w0]) + bfhi(pv[1][w0])) + (bfhi(pv[2][w0]) + bfhi(pv[3][w0])) : (bflo(pv[0][w0]) + bflo(pv[1][w0])) + (bflo(pv[2][w0]) + bflo(pv[3][w0]));
        const float a1 = (j & 1) ? (bfhi(pv[0][w1]) + bfhi(pv[1][w1])) + (bfhi(pv[2][w1]) + bfhi(pv[3][w1])) : (bflo(pv[0][w1]) + bflo(pv[1][w1])) + (bflo(pv[2][w1]) + bflo(pv[3][w1]));
        const float xa = (j & 1) ? bfhi(xv4[j >> 1]) : bflo(xv4[j >> 1]), xb = (j & 1) ? bfhi(xv4[2 + (j >> 1)]) : bflo(xv4[2 + (j >> 1)]);
        x1[j] = bfr(xa + bfr(g0[j] * bfr(a0)));
        x1[4 + j] = bfr(xb + bfr(g1[j] * bfr(a1)));
    }
    u32x4 o1 = {pk2(x1[0], x1[1]), pk2(x1[2], x1[3]), pk2(x1[4], x1[5]), pk2(x1[6], x1[7])};
    *(u32x4*)(ws + WS_X1 + ((size_t)row * 512 + 8 * l) * 2) = o1;
    ln_row(x1, xh);
    float h[8];
#pragma unroll
    for (int j = 0; j < 4; ++j) { h[j] = bfr(xh[j] * bfr(1.f + c0[j])) + s0[j]; h[4 + j] = bfr(xh[4 + j] * bfr(1.f + c1[j])) + s1[j]; }
    u32x4 oh = {pk2(h[0], h[1]), pk2(h[2], h[3]), pk2(h[4], h[5]), pk2(h[6], h[7])};
    *(u32x4*)(ws + WS_H2 + ((size_t)((row >> 5) * 32 + (l >> 1)) * 64 + (row & 31) + 32 * (l & 1)) * 16) = oh;
}

typedef G<2, 2, 2, 4, false, true> G4;
__global__ __launch_bounds__(512, 2) void k4_ffn1(const u16* __restrict__ w1, char* __restrict__ ws) {
    extern __shared__ __attribute__((aligned(16))) char lds[];
    const int b = blockIdx.x, xcd = b & 7, jj = b >> 3, rp = 4 * (xcd >> 1) + jj / 7, ct = 7 * (xcd & 1) + jj % 7;
    const int l = threadIdx.x & 63;
    float red[32]; int tq, tn, kw;
    G4::run<false>(0, ws + WS_H2 + (size_t)(rp * 4) * 32 * 1024, (size_t)32 * 1024, w1 + ct * 128, DFF, 8, (jj % 7 + 2 * (jj / 7)) & 7, lds, red, tq, tn, kw, ws, 3);
#pragma unroll
    for (int j = 0; j < 2; ++j)
#pragma unroll
        for (int s = 0; s < 2; ++s) {
            const int e = j * 16 + 8 * s;
            u32x4 v = {pk2(fmaxf(red[e], 0.f), fmaxf(red[e + 1], 0.f)), pk2(fmaxf(red[e + 2], 0.f), fmaxf(red[e + 3], 0.f)),
                       pk2(fmaxf(red[e + 4], 0.f), fmaxf(red[e + 5], 0.f)), pk2(fmaxf(red[e + 6], 0.f), fmaxf(red[e + 7], 0.f))};
            const int rbq = rp * 4 + 2 * tq + kw, kstep = (ct * 128 + 64 * tn + 32 * j) / 16 + s;
            *(u32x4*)(ws + WS_H1 + ((size_t)(rbq * 112 + kstep) * 64 + l) * 16) = v;
        }
    STAMP_AT(ws, 3, 4);
}

typedef G<1, 1, 8, 8, true, false> G5;
__global__ __launch_bounds__(512, 2) void k5_ffn2(const u16* __restrict__ w2, char* __restrict__ ws, u16* __restrict__ out) {
    extern __shared__ __attribute__((aligned(16))) char lds[];
    const int b = blockIdx.x, xcd = b & 7, jj = b >> 3, rp = 8 * (xcd >> 1) + (jj >> 2), ct = 4 * (xcd & 1) + (jj & 3);
    float red[8]; int tq, tn, kw;
    const float* gb = (const float*)(ws + WS_MOD) + 2560 + ct * 64;
    const u16* x1 = (const u16*)(ws + WS_X1) + (size_t)(rp * 64) * 512 + ct * 64;
    u16* op = out + (size_t)(rp * 64) * 512 + ct * 64;
    G5::run<true>([&](int row, int col, f32x4 a0, f32x4 a1) {
                      const u32x4 xv = *(const u32x4*)(x1 + (size_t)row * 512 + col);
                      const f32x4 g0 = *(const f32x4*)(gb + col), g1 = *(const f32x4*)(gb + col + 4);
                      u32x4 v = {pk2(bflo(xv[0]) + bfr(g0[0] * bfr(a0[0])), bfhi(xv[0]) + bfr(g0[1] * bfr(a0[1]))),
                                 pk2(bflo(xv[1]) + bfr(g0[2] * bfr(a0[2])), bfhi(xv[1]) + bfr(g0[3] * bfr(a0[3]))),
                                 pk2(bflo(xv[2]) + bfr(g1[0] * bfr(a1[0])), bfhi(xv[2]) + bfr(g1[1] * bfr(a1[1]))),
                                 pk2(bflo(xv[3]) + bfr(g1[2] * bfr(a1[2])), bfhi(xv[3]) + bfr(g1[3] * bfr(a1[3])))};
                      *(u32x4*)(op + (size_t)row * 512 + col) = v; },
                  ws + WS_H1 + (size_t)(rp * 2) * 112 * 1024, (size_t)112 * 1024, w2 + ct * 64, 512, 14, (3 * (jj & 3) + 2 * (jj >> 2)) % 14, lds, red, tq, tn, kw, ws, 4);
    STAMP_AT(ws, 4, 4);
}

extern "C" void kernel_launch(void* const* d_in, const int* in_sizes, int n_in, void* d_out, int out_size, void* d_ws, size_t ws_size,
                              hipStream_t stream) {
    (void)in_sizes; (void)n_in; (void)out_size; (void)ws_size;
    const u16* x = (const u16*)d_in[0];
    const u16* ckv = (const u16*)d_in[1];
    const u16* wq = (const u16*)d_in[2];
    const u16* wuk = (const u16*)d_in[3];
    const u16* wuv = (const u16*)d_in[4];
    const u16* wo = (const u16*)d_in[5];
    const u16* w1 = (const u16*)d_in[7];
    const u16* w2 = (const u16*)d_in[8];
    const u16* cvec = (const u16*)d_in[9];
    const u16* wmod = (const u16*)d_in[10];
    char* ws = (char*)d_ws;
    hipFuncSetAttribute((const void*)k1_kvproj, hipFuncAttributeMaxDynamicSharedMemorySize, K1_LDS);
    hipFuncSetAttribute((const void*)k2_attn, hipFuncAttributeMaxDynamicSharedMemorySize, K2_LDS);
    hipFuncSetAttribute((const void*)k3_oproj, hipFuncAttributeMaxDynamicSharedMemorySize, K3_LDS);
    hipFuncSetAttribute((const void*)k4_ffn1, hipFuncAttributeMaxDynamicSharedMemorySize, K3_LDS);
    hipFuncSetAttribute((const void*)k5_ffn2, hipFuncAttributeMaxDynamicSharedMemorySize, K3_LDS);
    k1_kvproj<<<256, 512, K1_LDS, stream>>>(ckv, wuk, wuv, x, cvec, wmod, ws);
    k2_attn<<<256, 512, K2_LDS, stream>>>(wq, ws);
    k3_oproj<<<256, 512, K3_LDS, stream>>>(wo, ws);
    k3b_ln2<<<256, 512, 0, stream>>>(x, ws);
    k4_ffn1<<<224, 512, K3_LDS, stream>>>(w1, ws);
    k5_ffn2<<<256, 512, K3_LDS, stream>>>(w2, ws, (u16*)d_out);
}
```

```cpp
#include <hip/hip_runtime.h>
#include <hip/hip_bf16.h>
#include <stdint.h>

typedef unsigned short u16;
typedef short bf16x8 __attribute__((ext_vector_type(8)));
typedef short s16x4 __attribute__((ext_vector_type(4)));
typedef short v4i16_t __attribute__((ext_vector_type(4)));
typedef float f32x16 __attribute__((ext_vector_type(16)));
typedef float f32x4 __attribute__((ext_vector_type(4)));
typedef float f32x2 __attribute__((ext_vector_type(2)));
typedef unsigned u32x4 __attribute__((ext_vector_type(4)));
typedef __bf16 bf16x2_t __attribute__((ext_vector_type(2)));
typedef __attribute__((address_space(3))) const char* lds_cptr;
#define DI __device__ __forceinline__
#define MFMA(a, b, c) __builtin_amdgcn_mfma_f32_32x32x16_bf16((a), (b), (c), 0, 0, 0)
#define SBAR() __builtin_amdgcn_sched_barrier(0)
#define STAMPX(ws_, idx) do { } while (0)
#define STAMP_AT(ws_, kid, idx) do { } while (0)

constexpr int SEQ = 2048, DM = 512, NH = 32, HD = 128, DFF = 1792;

constexpr size_t WS_KV = 0;
constexpr size_t WS_XH = WS_KV + (size_t)32 * 32 * 32768;
constexpr size_t WS_MOD = WS_XH + (size_t)64 * 32 * 1024;
constexpr size_t WS_GS = WS_MOD + 12288;
constexpr size_t WS_CTX = WS_MOD + 16384;
constexpr size_t WS_PART = WS_CTX + (size_t)64 * 256 * 1024;
constexpr size_t WS_X1 = WS_PART + (size_t)4 * 2048 * 512 * 4;
constexpr size_t WS_H2 = WS_X1 + (size_t)2048 * 512 * 2;
constexpr size_t WS_H1 = WS_H2 + (size_t)64 * 32 * 1024;

DI unsigned pk2(float a, float b) { f32x2 v = {a, b}; bf16x2_t r = __builtin_convertvector(v, bf16x2_t); return __builtin_bit_cast(unsigned, r); }
DI float bf2f(u16 v) { return __uint_as_float(((unsigned)v) << 16); }
DI float bfr(float x) { return __uint_as_float(pk2(x, 0.f) << 16); }
DI float bflo(unsigned w) { return __uint_as_float(w << 16); }
DI float bfhi(unsigned w) { return __uint_as_float(w & 0xffff0000u); }
DI bf16x8 pack8(const f32x16& a, int s) {
    u32x4 w = {pk2(a[8 * s], a[8 * s + 1]), pk2(a[8 * s + 2], a[8 * s + 3]), pk2(a[8 * s + 4], a[8 * s + 5]), pk2(a[8 * s + 6], a[8 * s + 7])};
    return __builtin_bit_cast(bf16x8, w);
}
DI int crow(int r, int hi) { return (r & 3) + 8 * (r >> 2) + 4 * hi; }
DI void glds16(const void* g, unsigned lds_base) {
    unsigned sv;
    asm volatile("s_mov_b32 %0, m0\n\ts_mov_b32 m0, %2\n\ts_nop 0\n\tglobal_load_lds_dwordx4 %1, off\n\ts_mov_b32 m0, %0"
                 : "=&s"(sv) : "v"(g), "s"(lds_base) : "memory");
}
DI bf16x8 ldsb128(lds_cptr p) { return *(const __attribute__((address_space(3))) bf16x8*)p; }
DI s16x4 ldstr(lds_cptr p) { return __builtin_bit_cast(s16x4, __builtin_amdgcn_ds_read_tr16_b64_v4i16((__attribute__((address_space(3))) v4i16_t*)p)); }
DI bf16x8 wfrag(lds_cptr p0, lds_cptr p1) { s16x4 lo = ldstr(p0), hi = ldstr(p1); return __builtin_shufflevector(lo, hi, 0, 1, 2, 3, 4, 5, 6, 7); }
#define WAIT_ALL_BAR() asm volatile("s_waitcnt vmcnt(0) lgkmcnt(0)\n\ts_barrier" ::: "memory")
#define BAR_LGKM() asm volatile("s_waitcnt lgkmcnt(0)\n\ts_barrier" ::: "memory")
DI float wave_sum(float v) {
#pragma unroll
    for (int m = 1; m < 64; m <<= 1) v += __shfl_xor(v, m);
    return v;
}
DI float half_swap_max(float m) { auto rr = __builtin_amdgcn_permlane32_swap(__float_as_uint(m), __float_as_uint(m), false, false); return fmaxf(__uint_as_float(rr[0]), __uint_as_float(rr[1])); }
DI float half_swap_sum(float m) { auto rr = __builtin_amdgcn_permlane32_swap(__float_as_uint(m), __float_as_uint(m), false, false); return __uint_as_float(rr[0]) + __uint_as_float(rr[1]); }

DI void ln_row(const float (&xv)[8], float (&o)[8]) {
    float s = 0.f;
#pragma unroll
    for (int j = 0; j < 8; ++j) s += xv[j];
    const float mean = wave_sum(s) * (1.f / 512.f);
    float q = 0.f;
#pragma unroll
    for (int j = 0; j < 8; ++j) { const float d = xv[j] - mean; q += d * d; }
    const float var = wave_sum(q) * (1.f / 512.f);
    const float mb = bfr(mean), vb = bfr(var);
    const float den = bfr(sqrtf(bfr(vb + 1e-5f)));
#pragma unroll
    for (int j = 0; j < 8; ++j) o[j] = bfr(bfr(xv[j] - mb) / den);
}

constexpr int K1_LDS = 131072;
template <bool VT>
DI void k1_gemm(const u16* __restrict__ ckv, const u16* __restrict__ W, char* __restrict__ kvimg, char* lds, int key0, int h0, int h1, int rot,
                 const u16* __restrict__ x, const u16* __restrict__ cvec, const u16* __restrict__ wmod, char* __restrict__ ws) {
    const int tid = threadIdx.x, l = tid & 63, r32 = l & 31, hi = l >> 5;
    const int w = __builtin_amdgcn_readfirstlane(tid >> 6);
    const int wk = w >> 2, wc = w & 3;
    const unsigned lds0 = (unsigned)(uintptr_t)lds;
    const u16* asrc[4]; const u16* wsrc[4];
#pragma unroll
    for (int n = 0; n < 4; ++n) {
        const int P = n * 8 + w, row = 8 * P + (l >> 3), c = (l & 7) ^ ((row >> 1) & 7);
        asrc[n] = ckv + (size_t)(key0 + row) * 512 + c * 8;
        const int half = P >> 4, rw = 4 * (P & 15) + (l >> 4), cw = (l & 15) ^ ((rw & 3) << 2);
        wsrc[n] = W + (size_t)rw * 4096 + (half ? h1 : h0) * 128 + cw * 8;
    }
#define K1_PIECE(i, kt_, bb_) do { if ((i) < 4) glds16(asrc[i] + (kt_) * 64, (bb_) + ((i) * 8 + w) * 1024); \
        else glds16(wsrc[(i) - 4] + (size_t)(kt_) * 64 * 4096, (bb_) + 32768 + (((i) - 4) * 8 + w) * 1024); } while (0)
    {
        const int b = blockIdx.x, row = 8 * b + w, half = tid & 1, rowi = tid >> 1;
        u32x4 xv4, wv0, wv1; unsigned cv0, cv1;
        asm volatile("global_load_dwordx4 %0, %1, off" : "=v"(xv4) : "v"(x + (size_t)row * 512 + 8 * l) : "memory");
        if (b < 64) {
            asm volatile("global_load_dwordx4 %0, %1, off" : "=v"(wv0) : "v"(wmod + (size_t)rowi * 3072 + 16 * b + 8 * half) : "memory");
            asm volatile("global_load_dwordx4 %0, %1, off" : "=v"(wv1) : "v"(wmod + (size_t)(rowi + 256) * 3072 + 16 * b + 8 * half) : "memory");
            asm volatile("global_load_ushort %0, %1, off" : "=v"(cv0) : "v"(cvec + rowi) : "memory");
            asm volatile("global_load_ushort %0, %1, off" : "=v"(cv1) : "v"(cvec + rowi + 256) : "memory");
        }
#pragma unroll
        for (int i = 0; i < 8; ++i) K1_PIECE(i, rot, lds0);
        float* scr = (float*)(lds + 65536);
        asm volatile("s_waitcnt vmcnt(8)" : "+v"(xv4) :: "memory");
        {
            float xv[8], o[8];
#pragma unroll
            for (int j = 0; j < 4; ++j) { xv[2 * j] = bflo(xv4[j]); xv[2 * j + 1] = bfhi(xv4[j]); }
            ln_row(xv, o);
            u32x4 ov = {pk2(o[0], o[1]), pk2(o[2], o[3]), pk2(o[4], o[5]), pk2(o[6], o[7])};
            *(u32x4*)(ws + WS_XH + ((size_t)((row >> 5) * 32 + (l >> 1)) * 64 + (row & 31) + 32 * (l & 1)) * 16) = ov;
        }
        if (b < 64) {
            asm volatile("" : "+v"(wv0), "+v"(wv1), "+v"(cv0), "+v"(cv1));
            float a8[8];
            const float c0 = bf2f((u16)cv0), c1 = bf2f((u16)cv1);
            const float sc0 = bfr(c0 / (1.f + __expf(-c0))), sc1 = bfr(c1 / (1.f + __expf(-c1)));
#pragma unroll
            for (int j = 0; j < 4; ++j) { a8[2 * j] = sc0 * bflo(wv0[j]) + sc1 * bflo(wv1[j]); a8[2 * j + 1] = sc0 * bfhi(wv0[j]) + sc1 * bfhi(wv1[j]); }
#pragma unroll
            for (int m = 2; m < 64; m <<= 1)
#pragma unroll
                for (int j = 0; j < 8; ++j) a8[j] += __shfl_xor(a8[j], m);
            if (l < 2) {
#pragma unroll
                for (int j = 0; j < 8; ++j) scr[(w * 2 + l) * 8 + j] = a8[j];
            }
            BAR_LGKM();
            if (tid < 16) {
                float s = 0.f;
#pragma unroll
                for (int ww = 0; ww < 8; ++ww) s += scr[(ww * 2 + (tid >> 3)) * 8 + (tid & 7)];
                const int col = 16 * b + tid; const float mv = bfr(s);
                if (col < 512) ((float*)(ws + WS_GS))[512 + col] = mv;
                else ((float*)(ws + WS_GS))[col - 512] = bfr(1.f + mv);
            }
        }
    }
    f32x16 acc[4][2];
#pragma unroll
    for (int a = 0; a < 4; ++a) { acc[a][0] = f32x16{}; acc[a][1] = f32x16{}; }
    const int q4 = (l & 15) >> 2, p4 = l & 3, blk = (l >> 4) & 1;
    const int xa = (l >> 1) & 7;
    STAMP_AT(kvimg, 0, 1);
    for (int kt = 0; kt < 8; ++kt) {
        WAIT_ALL_BAR();
        if (kt == 0) STAMP_AT(kvimg, 0, 2);
        const bool pre = kt + 1 < 8;
        const int ktn = (rot + kt + 1) & 7;
        const unsigned bb = lds0 + ((kt + 1) & 1) * 65536;
        lds_cptr ab = (lds_cptr)lds + (kt & 1) * 65536;
        lds_cptr wb = ab + 32768 + (wc >> 1) * 16384;
#pragma unroll
        for (int s = 0; s < 4; ++s) {
            bf16x8 af[4], wf[2];
#pragma unroll
            for (int kb = 0; kb < 4; ++kb) af[kb] = ldsb128(ab + (128 * wk + 32 * kb + r32) * 128 + (((2 * s + hi) ^ xa) << 4));
#pragma unroll
            for (int j = 0; j < 2; ++j) {
                const int db = 2 * (wc & 1) + j;
                lds_cptr p = wb + (16 * s + 8 * hi + q4) * 256 + 64 * (db ^ q4) + 32 * blk + 16 * (p4 >> 1) + 8 * (p4 & 1);
                wf[j] = wfrag(p, p + 1024);
            }
#pragma unroll
            for (int kb = 0; kb < 4; ++kb) {
#pragma unroll
                for (int j = 0; j < 2; ++j) {
                    if (VT) acc[kb][j] = MFMA(af[kb], wf[j], acc[kb][j]);
                    else acc[kb][j] = MFMA(wf[j], af[kb], acc[kb][j]);
                }
                if ((kb & 1) == 0) { if (pre) K1_PIECE(2 * s + (kb >> 1), ktn, bb); }
            }
        }
    }
#undef K1_PIECE
    STAMP_AT(kvimg, 0, 3);
    const int head = (wc >> 1) ? h1 : h0;
#pragma unroll
    for (int kb = 0; kb < 4; ++kb) {
        const int key = key0 + 128 * wk + 32 * kb, tile = key >> 6, kbl = kb & 1;
        char* tb = kvimg + ((size_t)(head * 32 + tile) << 15);
#pragma unroll
        for (int j = 0; j < 2; ++j) {
            const int db = 2 * (wc & 1) + j;
#pragma unroll
            for (int s2 = 0; s2 < 2; ++s2) {
                const int fo = VT ? (16384 + (db * 4 + kbl * 2 + s2) * 1024) : ((kbl * 8 + db * 2 + s2) * 1024);
                *(bf16x8*)(tb + fo + l * 16) = pack8(acc[kb][j], s2);
            }
        }
    }
}

__global__ __launch_bounds__(512, 2) void k1_kvproj(const u16* __restrict__ ckv, const u16* __restrict__ wuk, const u16* __restrict__ wuv,
                                                    const u16* __restrict__ x, const u16* __restrict__ cvec, const u16* __restrict__ wmod,
                                                    char* __restrict__ ws) {
    extern __shared__ __attribute__((aligned(16))) char lds[];
    const int b = blockIdx.x, tid = threadIdx.x, l = tid & 63;
    const int w = __builtin_amdgcn_readfirstlane(tid >> 6);
    const int xcd = b & 7, r = b >> 3, kb8 = r & 7, combo = r >> 3, type = combo & 1, hp = combo >> 1;
    const int h0 = xcd + 16 * hp, h1 = h0 + 8, key0 = kb8 * 256;
    const u16* W = type ? wuv : wuk;
    STAMP_AT(ws, 0, 0);
    const int rot = (kb8 + 2 * combo) & 7;
    if (type) k1_gemm<true>(ckv, W, ws + WS_KV, lds, key0, h0, h1, rot, x, cvec, wmod, ws);
    else k1_gemm<false>(ckv, W, ws + WS_KV, lds, key0, h0, h1, rot, x, cvec, wmod, ws);
    STAMP_AT(ws, 0, 4);
}

constexpr float ATT_THR = 8.f;
#define PIN(x) asm volatile("" : "+v"(x))
#define MX3(a, b, c) __builtin_fmaxf(__builtin_fmaxf((a), (b)), (c))
DI float att_rowmax(const f32x16& p0, const f32x16& p1) {
    float a = MX3(p0[0], p0[1], p1[0]), b = MX3(p0[2], p0[3], p1[1]); a = MX3(a, p1[2], p1[3]);
#pragma unroll
    for (int r = 4; r < 16; r += 4) { a = MX3(a, p0[r], p0[r + 1]); b = MX3(b, p0[r + 2], p0[r + 3]); a = MX3(a, p1[r], p1[r + 1]); b = MX3(b, p1[r + 2], p1[r + 3]); }
    return __builtin_fmaxf(a, b);
}
DI void att_mask(f32x16& s0, f32x16& s1, int dq) {
#pragma unroll
    for (int r = 0; r < 16; ++r) {
        const int c = (r & 3) + 8 * (r >> 2);
        if (c > dq) s0[r] = -INFINITY;
        if (c + 32 > dq) s1[r] = -INFINITY;
    }
}
constexpr int PD = 6;
DI int kfrag_off(int g) { return ((g & 1) * 8 + (g >> 1)) * 1024; }
DI int vfrag_off(int g) { return ((g & 3) * 4 + (g >> 2)) * 1024; }
template <bool MASK>
DI void att_step(f32x16& C0, f32x16& C1, const f32x16& P0, f32x16& P1, f32x16 (&o)[4], f32x16& negm, float& l_run,
                 const bf16x8 (&qf)[8], bf16x8 (&fr)[8], lds_cptr kp, lds_cptr vp, lds_cptr kpn, int dq) {
    unsigned pw[16];
    float sacc = 0.f, sacc2 = 0.f;
    SBAR();
#pragma unroll
    for (int g = 0; g < 16; ++g) {
        if ((g & 1) == 0) C0 = MFMA(fr[g & 7], qf[g >> 1], g < 2 ? negm : C0);
        else C1 = MFMA(fr[g & 7], qf[g >> 1], g < 2 ? negm : C1);
        if (g + PD < 16) fr[(g + PD) & 7] = ldsb128(kp + kfrag_off(g + PD));
        else fr[(g + PD) & 7] = ldsb128(vp + vfrag_off(g + PD - 16));
        P1[g] = __builtin_amdgcn_exp2f(P1[g]); PIN(P1[g]);
        sacc += P0[g]; PIN(sacc);
        if (g & 1) { pw[g >> 1] = pk2(P0[g - 1], P0[g]); PIN(pw[g >> 1]); }
        if (g >= 1) { sacc2 += P1[g - 1]; PIN(sacc2); }
        if (g >= 2 && (g & 1) == 0) { pw[8 + ((g - 2) >> 1)] = pk2(P1[g - 2], P1[g - 1]); PIN(pw[8 + ((g - 2) >> 1)]); }
        SBAR();
    }
    sacc2 += P1[15]; pw[15] = pk2(P1[14], P1[15]);
    l_run += sacc + sacc2;
    bool resc = false; float f = 1.f;
    float ma, mb;
#pragma unroll
    for (int g = 0; g < 16; ++g) {
        { const u32x4 pv = {pw[4 * (g >> 2)], pw[4 * (g >> 2) + 1], pw[4 * (g >> 2) + 2], pw[4 * (g >> 2) + 3]};
          o[g & 3] = MFMA(fr[g & 7], __builtin_bit_cast(bf16x8, pv), o[g & 3]); }
        if (g + PD < 16) fr[(g + PD) & 7] = ldsb128(vp + vfrag_off(g + PD));
        else fr[(g + PD) & 7] = ldsb128(kpn + kfrag_off(g + PD - 16));
        if (g == 1 && MASK) att_mask(C0, C1, dq);
        if (g == 1) { ma = MX3(C0[0], C0[1], C0[2]); mb = MX3(C0[3], C0[4], C0[5]); ma = MX3(ma, C0[6], C0[7]); mb = MX3(mb, C0[8], C0[9]); PIN(ma); PIN(mb); }
        if (g == 2) { ma = MX3(ma, C0[10], C0[11]); mb = MX3(mb, C0[12], C0[13]); ma = MX3(ma, C0[14], C0[15]); mb = MX3(mb, C1[0], C1[1]); PIN(ma); PIN(mb); }
        if (g == 3) { ma = MX3(ma, C1[2], C1[3]); mb = MX3(mb, C1[4], C1[5]); ma = MX3(ma, C1[6], C1[7]); mb = MX3(mb, C1[8], C1[9]); PIN(ma); PIN(mb); }
        if (g == 4) {
            ma = MX3(ma, C1[10], C1[11]); mb = MX3(mb, C1[12], C1[13]); ma = MX3(ma, C1[14], C1[15]);
            const float rm = __builtin_fmaxf(ma, mb);
            if (__builtin_expect(__any(rm > ATT_THR), 0)) {
                const float dl = __builtin_fmaxf(half_swap_max(rm), 0.f);
                f = __builtin_amdgcn_exp2f(-dl); l_run *= f; resc = true;
#pragma unroll
                for (int r = 0; r < 16; ++r) { C0[r] -= dl; C1[r] -= dl; negm[r] -= dl; }
            }
        }
        if (g >= 5) {
            const int e0 = g < 10 ? 2 * (g - 5) : 10 + (g - 10), e1 = g < 10 ? e0 + 2 : e0 + 1;
#pragma unroll
            for (int e = e0; e < e1; ++e) { C0[e] = __builtin_amdgcn_exp2f(C0[e]); PIN(C0[e]); }
        }
        SBAR();
    }
    if (__builtin_expect(resc, 0)) {
#pragma unroll
        for (int d = 0; d < 4; ++d)
#pragma unroll
            for (int r = 0; r < 16; ++r) o[d][r] *= f;
    }
}
constexpr int K2_LDS = 147456 + 4096;
constexpr float QSCALE = 0.08838834764831845f * 1.4426950408889634f;
__global__ __launch_bounds__(512, 2) void k2_attn(const u16* __restrict__ wq, const u16* __restrict__ cvec, const u16* __restrict__ wmod, char* __restrict__ ws) {
    extern __shared__ __attribute__((aligned(16))) char lds[];
    const int b = blockIdx.x, tid = threadIdx.x, l = tid & 63, r32 = l & 31, hi = l >> 5;
    const int w = __builtin_amdgcn_readfirstlane(tid >> 6);
    const int xcd = b & 7, kk = b >> 3, head = xcd + 8 * (kk >> 3), pi = kk & 7;
    const int qb = (w < 4) ? pi : 15 - pi, sub = w & 3, rb = qb * 4 + sub;
    const int tl = 2 * qb + (sub >> 1), NT = 32 - 2 * pi;
    const unsigned lds0 = (unsigned)(uintptr_t)lds;
    STAMP_AT(ws, 1, 0);
    const char* kvh = ws + WS_KV + ((size_t)head << 20);
    const char* ksrc = kvh + w * 2048 + l * 16;
    const unsigned kdst = lds0 + w * 2048, vdst = lds0 + 65536 + w * 2048;
#define DMA_K(t_, slot_) do { const char* g_ = ksrc + (size_t)(t_) * 32768; glds16(g_, kdst + (slot_)); glds16(g_ + 1024, kdst + (slot_) + 1024); } while (0)
#define DMA_V(t_, slot_) do { const char* g_ = ksrc + (size_t)(t_) * 32768 + 16384; glds16(g_, vdst + (slot_)); glds16(g_ + 1024, vdst + (slot_) + 1024); } while (0)
    DMA_K(0, 0);
    glds16(ws + WS_GS + (w & 3) * 1024 + l * 16, lds0 + 147456 + (w & 3) * 1024);
    u32x4 xf[16];
    const char* xsrc = ws + WS_XH + ((size_t)(rb * 32) * 64 + l) * 16;
#define XISSUE(c) do { _Pragma("unroll") for (int k8 = 0; k8 < 8; ++k8) asm volatile("global_load_dwordx4 %0, %1, off" : "=v"(xf[8 * ((c) & 1) + k8]) : "v"(xsrc + (8 * (c) + k8) * 1024) : "memory"); } while (0)
#define WISSUE(c) do { _Pragma("unroll") for (int n = 0; n < 4; ++n) { const int P = (4 * (c) + n) * 8 + w, row = 4 * P + (l >> 4), cc = (l & 15) ^ ((row & 3) << 2); \
            glds16(wq + (size_t)row * 4096 + head * 128 + cc * 8, lds0 + 16384 + P * 1024); } } while (0)
    XISSUE(0); WISSUE(0); XISSUE(1); WISSUE(1);
    const int q4 = (l & 15) >> 2, p4 = l & 3, blk = (l >> 4) & 1;
    const lds_cptr gsp = (lds_cptr)lds + 147456 + 32 * hi;
    lds_cptr wp[4];
#pragma unroll
    for (int db = 0; db < 4; ++db) wp[db] = (lds_cptr)lds + 16384 + (8 * hi + q4) * 256 + 64 * (db ^ q4) + 32 * blk + 16 * (p4 >> 1) + 8 * (p4 & 1);
    bf16x8 qf[8];
    {
        f32x16 qa[4];
#pragma unroll
        for (int d = 0; d < 4; ++d) qa[d] = f32x16{};
#define XWAIT(N, c) asm volatile("s_waitcnt vmcnt(" #N ") lgkmcnt(0)\n\ts_barrier" : "+v"(xf[8 * c]), "+v"(xf[8 * c + 1]), "+v"(xf[8 * c + 2]), "+v"(xf[8 * c + 3]), \
                                 "+v"(xf[8 * c + 4]), "+v"(xf[8 * c + 5]), "+v"(xf[8 * c + 6]), "+v"(xf[8 * c + 7]) :: "memory")
#define XONE(c, k8) asm volatile("global_load_dwordx4 %0, %1, off" : "=v"(xf[8 * ((c) & 1) + (k8)]) : "v"(xsrc + (8 * (c) + (k8)) * 1024) : "memory")
#define WONE(c, n) do { const int P = (4 * (c) + (n)) * 8 + w, row = 4 * P + (l >> 4), cc = (l & 15) ^ ((row & 3) << 2); \
            glds16(wq + (size_t)row * 4096 + head * 128 + cc * 8, lds0 + 16384 + P * 1024); } while (0)
#define KVONE(off_g, dst_) glds16(ksrc + (off_g), (dst_))
#define LDF4(p_) (*(const __attribute__((address_space(3))) f32x4*)(p_))
#define HPART(j_, G_, S_, e_) pk2(fmaf(bflo(xw[j_]), G_[e_], S_[e_]), fmaf(bfhi(xw[j_]), G_[e_ + 1], S_[e_ + 1]))
#pragma unroll
        for (int c = 0; c < 4; ++c) {
            STAMPX(ws, 2 * c);
            if (c == 0) { XWAIT(12, 0); STAMP_AT(ws, 1, 1); }
            if (c == 1) { XWAIT(4, 1); }
            if (c == 2) { XWAIT(8, 0); }
            if (c == 3) { XWAIT(2, 1); }
            STAMPX(ws, 2 * c + 1);
            bf16x8 wf[2][4]; f32x4 g0, g1, s0, s1; u32x4 hc;
            {
                const int ks = 8 * c;
                g0 = LDF4(gsp + 64 * ks); g1 = LDF4(gsp + 64 * ks + 16); s0 = LDF4(gsp + 2048 + 64 * ks); s1 = LDF4(gsp + 2048 + 64 * ks + 16);
#pragma unroll
                for (int db = 0; db < 4; ++db) wf[0][db] = wfrag(wp[db] + ks * 4096, wp[db] + ks * 4096 + 1024);
                const u32x4 xw = xf[8 * (c & 1)];
                hc = (u32x4){HPART(0, g0, s0, 0), HPART(1, g0, s0, 2), HPART(2, g1, s1, 0), HPART(3, g1, s1, 2)};
                g0 = LDF4(gsp + 64 * (ks + 1)); g1 = LDF4(gsp + 64 * (ks + 1) + 16); s0 = LDF4(gsp + 2048 + 64 * (ks + 1)); s1 = LDF4(gsp + 2048 + 64 * (ks + 1) + 16);
            }
            SBAR();
#pragma unroll
            for (int k8 = 0; k8 < 8; ++k8) {
                const int ks = 8 * c + k8;
                const bf16x8 hf = __builtin_bit_cast(bf16x8, hc);
                u32x4 hn = hc;
                const u32x4 xw = xf[8 * (c & 1) + (k8 < 7 ? k8 + 1 : k8)];
#pragma unroll
                for (int db = 0; db < 4; ++db) {
                    qa[db] = MFMA(wf[k8 & 1][db], hf, qa[db]);
                    {
                        const int ix = k8 * 4 + db;
                        if (c == 0) { if (ix < 4) WONE(2, ix); }
                        if (c == 1) { if (ix < 8) XONE(2, ix); else if (ix < 12) WONE(3, ix - 8);
                                      else if (ix < 16) KVONE((size_t)(1 + ((ix - 12) >> 1)) * 32768 + ((ix - 12) & 1) * 1024, kdst + 16384 * (1 + ((ix - 12) >> 1)) + ((ix - 12) & 1) * 1024); }
                        if (c == 2) { if (ix < 8) XONE(3, ix); else if (ix < 10) KVONE(16384 + (ix - 8) * 1024, vdst + (ix - 8) * 1024); }
                        if (c == 3) { if (ix < 2) KVONE(32768 + 16384 + ix * 1024, vdst + 16384 + ix * 1024); }
                    }
                    if (k8 < 7) {
                        wf[(k8 + 1) & 1][db] = wfrag(wp[db] + (ks + 1) * 4096, wp[db] + (ks + 1) * 4096 + 1024);
                        if (db == 0) hn[0] = HPART(0, g0, s0, 0);
                        if (db == 1) hn[1] = HPART(1, g0, s0, 2);
                        if (db == 2) hn[2] = HPART(2, g1, s1, 0);
                        if (db == 3) { hn[3] = HPART(3, g1, s1, 2); }
                        PIN(hn);
                        if (db == 1 && k8 < 6) { g0 = LDF4(gsp + 64 * (ks + 2)); s0 = LDF4(gsp + 2048 + 64 * (ks + 2)); }
                        if (db == 3 && k8 < 6) { g1 = LDF4(gsp + 64 * (ks + 2) + 16); s1 = LDF4(gsp + 2048 + 64 * (ks + 2) + 16); }
                    }
                    SBAR();
                }
                hc = hn;
            }
        }
#undef XWAIT
#undef XISSUE
#undef WISSUE
#undef HPART
#undef XONE
#undef WONE
#undef KVONE
#pragma unroll
        for (int db = 0; db < 4; ++db) {
#pragma unroll
            for (int r = 0; r < 16; ++r) qa[db][r] *= QSCALE;
            qf[2 * db] = pack8(qa[db], 0); qf[2 * db + 1] = pack8(qa[db], 1);
        }
    }
    STAMP_AT(ws, 1, 2); STAMPX(ws, 8);
#define WAIT_BAR4() asm volatile("s_waitcnt vmcnt(4)\n\ts_barrier" ::: "memory")
#define WAITB(t_) do { if ((t_) < tsw) WAIT_BAR4(); else asm volatile("s_waitcnt vmcnt(0)\n\ts_barrier" ::: "memory"); } while (0)
    float l_run = 0.f;
    f32x16 o[4], negm;
#pragma unroll
    for (int d = 0; d < 4; ++d) o[d] = f32x16{};
    const int qrow = rb * 32 + r32;
    const lds_cptr kp0 = (lds_cptr)lds + l * 16, vp0 = (lds_cptr)lds + 65536 + l * 16;
    f32x16 A0, A1, B0, B1;
    bf16x8 pf0, pf1, pf2, pf3, fr[8];
    int sl_prev = 49152, sl_cur = 0;
#define ROT() do { sl_prev = sl_cur; sl_cur = (sl_cur + 16384) & 65535; } while (0)
    const int tsw = 2 * pi + 3;
    const char* hsrc = kvh + (w & 3) * 4096 + l * 16;
    const unsigned hk = lds0 + (w & 3) * 4096, hv = lds0 + 65536 + (w & 3) * 4096;
#define STAGE_ISSUE(t_) do { if ((t_) < tsw) { const int tk_ = (t_) + 3 < NT ? (t_) + 3 : NT - 1, tv_ = (t_) + 2 < NT ? (t_) + 2 : NT - 1; DMA_K(tk_, sl_prev); DMA_V(tv_, (sl_cur + 32768) & 65535); } } while (0)
#define DMA_ARGS(t_) (t_) < tsw, ksrc + (size_t)((t_) + 3 < NT ? (t_) + 3 : NT - 1) * 32768, kdst + sl_prev, ksrc + (size_t)((t_) + 2 < NT ? (t_) + 2 : NT - 1) * 32768 + 16384, vdst + ((sl_cur + 32768) & 65535)
#define STAGE_HELP(t_) do { const int tk_ = (t_) + 3 < NT ? (t_) + 3 : NT - 1, tv_ = (t_) + 2 < NT ? (t_) + 2 : NT - 1; \
        const char* gk_ = hsrc + (size_t)tk_ * 32768; const char* gv_ = hsrc + (size_t)tv_ * 32768 + 16384; const unsigned sk_ = hk + sl_prev, sv_ = hv + ((sl_cur + 32768) & 65535); \
        glds16(gk_, sk_); glds16(gk_ + 1024, sk_ + 1024); glds16(gk_ + 2048, sk_ + 2048); glds16(gk_ + 3072, sk_ + 3072); \
        glds16(gv_, sv_); glds16(gv_ + 1024, sv_ + 1024); glds16(gv_ + 2048, sv_ + 2048); glds16(gv_ + 3072, sv_ + 3072); } while (0)
    asm volatile("s_waitcnt vmcnt(2) lgkmcnt(0)\n\ts_barrier" ::: "memory");
    STAGE_ISSUE(0);
    {
        A0 = f32x16{}; A1 = f32x16{};
#pragma unroll
        for (int ds = 0; ds < 8; ++ds) {
            A0 = MFMA(ldsb128(kp0 + ds * 1024), qf[ds], A0);
            A1 = MFMA(ldsb128(kp0 + 8192 + ds * 1024), qf[ds], A1);
        }
        if (tl == 0) att_mask(A0, A1, qrow - 4 * hi);
        const float mx = half_swap_max(att_rowmax(A0, A1));
#pragma unroll
        for (int r = 0; r < 16; ++r) { A0[r] = __builtin_amdgcn_exp2f(A0[r] - mx); A1[r] = A1[r] - mx; negm[r] = -mx; }
#pragma unroll
        for (int g = 0; g < PD; ++g) fr[g] = ldsb128(kp0 + 16384 + kfrag_off(g));
    }
    WAIT_BAR4(); ROT();
    int t = 1;
    for (; t + 1 <= tl - 1; t += 2) {
        STAGE_ISSUE(t); att_step<false>(B0, B1, A0, A1, o, negm, l_run, qf, fr, kp0 + sl_cur, vp0 + sl_prev, kp0 + ((sl_cur + 16384) & 65535), 0); WAITB(t); ROT();
        STAGE_ISSUE(t + 1); att_step<false>(A0, A1, B0, B1, o, negm, l_run, qf, fr, kp0 + sl_cur, vp0 + sl_prev, kp0 + ((sl_cur + 16384) & 65535), 0); WAITB(t + 1); ROT();
    }
    if (t <= tl - 1) {
        STAGE_ISSUE(t); att_step<false>(B0, B1, A0, A1, o, negm, l_run, qf, fr, kp0 + sl_cur, vp0 + sl_prev, kp0 + ((sl_cur + 16384) & 65535), 0); WAITB(t); ROT();
        A0 = B0; A1 = B1; ++t;
    }
    if (tl >= 1) {
        STAGE_ISSUE(t); att_step<true>(B0, B1, A0, A1, o, negm, l_run, qf, fr, kp0 + sl_cur, vp0 + sl_prev, kp0 + ((sl_cur + 16384) & 65535), qrow - t * 64 - 4 * hi); WAITB(t); ROT();
        ++t;
    } else { B0 = A0; B1 = A1; }
    if (t < NT) STAGE_ISSUE(t);
    {
        float sacc = 0.f;
#pragma unroll
        for (int r = 0; r < 16; ++r) { B1[r] = __builtin_amdgcn_exp2f(B1[r]); sacc += B0[r] + B1[r]; }
        l_run += sacc;
        pf0 = pack8(B0, 0); pf1 = pack8(B0, 1); pf2 = pack8(B1, 0); pf3 = pack8(B1, 1);
        const lds_cptr vp = vp0 + sl_prev;
#pragma unroll
        for (int d = 0; d < 4; ++d) {
            o[d] = MFMA(ldsb128(vp + (d * 4 + 0) * 1024), pf0, o[d]);
            o[d] = MFMA(ldsb128(vp + (d * 4 + 1) * 1024), pf1, o[d]);
            o[d] = MFMA(ldsb128(vp + (d * 4 + 2) * 1024), pf2, o[d]);
            o[d] = MFMA(ldsb128(vp + (d * 4 + 3) * 1024), pf3, o[d]);
        }
    }
    STAMP_AT(ws, 1, 3);
    {
        const float inv = 1.f / half_swap_sum(l_run);
        char* cb = ws + WS_CTX + ((size_t)(rb * 256 + head * 8) * 64 + l) * 16;
#pragma unroll
        for (int d = 0; d < 4; ++d) {
#pragma unroll
            for (int r = 0; r < 16; ++r) o[d][r] *= inv;
            *(bf16x8*)(cb + (2 * d) * 1024) = pack8(o[d], 0);
            *(bf16x8*)(cb + (2 * d + 1) * 1024) = pack8(o[d], 1);
        }
    }
    if (t < NT) {
        WAIT_ALL_BAR(); ROT(); ++t;
        for (; t < NT && t < tsw; ++t) { STAGE_ISSUE(t); WAIT_BAR4(); ROT(); }
        if (w < 4) {
            float* scr = (float*)(lds + 147456);
            const int t0 = t;
            for (; t < NT; ++t) {
                STAGE_HELP(t);
                if (pi == 4 && t == t0) {
                    const int c8 = tid & 7, rowi = tid >> 3;
                    float a8[8];
#pragma unroll
                    for (int e = 0; e < 8; ++e) a8[e] = 0.f;
#pragma unroll
                    for (int q = 0; q < 16; ++q) {
                        const u32x4 wv = *(const u32x4*)(wmod + (size_t)(rowi + 32 * q) * 3072 + 1024 + 64 * head + 8 * c8);
                        const float cv = bf2f(cvec[rowi + 32 * q]), sc = bfr(cv / (1.f + __expf(-cv)));
#pragma unroll
                        for (int e = 0; e < 4; ++e) { a8[2 * e] += sc * bflo(wv[e]); a8[2 * e + 1] += sc * bfhi(wv[e]); }
                    }
#pragma unroll
                    for (int m = 8; m < 64; m <<= 1)
#pragma unroll
                        for (int e = 0; e < 8; ++e) a8[e] += __shfl_xor(a8[e], m);
                    if (l < 8) {
#pragma unroll
                        for (int e = 0; e < 8; ++e) scr[(w * 8 + l) * 8 + e] = a8[e];
                    }
                }
                if (pi == 4 && t == t0 + 1 && tid < 64) {
                    const float s = (scr[tid] + scr[64 + tid]) + (scr[128 + tid] + scr[192 + tid]);
                    ((float*)(ws + WS_MOD))[1024 + 64 * head + tid] = bfr(s);
                }
                asm volatile("s_waitcnt vmcnt(8) lgkmcnt(0)\n\ts_barrier" ::: "memory"); ROT();
            }
        }
        else { for (; t < NT; ++t) { WAIT_ALL_BAR(); ROT(); } }
    }
    asm volatile("s_waitcnt vmcnt(0)" ::: "memory");
    STAMP_AT(ws, 1, 4);
#undef DMA_K
#undef DMA_V
#undef ROT
#undef STAGE_ISSUE
#undef STAGE_HELP
#undef DMA_ARGS
#undef WAITB
}

template <int TQ, int TN, int KW, int BKS, bool APERM, bool CT>
struct G {
    static constexpr int NWAVE = TQ * TN * KW, RB = TN * 128, A_BYTES = 2 * TQ * BKS * 1024, W_BYTES = 16 * BKS * RB, STAGE = A_BYTES + W_BYTES;
    static constexpr int CPR = RB / 16;
    static_assert(STAGE == 32768 && (2 * TQ * BKS + W_BYTES / 1024) / NWAVE == 4, "4 glds per wave per stage, 4 x 32 KB ring");
    static DI int swz(int row) { return RB == 256 ? ((row & 3) << 2) : (((row >> 1) & 1) << 2); }
    static DI void stage(const char* AF, size_t a_rb_stride, const u16* Wp, int ldw, int kst, unsigned ldsaddr, int w, int l) {
#pragma unroll
        for (int p0 = 0; p0 < 2 * TQ * BKS; p0 += NWAVE) {
            const int p = p0 + w, rbi = p / BKS, ks = p % BKS;
            glds16(AF + rbi * a_rb_stride + (size_t)(kst * BKS + ks) * 1024 + l * 16, ldsaddr + p * 1024);
        }
#pragma unroll
        for (int p0 = 0; p0 < W_BYTES / 1024; p0 += NWAVE) {
            const int p = p0 + w, row = p * (1024 / RB) + l / CPR, c = (l % CPR) ^ swz(row);
            glds16(Wp + (size_t)(kst * BKS * 16 + row) * ldw + c * 8, ldsaddr + A_BYTES + p * 1024);
        }
    }
    static constexpr int NPA = 2 * TQ * BKS / NWAVE, NPW = W_BYTES / 1024 / NWAVE;
    static DI void stage_piece(int i, const char* AF, size_t a_rb_stride, const u16* Wp, int ldw, int kst, unsigned ldsaddr, int w, int l) {
        if (i < NPA) {
            const int p = i * NWAVE + w, rbi = p / BKS, ks = p % BKS;
            glds16(AF + rbi * a_rb_stride + (size_t)(kst * BKS + ks) * 1024 + l * 16, ldsaddr + p * 1024);
        } else {
            const int p = (i - NPA) * NWAVE + w, row = p * (1024 / RB) + l / CPR, c = (l % CPR) ^ swz(row);
            glds16(Wp + (size_t)(kst * BKS * 16 + row) * ldw + c * 8, ldsaddr + A_BYTES + p * 1024);
        }
    }
    template <bool RM, class EPI>
    static DI void run(EPI epi, const char* AF, size_t a_rb_stride, const u16* Wp, int ldw, int nstage, int rot, char* lds, float (&red)[64 / KW], int& tq, int& tn, int& kw, char* ws, int kid) {
        const int tid = threadIdx.x, l = tid & 63, hi = l >> 5;
        STAMP_AT(ws, kid, 0);
        const int w = __builtin_amdgcn_readfirstlane(tid >> 6);
        kw = w % KW; const int tt = w / KW; tq = tt / TN; tn = tt % TN;
        const unsigned lds0 = (unsigned)(uintptr_t)lds;
        const int q4 = (l & 15) >> 2, p4 = l & 3, blk = (l >> 4) & 1;
        f32x16 acc[2][2];
#pragma unroll
        for (int i = 0; i < 2; ++i) { acc[i][0] = f32x16{}; acc[i][1] = f32x16{}; }
        int kst = rot;
#define NEXTK() do { kst = (kst + 1 == nstage) ? 0 : kst + 1; } while (0)
        stage(AF, a_rb_stride, Wp, ldw, kst, lds0, w, l); NEXTK();
        stage(AF, a_rb_stride, Wp, ldw, kst, lds0 + STAGE, w, l); NEXTK();
        stage(AF, a_rb_stride, Wp, ldw, kst, lds0 + 2 * STAGE, w, l); NEXTK();
        for (int st = 0; st < nstage; ++st) {
            if (st + 2 < nstage) asm volatile("s_waitcnt vmcnt(8) lgkmcnt(0)\n\ts_barrier" ::: "memory");
            else if (st + 1 < nstage) asm volatile("s_waitcnt vmcnt(4) lgkmcnt(0)\n\ts_barrier" ::: "memory");
            else WAIT_ALL_BAR();
            if (st == 0) STAMP_AT(ws, kid, 1);
            const bool pre = st + 3 < nstage; const unsigned pdst = lds0 + ((st + 3) & 3) * STAGE;
            lds_cptr ab = (lds_cptr)lds + (st & 3) * STAGE;
            lds_cptr wb = ab + A_BYTES;
#pragma unroll
            for (int kk = 0; kk < BKS / KW; ++kk) {
                const int ks = kw * (BKS / KW) + kk;
                bf16x8 af[2], wf[2];
#pragma unroll
                for (int i = 0; i < 2; ++i) af[i] = ldsb128(ab + ((2 * tq + i) * BKS + ks) * 1024 + l * 16);
#pragma unroll
                for (int j = 0; j < 2; ++j) {
                    const int r0 = 16 * ks + (APERM ? 4 * hi : 8 * hi) + q4, r1 = r0 + (APERM ? 8 : 4);
                    const int ch = 4 * (2 * tn + j) + 2 * blk + (p4 >> 1);
                    wf[j] = wfrag(wb + r0 * RB + ((ch ^ swz(r0)) << 4) + 8 * (p4 & 1), wb + r1 * RB + ((ch ^ swz(r1)) << 4) + 8 * (p4 & 1));
                }
#pragma unroll
                for (int i = 0; i < 2; ++i)
#pragma unroll
                    for (int j = 0; j < 2; ++j) {
                        if (CT) acc[i][j] = MFMA(wf[j], af[i], acc[i][j]);
                        else acc[i][j] = MFMA(af[i], wf[j], acc[i][j]);
                        constexpr int PER = 4 / (BKS / KW);
                        const int m = i * 2 + j;
                        if (PER == 4) { SBAR(); if (pre) stage_piece(m, AF, a_rb_stride, Wp, ldw, kst, pdst, w, l); SBAR(); }
                        else if (m & 1) { SBAR(); if (pre) stage_piece(kk * 2 + (m >> 1), AF, a_rb_stride, Wp, ldw, kst, pdst, w, l); SBAR(); }
                    }
            }
            if (pre) NEXTK();
        }
        BAR_LGKM();
        STAMP_AT(ws, kid, 2);
        float* rb_ = (float*)lds;
        constexpr int RS = RM ? 68 : 64;
#pragma unroll
        for (int i = 0; i < 2; ++i)
#pragma unroll
            for (int j = 0; j < 2; ++j)
#pragma unroll
                for (int r = 0; r < 16; ++r) rb_[(w * 64 + (i * 2 + j) * 16 + r) * RS + l] = acc[i][j][r];
        BAR_LGKM();
        if constexpr (RM) {
            constexpr int C8 = TN * 8, NITEM = 64 * TQ * C8 / (64 * NWAVE);
#pragma unroll
            for (int it = 0; it < NITEM; ++it) {
                const int id = it * (64 * NWAVE) + tid, row = id / C8, c8 = id % C8;
                const int rr = row & 31, r = (rr & 3) + 4 * (rr >> 3), h = (rr >> 2) & 1;
                const int t2 = (row >> 6) * TN + (c8 >> 3), reg = (((row >> 5) & 1) * 2 + ((c8 >> 2) & 1)) * 16 + r, ln = (c8 & 3) * 8 + 32 * h;
                f32x4 a0 = {0.f, 0.f, 0.f, 0.f}, a1 = a0;
#pragma unroll
                for (int k2 = 0; k2 < KW; ++k2) {
                    const float* q_ = rb_ + ((t2 * KW + k2) * 64 + reg) * RS + ln;
                    a0 += *(const f32x4*)q_; a1 += *(const f32x4*)(q_ + 4);
                }
                epi(row, c8 * 8, a0, a1);
            }
        } else {
#pragma unroll
            for (int e = 0; e < 64 / KW; ++e) {
                float s = 0.f;
#pragma unroll
                for (int k2 = 0; k2 < KW; ++k2) s += rb_[((tt * KW + k2) * 64 + kw * (64 / KW) + e) * RS + l];
                red[e] = s;
            }
        }
        STAMP_AT(ws, kid, 3);
    }
};

typedef G<2, 2, 2, 4, true, false> G3;
constexpr int K3_LDS = 139264;
__global__ __launch_bounds__(512, 2) void k3_oproj(const u16* __restrict__ wo, char* __restrict__ ws) {
    extern __shared__ __attribute__((aligned(16))) char lds[];
    const int b = blockIdx.x, xcd = b & 7, r = b >> 3, ksl = xcd & 3, rp = (xcd >> 2) * 8 + (r >> 2), ct = r & 3;
    float red[32]; int tq, tn, kw;
    u16* part = (u16*)(ws + WS_PART) + (size_t)ksl * 2048 * 512 + (size_t)(rp * 128) * 512 + ct * 128;
    G3::run<true>([&](int row, int col, f32x4 a0, f32x4 a1) {
                      u32x4 v = {pk2(a0[0], a0[1]), pk2(a0[2], a0[3]), pk2(a1[0], a1[1]), pk2(a1[2], a1[3])};
                      *(u32x4*)(part + (size_t)row * 512 + col) = v; },
                  ws + WS_CTX + ((size_t)(rp * 4) * 256 + ksl * 64) * 1024, (size_t)256 * 1024, wo + (size_t)ksl * 1024 * 512 + ct * 128, 512, 16, (4 * ct + 2 * (r >> 2)) & 15, lds, red, tq, tn, kw, ws, 2);
    STAMP_AT(ws, 2, 4);
}

__global__ __launch_bounds__(512) void k3b_ln2(const u16* __restrict__ x, char* __restrict__ ws) {
    const int l = threadIdx.x & 63, row = blockIdx.x * 8 + (threadIdx.x >> 6);
    const u16* part = (const u16*)(ws + WS_PART) + (size_t)row * 512 + 8 * l;
    const float* mod = (const float*)(ws + WS_MOD);
    u32x4 pv[4];
#pragma unroll
    for (int s = 0; s < 4; ++s) pv[s] = *(const u32x4*)(part + (size_t)s * 2048 * 512);
    const u32x4 xv4 = *(const u32x4*)(x + (size_t)row * 512 + 8 * l);
    const f32x4 g0 = *(const f32x4*)(mod + 1024 + 8 * l), g1 = *(const f32x4*)(mod + 1024 + 8 * l + 4);
    const f32x4 c0 = *(const f32x4*)(mod + 2048 + 8 * l), c1 = *(const f32x4*)(mod + 2048 + 8 * l + 4);
    const f32x4 s0 = *(const f32x4*)(mod + 1536 + 8 * l), s1 = *(const f32x4*)(mod + 1536 + 8 * l + 4);
    float x1[8], xh[8];
#pragma unroll
    for (int j = 0; j < 4; ++j) {
        const int w0 = j >> 1, w1 = 2 + (j >> 1);
        const float a0 = (j & 1) ? (bfhi(pv[0][w0]) + bfhi(pv[1][w0])) + (bfhi(pv[2][w0]) + bfhi(pv[3][w0])) : (bflo(pv[0][w0]) + bflo(pv[1][w0])) + (bflo(pv[2][w0]) + bflo(pv[3][w0]));
        const float a1 = (j & 1) ? (bfhi(pv[0][w1]) + bfhi(pv[1][w1])) + (bfhi(pv[2][w1]) + bfhi(pv[3][w1])) : (bflo(pv[0][w1]) + bflo(pv[1][w1])) + (bflo(pv[2][w1]) + bflo(pv[3][w1]));
        const float xa = (j & 1) ? bfhi(xv4[j >> 1]) : bflo(xv4[j >> 1]), xb = (j & 1) ? bfhi(xv4[2 + (j >> 1)]) : bflo(xv4[2 + (j >> 1)]);
        x1[j] = bfr(xa + bfr(g0[j] * bfr(a0)));
        x1[4 + j] = bfr(xb + bfr(g1[j] * bfr(a1)));
    }
    u32x4 o1 = {pk2(x1[0], x1[1]), pk2(x1[2], x1[3]), pk2(x1[4], x1[5]), pk2(x1[6], x1[7])};
    *(u32x4*)(ws + WS_X1 + ((size_t)row * 512 + 8 * l) * 2) = o1;
    ln_row(x1, xh);
    float h[8];
#pragma unroll
    for (int j = 0; j < 4; ++j) { h[j] = bfr(xh[j] * bfr(1.f + c0[j])) + s0[j]; h[4 + j] = bfr(xh[4 + j] * bfr(1.f + c1[j])) + s1[j]; }
    u32x4 oh = {pk2(h[0], h[1]), pk2(h[2], h[3]), pk2(h[4], h[5]), pk2(h[6], h[7])};
    *(u32x4*)(ws + WS_H2 + ((size_t)((row >> 5) * 32 + (l >> 1)) * 64 + (row & 31) + 32 * (l & 1)) * 16) = oh;
}

typedef G<2, 2, 2, 4, false, true> G4;
__global__ __launch_bounds__(512, 2) void k4_ffn1(const u16* __restrict__ w1, char* __restrict__ ws) {
    extern __shared__ __attribute__((aligned(16))) char lds[];
    const int b = blockIdx.x, xcd = b & 7, jj = b >> 3, rp = 4 * (xcd >> 1) + jj / 7, ct = 7 * (xcd & 1) + jj % 7;
    const int l = threadIdx.x & 63;
    float red[32]; int tq, tn, kw;
    G4::run<false>(0, ws + WS_H2 + (size_t)(rp * 4) * 32 * 1024, (size_t)32 * 1024, w1 + ct * 128, DFF, 8, (jj % 7 + 2 * (jj / 7)) & 7, lds, red, tq, tn, kw, ws, 3);
#pragma unroll
    for (int j = 0; j < 2; ++j)
#pragma unroll
        for (int s = 0; s < 2; ++s) {
            const int e = j * 16 + 8 * s;
            u32x4 v = {pk2(fmaxf(red[e], 0.f), fmaxf(red[e + 1], 0.f)), pk2(fmaxf(red[e + 2], 0.f), fmaxf(red[e + 3], 0.f)),
                       pk2(fmaxf(red[e + 4], 0.f), fmaxf(red[e + 5], 0.f)), pk2(fmaxf(red[e + 6], 0.f), fmaxf(red[e + 7], 0.f))};
            const int rbq = rp * 4 + 2 * tq + kw, kstep = (ct * 128 + 64 * tn + 32 * j) / 16 + s;
            *(u32x4*)(ws + WS_H1 + ((size_t)(rbq * 112 + kstep) * 64 + l) * 16) = v;
        }
    STAMP_AT(ws, 3, 4);
}

typedef G<1, 1, 8, 8, true, false> G5;
__global__ __launch_bounds__(512, 2) void k5_ffn2(const u16* __restrict__ w2, char* __restrict__ ws, u16* __restrict__ out) {
    extern __shared__ __attribute__((aligned(16))) char lds[];
    const int b = blockIdx.x, xcd = b & 7, jj = b >> 3, rp = 8 * (xcd >> 1) + (jj >> 2), ct = 4 * (xcd & 1) + (jj & 3);
    float red[8]; int tq, tn, kw;
    const float* gb = (const float*)(ws + WS_MOD) + 2560 + ct * 64;
    const u16* x1 = (const u16*)(ws + WS_X1) + (size_t)(rp * 64) * 512 + ct * 64;
    u16* op = out + (size_t)(rp * 64) * 512 + ct * 64;
    G5::run<true>([&](int row, int col, f32x4 a0, f32x4 a1) {
                      const u32x4 xv = *(const u32x4*)(x1 + (size_t)row * 512 + col);
                      const f32x4 g0 = *(const f32x4*)(gb + col), g1 = *(const f32x4*)(gb + col + 4);
                      u32x4 v = {pk2(bflo(xv[0]) + bfr(g0[0] * bfr(a0[0])), bfhi(xv[0]) + bfr(g0[1] * bfr(a0[1]))),
                                 pk2(bflo(xv[1]) + bfr(g0[2] * bfr(a0[2])), bfhi(xv[1]) + bfr(g0[3] * bfr(a0[3]))),
                                 pk2(bflo(xv[2]) + bfr(g1[0] * bfr(a1[0])), bfhi(xv[2]) + bfr(g1[1] * bfr(a1[1]))),
                                 pk2(bflo(xv[3]) + bfr(g1[2] * bfr(a1[2])), bfhi(xv[3]) + bfr(g1[3] * bfr(a1[3])))};
                      *(u32x4*)(op + (size_t)row * 512 + col) = v; },
                  ws + WS_H1 + (size_t)(rp * 2) * 112 * 1024, (size_t)112 * 1024, w2 + ct * 64, 512, 14, (3 * (jj & 3) + 2 * (jj >> 2)) % 14, lds, red, tq, tn, kw, ws, 4);
    STAMP_AT(ws, 4, 4);
}

extern "C" void kernel_launch(void* const* d_in, const int* in_sizes, int n_in, void* d_out, int out_size, void* d_ws, size_t ws_size,
                              hipStream_t stream) {
    (void)in_sizes; (void)n_in; (void)out_size; (void)ws_size;
    const u16* x = (const u16*)d_in[0];
    const u16* ckv = (const u16*)d_in[1];
    const u16* wq = (const u16*)d_in[2];
    const u16* wuk = (const u16*)d_in[3];
    const u16* wuv = (const u16*)d_in[4];
    const u16* wo = (const u16*)d_in[5];
    const u16* w1 = (const u16*)d_in[7];
    const u16* w2 = (const u16*)d_in[8];
    const u16* cvec = (const u16*)d_in[9];
    const u16* wmod = (const u16*)d_in[10];
    char* ws = (char*)d_ws;
    hipFuncSetAttribute((const void*)k1_kvproj, hipFuncAttributeMaxDynamicSharedMemorySize, K1_LDS);
    hipFuncSetAttribute((const void*)k2_attn, hipFuncAttributeMaxDynamicSharedMemorySize, K2_LDS);
    hipFuncSetAttribute((const void*)k3_oproj, hipFuncAttributeMaxDynamicSharedMemorySize, K3_LDS);
    hipFuncSetAttribute((const void*)k4_ffn1, hipFuncAttributeMaxDynamicSharedMemorySize, K3_LDS);
    hipFuncSetAttribute((const void*)k5_ffn2, hipFuncAttributeMaxDynamicSharedMemorySize, K3_LDS);
    k1_kvproj<<<256, 512, K1_LDS, stream>>>(ckv, wuk, wuv, x, cvec, wmod, ws);
    k2_attn<<<256, 512, K2_LDS, stream>>>(wq, cvec, wmod, ws);
    k3_oproj<<<256, 512, K3_LDS, stream>>>(wo, ws);
    k3b_ln2<<<256, 512, 0, stream>>>(x, ws);
    k4_ffn1<<<224, 512, K3_LDS, stream>>>(w1, ws);
    k5_ffn2<<<256, 512, K3_LDS, stream>>>(w2, ws, (u16*)d_out);
}
```

```cpp
#include <hip/hip_runtime.h>
#include <hip/hip_bf16.h>
#include <stdint.h>

typedef unsigned short u16;
typedef short bf16x8 __attribute__((ext_vector_type(8)));
typedef short s16x4 __attribute__((ext_vector_type(4)));
typedef short v4i16_t __attribute__((ext_vector_type(4)));
typedef float f32x16 __attribute__((ext_vector_type(16)));
typedef float f32x4 __attribute__((ext_vector_type(4)));
typedef float f32x2 __attribute__((ext_vector_type(2)));
typedef unsigned u32x4 __attribute__((ext_vector_type(4)));
typedef __bf16 bf16x2_t __attribute__((ext_vector_type(2)));
typedef __attribute__((address_space(3))) const char* lds_cptr;
#define DI __device__ __forceinline__
#define MFMA(a, b, c) __builtin_amdgcn_mfma_f32_32x32x16_bf16((a), (b), (c), 0, 0, 0)
#define SBAR() __builtin_amdgcn_sched_barrier(0)
#define STAMPX(ws_, idx) do { } while (0)
#define STAMP_AT(ws_, kid, idx) do { } while (0)

constexpr int SEQ = 2048, DM = 512, NH = 32, HD = 128, DFF = 1792;

constexpr size_t WS_KV = 0;
constexpr size_t WS_XH = WS_KV + (size_t)32 * 32 * 32768;
constexpr size_t WS_MOD = WS_XH + (size_t)64 * 32 * 1024;
constexpr size_t WS_GS = WS_MOD + 12288;
constexpr size_t WS_CTX = WS_MOD + 16384;
constexpr size_t WS_PART = WS_CTX + (size_t)64 * 256 * 1024;
constexpr size_t WS_X1 = WS_PART + (size_t)4 * 2048 * 512 * 4;
constexpr size_t WS_H2 = WS_X1 + (size_t)2048 * 512 * 2;
constexpr size_t WS_H1 = WS_H2 + (size_t)64 * 32 * 1024;
constexpr size_t WS_QX = WS_H1 + (size_t)64 * 112 * 1024;

DI unsigned pk2(float a, float b) { f32x2 v = {a, b}; bf16x2_t r = __builtin_convertvector(v, bf16x2_t); return __builtin_bit_cast(unsigned, r); }
DI float bf2f(u16 v) { return __uint_as_float(((unsigned)v) << 16); }
DI float bfr(float x) { return __uint_as_float(pk2(x, 0.f) << 16); }
DI float bflo(unsigned w) { return __uint_as_float(w << 16); }
DI float bfhi(unsigned w) { return __uint_as_float(w & 0xffff0000u); }
DI bf16x8 pack8(const f32x16& a, int s) {
    u32x4 w = {pk2(a[8 * s], a[8 * s + 1]), pk2(a[8 * s + 2], a[8 * s + 3]), pk2(a[8 * s + 4], a[8 * s + 5]), pk2(a[8 * s + 6], a[8 * s + 7])};
    return __builtin_bit_cast(bf16x8, w);
}
DI int crow(int r, int hi) { return (r & 3) + 8 * (r >> 2) + 4 * hi; }
DI void glds16(const void* g, unsigned lds_base) {
    unsigned sv;
    asm volatile("s_mov_b32 %0, m0\n\ts_mov_b32 m0, %2\n\ts_nop 0\n\tglobal_load_lds_dwordx4 %1, off\n\ts_mov_b32 m0, %0"
                 : "=&s"(sv) : "v"(g), "s"(lds_base) : "memory");
}
DI void glds4(const void* g, unsigned lds_base) {
    unsigned sv;
    asm volatile("s_mov_b32 %0, m0\n\ts_mov_b32 m0, %2\n\ts_nop 0\n\tglobal_load_lds_dword %1, off\n\ts_mov_b32 m0, %0"
                 : "=&s"(sv) : "v"(g), "s"(lds_base) : "memory");
}
DI bf16x8 ldsb128(lds_cptr p) { return *(const __attribute__((address_space(3))) bf16x8*)p; }
DI s16x4 ldstr(lds_cptr p) { return __builtin_bit_cast(s16x4, __builtin_amdgcn_ds_read_tr16_b64_v4i16((__attribute__((address_space(3))) v4i16_t*)p)); }
DI bf16x8 wfrag(lds_cptr p0, lds_cptr p1) { s16x4 lo = ldstr(p0), hi = ldstr(p1); return __builtin_shufflevector(lo, hi, 0, 1, 2, 3, 4, 5, 6, 7); }
#define WAIT_ALL_BAR() asm volatile("s_waitcnt vmcnt(0) lgkmcnt(0)\n\ts_barrier" ::: "memory")
#define BAR_LGKM() asm volatile("s_waitcnt lgkmcnt(0)\n\ts_barrier" ::: "memory")
DI float wave_sum(float v) {
#pragma unroll
    for (int m = 1; m < 64; m <<= 1) v += __shfl_xor(v, m);
    return v;
}
DI float half_swap_max(float m) { auto rr = __builtin_amdgcn_permlane32_swap(__float_as_uint(m), __float_as_uint(m), false, false); return fmaxf(__uint_as_float(rr[0]), __uint_as_float(rr[1])); }
DI float half_swap_sum(float m) { auto rr = __builtin_amdgcn_permlane32_swap(__float_as_uint(m), __float_as_uint(m), false, false); return __uint_as_float(rr[0]) + __uint_as_float(rr[1]); }

DI void ln_row(const float (&xv)[8], float (&o)[8]) {
    float s = 0.f;
#pragma unroll
    for (int j = 0; j < 8; ++j) s += xv[j];
    const float mean = wave_sum(s) * (1.f / 512.f);
    float q = 0.f;
#pragma unroll
    for (int j = 0; j < 8; ++j) { const float d = xv[j] - mean; q += d * d; }
    const float var = wave_sum(q) * (1.f / 512.f);
    const float mb = bfr(mean), vb = bfr(var);
    const float den = bfr(sqrtf(bfr(vb + 1e-5f)));
#pragma unroll
    for (int j = 0; j < 8; ++j) o[j] = bfr(bfr(xv[j] - mb) / den);
}

constexpr int K1_LDS = 131072;
template <bool VT>
DI void k1_gemm(const u16* __restrict__ ckv, const u16* __restrict__ W, char* __restrict__ kvimg, char* lds, int key0, int h0, int h1, int rot,
                 const u16* __restrict__ x, const u16* __restrict__ cvec, const u16* __restrict__ wmod, char* __restrict__ ws) {
    const int tid = threadIdx.x, l = tid & 63, r32 = l & 31, hi = l >> 5;
    const int w = __builtin_amdgcn_readfirstlane(tid >> 6);
    const int wk = w >> 2, wc = w & 3;
    const unsigned lds0 = (unsigned)(uintptr_t)lds;
    const u16* asrc[4]; const u16* wsrc[4];
#pragma unroll
    for (int n = 0; n < 4; ++n) {
        const int P = n * 8 + w, row = 8 * P + (l >> 3), c = (l & 7) ^ ((row >> 1) & 7);
        asrc[n] = ckv + (size_t)(key0 + row) * 512 + c * 8;
        const int half = P >> 4, rw = 4 * (P & 15) + (l >> 4), cw = (l & 15) ^ ((rw & 3) << 2);
        wsrc[n] = W + (size_t)rw * 4096 + (half ? h1 : h0) * 128 + cw * 8;
    }
#define K1_PIECE(i, kt_, bb_) do { if ((i) < 4) glds16(asrc[i] + (kt_) * 64, (bb_) + ((i) * 8 + w) * 1024); \
        else glds16(wsrc[(i) - 4] + (size_t)(kt_) * 64 * 4096, (bb_) + 32768 + (((i) - 4) * 8 + w) * 1024); } while (0)
    {
        const int b = blockIdx.x, row = 8 * b + w, half = tid & 1, rowi = tid >> 1;
        u32x4 xv4, wv0, wv1; unsigned cv0, cv1;
        asm volatile("global_load_dwordx4 %0, %1, off" : "=v"(xv4) : "v"(x + (size_t)row * 512 + 8 * l) : "memory");
        if (b < 192) {
            asm volatile("global_load_dwordx4 %0, %1, off" : "=v"(wv0) : "v"(wmod + (size_t)rowi * 3072 + 16 * b + 8 * half) : "memory");
            asm volatile("global_load_dwordx4 %0, %1, off" : "=v"(wv1) : "v"(wmod + (size_t)(rowi + 256) * 3072 + 16 * b + 8 * half) : "memory");
            asm volatile("global_load_ushort %0, %1, off" : "=v"(cv0) : "v"(cvec + rowi) : "memory");
            asm volatile("global_load_ushort %0, %1, off" : "=v"(cv1) : "v"(cvec + rowi + 256) : "memory");
        }
#pragma unroll
        for (int i = 0; i < 8; ++i) K1_PIECE(i, rot, lds0);
        float* scr = (float*)(lds + 65536);
        asm volatile("s_waitcnt vmcnt(8)" : "+v"(xv4) :: "memory");
        {
            float xv[8], o[8];
#pragma unroll
            for (int j = 0; j < 4; ++j) { xv[2 * j] = bflo(xv4[j]); xv[2 * j + 1] = bfhi(xv4[j]); }
            ln_row(xv, o);
            u32x4 ov = {pk2(o[0], o[1]), pk2(o[2], o[3]), pk2(o[4], o[5]), pk2(o[6], o[7])};
            *(u32x4*)(ws + WS_XH + ((size_t)((row >> 5) * 32 + (l >> 1)) * 64 + (row & 31) + 32 * (l & 1)) * 16) = ov;
        }
        if (b < 192) {
            asm volatile("" : "+v"(wv0), "+v"(wv1), "+v"(cv0), "+v"(cv1));
            float a8[8];
            const float c0 = bf2f((u16)cv0), c1 = bf2f((u16)cv1);
            const float sc0 = bfr(c0 / (1.f + __expf(-c0))), sc1 = bfr(c1 / (1.f + __expf(-c1)));
#pragma unroll
            for (int j = 0; j < 4; ++j) { a8[2 * j] = sc0 * bflo(wv0[j]) + sc1 * bflo(wv1[j]); a8[2 * j + 1] = sc0 * bfhi(wv0[j]) + sc1 * bfhi(wv1[j]); }
#pragma unroll
            for (int m = 2; m < 64; m <<= 1)
#pragma unroll
                for (int j = 0; j < 8; ++j) a8[j] += __shfl_xor(a8[j], m);
            if (l < 2) {
#pragma unroll
                for (int j = 0; j < 8; ++j) scr[(w * 2 + l) * 8 + j] = a8[j];
            }
            BAR_LGKM();
            if (tid < 16) {
                float s = 0.f;
#pragma unroll
                for (int ww = 0; ww < 8; ++ww) s += scr[(ww * 2 + (tid >> 3)) * 8 + (tid & 7)];
                const int col = 16 * b + tid; const float mv = bfr(s);
                ((float*)(ws + WS_MOD))[col] = mv;
                if (col < 512) ((float*)(ws + WS_GS))[512 + col] = mv;
                else if (col < 1024) ((float*)(ws + WS_GS))[col - 512] = bfr(1.f + mv);
            }
        }
    }
    f32x16 acc[4][2];
#pragma unroll
    for (int a = 0; a < 4; ++a) { acc[a][0] = f32x16{}; acc[a][1] = f32x16{}; }
    const int q4 = (l & 15) >> 2, p4 = l & 3, blk = (l >> 4) & 1;
    const int xa = (l >> 1) & 7;
    STAMP_AT(kvimg, 0, 1);
    for (int kt = 0; kt < 8; ++kt) {
        WAIT_ALL_BAR();
        if (kt == 0) STAMP_AT(kvimg, 0, 2);
        const bool pre = kt + 1 < 8;
        const int ktn = (rot + kt + 1) & 7;
        const unsigned bb = lds0 + ((kt + 1) & 1) * 65536;
        lds_cptr ab = (lds_cptr)lds + (kt & 1) * 65536;
        lds_cptr wb = ab + 32768 + (wc >> 1) * 16384;
#pragma unroll
        for (int s = 0; s < 4; ++s) {
            bf16x8 af[4], wf[2];
#pragma unroll
            for (int kb = 0; kb < 4; ++kb) af[kb] = ldsb128(ab + (128 * wk + 32 * kb + r32) * 128 + (((2 * s + hi) ^ xa) << 4));
#pragma unroll
            for (int j = 0; j < 2; ++j) {
                const int db = 2 * (wc & 1) + j;
                lds_cptr p = wb + (16 * s + 8 * hi + q4) * 256 + 64 * (db ^ q4) + 32 * blk + 16 * (p4 >> 1) + 8 * (p4 & 1);
                wf[j] = wfrag(p, p + 1024);
            }
#pragma unroll
            for (int kb = 0; kb < 4; ++kb) {
#pragma unroll
                for (int j = 0; j < 2; ++j) {
                    if (VT) acc[kb][j] = MFMA(af[kb], wf[j], acc[kb][j]);
                    else acc[kb][j] = MFMA(wf[j], af[kb], acc[kb][j]);
                }
                if ((kb & 1) == 0) { if (pre) K1_PIECE(2 * s + (kb >> 1), ktn, bb); }
            }
        }
    }
#undef K1_PIECE
    STAMP_AT(kvimg, 0, 3);
    const int head = (wc >> 1) ? h1 : h0;
#pragma unroll
    for (int kb = 0; kb < 4; ++kb) {
        const int key = key0 + 128 * wk + 32 * kb, tile = key >> 6, kbl = kb & 1;
        char* tb = kvimg + ((size_t)(head * 32 + tile) << 15);
#pragma unroll
        for (int j = 0; j < 2; ++j) {
            const int db = 2 * (wc & 1) + j;
#pragma unroll
            for (int s2 = 0; s2 < 2; ++s2) {
                const int fo = VT ? (16384 + (db * 4 + kbl * 2 + s2) * 1024) : ((kbl * 8 + db * 2 + s2) * 1024);
                *(bf16x8*)(tb + fo + l * 16) = pack8(acc[kb][j], s2);
            }
        }
    }
}

__global__ __launch_bounds__(512, 2) void k1_kvproj(const u16* __restrict__ ckv, const u16* __restrict__ wuk, const u16* __restrict__ wuv,
                                                    const u16* __restrict__ x, const u16* __restrict__ cvec, const u16* __restrict__ wmod,
                                                    char* __restrict__ ws) {
    extern __shared__ __attribute__((aligned(16))) char lds[];
    const int b = blockIdx.x, tid = threadIdx.x, l = tid & 63;
    const int w = __builtin_amdgcn_readfirstlane(tid >> 6);
    const int xcd = b & 7, r = b >> 3, kb8 = r & 7, combo = r >> 3, type = combo & 1, hp = combo >> 1;
    const int h0 = xcd + 16 * hp, h1 = h0 + 8, key0 = kb8 * 256;
    const u16* W = type ? wuv : wuk;
    STAMP_AT(ws, 0, 0);
    const int rot = (kb8 + 2 * combo) & 7;
    if (type) k1_gemm<true>(ckv, W, ws + WS_KV, lds, key0, h0, h1, rot, x, cvec, wmod, ws);
    else k1_gemm<false>(ckv, W, ws + WS_KV, lds, key0, h0, h1, rot, x, cvec, wmod, ws);
    STAMP_AT(ws, 0, 4);
}

constexpr float ATT_THR = 8.f;
#define PIN(x) asm volatile("" : "+v"(x))
#define MX3(a, b, c) __builtin_fmaxf(__builtin_fmaxf((a), (b)), (c))
DI float att_rowmax(const f32x16& p0, const f32x16& p1) {
    float a = MX3(p0[0], p0[1], p1[0]), b = MX3(p0[2], p0[3], p1[1]); a = MX3(a, p1[2], p1[3]);
#pragma unroll
    for (int r = 4; r < 16; r += 4) { a = MX3(a, p0[r], p0[r + 1]); b = MX3(b, p0[r + 2], p0[r + 3]); a = MX3(a, p1[r], p1[r + 1]); b = MX3(b, p1[r + 2], p1[r + 3]); }
    return __builtin_fmaxf(a, b);
}
DI void att_mask(f32x16& s0, f32x16& s1, int dq) {
#pragma unroll
    for (int r = 0; r < 16; ++r) {
        const int c = (r & 3) + 8 * (r >> 2);
        if (c > dq) s0[r] = -INFINITY;
        if (c + 32 > dq) s1[r] = -INFINITY;
    }
}
DI void att_mask2(f32x16& s0, f32x16& s1, int dq0, int dq1) {
#pragma unroll
    for (int r = 0; r < 16; ++r) {
        const int c = (r & 3) + 8 * (r >> 2);
        if (c > dq0) s0[r] = -INFINITY;
        if (c > dq1) s1[r] = -INFINITY;
    }
}
constexpr int PD = 6;
DI int kfrag_off(int g) { return ((g & 1) * 8 + (g >> 1)) * 1024; }
DI int vfrag_off(int g) { return ((g & 3) * 4 + (g >> 2)) * 1024; }
template <bool MASK, bool PRE>
DI void att_step(f32x16& C0, f32x16& C1, const f32x16& P0, f32x16& P1, f32x16 (&o)[4], f32x16& negm, float& l_run,
                 const bf16x8 (&qf)[8], bf16x8 (&fr)[8], lds_cptr kA, lds_cptr kB, lds_cptr vA, lds_cptr vB, lds_cptr nA, lds_cptr nB, int dq0, int dq1) {
#define KFR(g_) ldsb128((((g_) & 1) ? kB : kA) + ((g_) >> 1) * 1024)
#define VFR(g_) ldsb128((((g_) >> 3) ? vB : vA) + (((g_) & 3) * 4 + (((g_) >> 2) & 1)) * 1024)
#define NFR(g_) ldsb128((((g_) & 1) ? nB : nA) + ((g_) >> 1) * 1024)
    unsigned pw[16];
    float sacc = 0.f, sacc2 = 0.f;
    if (!PRE) {
#pragma unroll
        for (int g = 0; g < PD; ++g) fr[g] = KFR(g);
    }
    SBAR();
#pragma unroll
    for (int g = 0; g < 16; ++g) {
        if ((g & 1) == 0) C0 = MFMA(fr[g & 7], qf[g >> 1], g < 2 ? negm : C0);
        else C1 = MFMA(fr[g & 7], qf[g >> 1], g < 2 ? negm : C1);
        if (g + PD < 16) fr[(g + PD) & 7] = KFR(g + PD);
        else fr[(g + PD) & 7] = VFR(g + PD - 16);
        P1[g] = __builtin_amdgcn_exp2f(P1[g]); PIN(P1[g]);
        sacc += P0[g]; PIN(sacc);
        if (g & 1) { pw[g >> 1] = pk2(P0[g - 1], P0[g]); PIN(pw[g >> 1]); }
        if (g >= 1) { sacc2 += P1[g - 1]; PIN(sacc2); }
        if (g >= 2 && (g & 1) == 0) { pw[8 + ((g - 2) >> 1)] = pk2(P1[g - 2], P1[g - 1]); PIN(pw[8 + ((g - 2) >> 1)]); }
        SBAR();
    }
    sacc2 += P1[15]; pw[15] = pk2(P1[14], P1[15]);
    l_run += sacc + sacc2;
    bool resc = false; float f = 1.f;
    float ma, mb;
#pragma unroll
    for (int g = 0; g < 16; ++g) {
        { const u32x4 pv = {pw[4 * (g >> 2)], pw[4 * (g >> 2) + 1], pw[4 * (g >> 2) + 2], pw[4 * (g >> 2) + 3]};
          o[g & 3] = MFMA(fr[g & 7], __builtin_bit_cast(bf16x8, pv), o[g & 3]); }
        if (g + PD < 16) fr[(g + PD) & 7] = VFR(g + PD);
        else if (PRE) fr[(g + PD) & 7] = NFR(g + PD - 16);
        if (g == 1 && MASK) att_mask2(C0, C1, dq0, dq1);
        if (g == 1) { ma = MX3(C0[0], C0[1], C0[2]); mb = MX3(C0[3], C0[4], C0[5]); ma = MX3(ma, C0[6], C0[7]); mb = MX3(mb, C0[8], C0[9]); PIN(ma); PIN(mb); }
        if (g == 2) { ma = MX3(ma, C0[10], C0[11]); mb = MX3(mb, C0[12], C0[13]); ma = MX3(ma, C0[14], C0[15]); mb = MX3(mb, C1[0], C1[1]); PIN(ma); PIN(mb); }
        if (g == 3) { ma = MX3(ma, C1[2], C1[3]); mb = MX3(mb, C1[4], C1[5]); ma = MX3(ma, C1[6], C1[7]); mb = MX3(mb, C1[8], C1[9]); PIN(ma); PIN(mb); }
        if (g == 4) {
            ma = MX3(ma, C1[10], C1[11]); mb = MX3(mb, C1[12], C1[13]); ma = MX3(ma, C1[14], C1[15]);
            const float rm = __builtin_fmaxf(ma, mb);
            if (__builtin_expect(__any(rm > ATT_THR), 0)) {
                const float dl = __builtin_fmaxf(half_swap_max(rm), 0.f);
                f = __builtin_amdgcn_exp2f(-dl); l_run *= f; resc = true;
#pragma unroll
                for (int r = 0; r < 16; ++r) { C0[r] -= dl; C1[r] -= dl; negm[r] -= dl; }
            }
        }
        if (g >= 5) {
            const int e0 = g < 10 ? 2 * (g - 5) : 10 + (g - 10), e1 = g < 10 ? e0 + 2 : e0 + 1;
#pragma unroll
            for (int e = e0; e < e1; ++e) { C0[e] = __builtin_amdgcn_exp2f(C0[e]); PIN(C0[e]); }
        }
        SBAR();
    }
    if (__builtin_expect(resc, 0)) {
#pragma unroll
        for (int d = 0; d < 4; ++d)
#pragma unroll
            for (int r = 0; r < 16; ++r) o[d][r] *= f;
    }
#undef KFR
#undef VFR
#undef NFR
}
constexpr int K2_LDS = 147456 + 4096;
constexpr float QSCALE = 0.08838834764831845f * 1.4426950408889634f;
__global__ __launch_bounds__(512, 2) void k2_attn(const u16* __restrict__ wq, const u16* __restrict__ cvec, const u16* __restrict__ wmod,
                                                  const u16* __restrict__ wo, const u16* __restrict__ w1, const u16* __restrict__ w2, char* __restrict__ ws) {
    extern __shared__ __attribute__((aligned(16))) char lds[];
    const int b = blockIdx.x, tid = threadIdx.x, l = tid & 63, r32 = l & 31, hi = l >> 5;
    const int w = __builtin_amdgcn_readfirstlane(tid >> 6);
    const int xcd = b & 7, kk = b >> 3, head = xcd + 8 * (kk >> 3), pi = kk & 7;
    const int qb = (w < 4) ? pi : 15 - pi, sub = w & 3, rb = qb * 4 + sub;
    const int tl = 2 * qb + (sub >> 1), NT = 32 - 2 * pi;
    const unsigned lds0 = (unsigned)(uintptr_t)lds;
    STAMP_AT(ws, 1, 0);
    const char* kvh = ws + WS_KV + ((size_t)head << 20);
    const char* ksrc = kvh + w * 2048 + l * 16;
    const unsigned kdst = lds0 + w * 2048, vdst = lds0 + 65536 + w * 2048;
#define DMA_K(t_, slot_) do { const char* g_ = ksrc + (size_t)(t_) * 32768; glds16(g_, kdst + (slot_)); glds16(g_ + 1024, kdst + (slot_) + 1024); } while (0)
#define DMA_V(t_, slot_) do { const char* g_ = ksrc + (size_t)(t_) * 32768 + 16384; glds16(g_, vdst + (slot_)); glds16(g_ + 1024, vdst + (slot_) + 1024); } while (0)
    DMA_K(0, 0);
    glds16(ws + WS_GS + (w & 3) * 1024 + l * 16, lds0 + 147456 + (w & 3) * 1024);
    u32x4 xf[16];
    const char* xsrc = ws + WS_XH + ((size_t)(rb * 32) * 64 + l) * 16;
#define XISSUE(c) do { _Pragma("unroll") for (int k8 = 0; k8 < 8; ++k8) asm volatile("global_load_dwordx4 %0, %1, off" : "=v"(xf[8 * ((c) & 1) + k8]) : "v"(xsrc + (8 * (c) + k8) * 1024) : "memory"); } while (0)
#define WISSUE(c) do { _Pragma("unroll") for (int n = 0; n < 4; ++n) { const int P = (4 * (c) + n) * 8 + w, row = 4 * P + (l >> 4), cc = (l & 15) ^ ((row & 3) << 2); \
            glds16(wq + (size_t)row * 4096 + head * 128 + cc * 8, lds0 + 16384 + P * 1024); } } while (0)
    XISSUE(0); WISSUE(0); XISSUE(1); WISSUE(1);
    const int q4 = (l & 15) >> 2, p4 = l & 3, blk = (l >> 4) & 1;
    const lds_cptr gsp = (lds_cptr)lds + 147456 + 32 * hi;
    lds_cptr wp[4];
#pragma unroll
    for (int db = 0; db < 4; ++db) wp[db] = (lds_cptr)lds + 16384 + (8 * hi + q4) * 256 + 64 * (db ^ q4) + 32 * blk + 16 * (p4 >> 1) + 8 * (p4 & 1);
    bf16x8 qf[8];
    {
        f32x16 qa[4];
#pragma unroll
        for (int d = 0; d < 4; ++d) qa[d] = f32x16{};
#define XWAIT(N, c) asm volatile("s_waitcnt vmcnt(" #N ") lgkmcnt(0)\n\ts_barrier" : "+v"(xf[8 * c]), "+v"(xf[8 * c + 1]), "+v"(xf[8 * c + 2]), "+v"(xf[8 * c + 3]), \
                                 "+v"(xf[8 * c + 4]), "+v"(xf[8 * c + 5]), "+v"(xf[8 * c + 6]), "+v"(xf[8 * c + 7]) :: "memory")
#define XONE(c, k8) asm volatile("global_load_dwordx4 %0, %1, off" : "=v"(xf[8 * ((c) & 1) + (k8)]) : "v"(xsrc + (8 * (c) + (k8)) * 1024) : "memory")
#define WONE(c, n) do { const int P = (4 * (c) + (n)) * 8 + w, row = 4 * P + (l >> 4), cc = (l & 15) ^ ((row & 3) << 2); \
            glds16(wq + (size_t)row * 4096 + head * 128 + cc * 8, lds0 + 16384 + P * 1024); } while (0)
#define KVONE(off_g, dst_) glds16(ksrc + (off_g), (dst_))
#define LDF4(p_) (*(const __attribute__((address_space(3))) f32x4*)(p_))
#define HPART(j_, G_, S_, e_) pk2(fmaf(bflo(xw[j_]), G_[e_], S_[e_]), fmaf(bfhi(xw[j_]), G_[e_ + 1], S_[e_ + 1]))
#pragma unroll
        for (int c = 0; c < 4; ++c) {
            STAMPX(ws, 2 * c);
            if (c == 0) { XWAIT(12, 0); STAMP_AT(ws, 1, 1); }
            if (c == 1) { XWAIT(4, 1); }
            if (c == 2) { XWAIT(8, 0); }
            if (c == 3) { XWAIT(2, 1); }
            STAMPX(ws, 2 * c + 1);
            bf16x8 wf[2][4]; f32x4 g0, g1, s0, s1; u32x4 hc;
            {
                const int ks = 8 * c;
                g0 = LDF4(gsp + 64 * ks); g1 = LDF4(gsp + 64 * ks + 16); s0 = LDF4(gsp + 2048 + 64 * ks); s1 = LDF4(gsp + 2048 + 64 * ks + 16);
#pragma unroll
                for (int db = 0; db < 4; ++db) wf[0][db] = wfrag(wp[db] + ks * 4096, wp[db] + ks * 4096 + 1024);
                const u32x4 xw = xf[8 * (c & 1)];
                hc = (u32x4){HPART(0, g0, s0, 0), HPART(1, g0, s0, 2), HPART(2, g1, s1, 0), HPART(3, g1, s1, 2)};
                g0 = LDF4(gsp + 64 * (ks + 1)); g1 = LDF4(gsp + 64 * (ks + 1) + 16); s0 = LDF4(gsp + 2048 + 64 * (ks + 1)); s1 = LDF4(gsp + 2048 + 64 * (ks + 1) + 16);
            }
            SBAR();
#pragma unroll
            for (int k8 = 0; k8 < 8; ++k8) {
                const int ks = 8 * c + k8;
                const bf16x8 hf = __builtin_bit_cast(bf16x8, hc);
                u32x4 hn = hc;
                const u32x4 xw = xf[8 * (c & 1) + (k8 < 7 ? k8 + 1 : k8)];
#pragma unroll
                for (int db = 0; db < 4; ++db) {
                    qa[db] = MFMA(wf[k8 & 1][db], hf, qa[db]);
                    {
                        const int ix = k8 * 4 + db;
                        if (c == 0) { if (ix < 4) WONE(2, ix); }
                        if (c == 1) { if (ix < 8) XONE(2, ix); else if (ix < 12) WONE(3, ix - 8);
                                      else if (ix < 16) KVONE((size_t)(1 + ((ix - 12) >> 1)) * 32768 + ((ix - 12) & 1) * 1024, kdst + 16384 * (1 + ((ix - 12) >> 1)) + ((ix - 12) & 1) * 1024); }
                        if (c == 2) { if (ix < 8) XONE(3, ix); else if (ix < 10) KVONE(16384 + (ix - 8) * 1024, vdst + (ix - 8) * 1024); }
                        if (c == 3) { if (ix < 2) KVONE(32768 + 16384 + ix * 1024, vdst + 16384 + ix * 1024); }
                    }
                    if (k8 < 7) {
                        wf[(k8 + 1) & 1][db] = wfrag(wp[db] + (ks + 1) * 4096, wp[db] + (ks + 1) * 4096 + 1024);
                        if (db == 0) hn[0] = HPART(0, g0, s0, 0);
                        if (db == 1) hn[1] = HPART(1, g0, s0, 2);
                        if (db == 2) hn[2] = HPART(2, g1, s1, 0);
                        if (db == 3) { hn[3] = HPART(3, g1, s1, 2); }
                        PIN(hn);
                        if (db == 1 && k8 < 6) { g0 = LDF4(gsp + 64 * (ks + 2)); s0 = LDF4(gsp + 2048 + 64 * (ks + 2)); }
                        if (db == 3 && k8 < 6) { g1 = LDF4(gsp + 64 * (ks + 2) + 16); s1 = LDF4(gsp + 2048 + 64 * (ks + 2) + 16); }
                    }
                    SBAR();
                }
                hc = hn;
            }
        }
#undef XWAIT
#undef XISSUE
#undef WISSUE
#undef HPART
#undef XONE
#undef WONE
#undef KVONE
#pragma unroll
        for (int db = 0; db < 4; ++db) {
#pragma unroll
            for (int r = 0; r < 16; ++r) qa[db][r] *= QSCALE;
            qf[2 * db] = pack8(qa[db], 0); qf[2 * db + 1] = pack8(qa[db], 1);
        }
    }
    STAMP_AT(ws, 1, 2); STAMPX(ws, 8);
#define WAIT_BAR4() asm volatile("s_waitcnt vmcnt(4)\n\ts_barrier" ::: "memory")
    float l_run = 0.f;
    f32x16 o[4], negm;
#pragma unroll
    for (int d = 0; d < 4; ++d) o[d] = f32x16{};
    const int qrow = rb * 32 + r32;
    const lds_cptr kp0 = (lds_cptr)lds + l * 16, vp0 = (lds_cptr)lds + 65536 + l * 16;
    f32x16 A0, A1, B0, B1;
    bf16x8 pf0, pf1, pf2, pf3, fr[8];
    int sl_prev = 49152, sl_cur = 0;
#define ROT() do { sl_prev = sl_cur; sl_cur = (sl_cur + 16384) & 65535; } while (0)
#define SLOT(t_) (((t_) & 3) * 16384)
#define CL(t_) ((t_) < NT ? (t_) : NT - 1)
    const int tsw = 2 * pi + 3;
    const bool split = pi <= 6;
#define STAGE_ISSUE(t_) do { DMA_K(CL((t_) + 3), sl_prev); DMA_V(CL((t_) + 2), (sl_cur + 32768) & 65535); } while (0)
#define FULL_ARGS kp0 + sl_cur, kp0 + sl_cur + 8192, vp0 + sl_prev, vp0 + sl_prev + 2048, kp0 + ((sl_cur + 16384) & 65535), kp0 + ((sl_cur + 16384) & 65535) + 8192
#define DRAIN(X0, X1, vA_, vB_) do { float sacc_ = 0.f; \
        _Pragma("unroll") for (int r = 0; r < 16; ++r) { X1[r] = __builtin_amdgcn_exp2f(X1[r]); sacc_ += X0[r] + X1[r]; } \
        l_run += sacc_; pf0 = pack8(X0, 0); pf1 = pack8(X0, 1); pf2 = pack8(X1, 0); pf3 = pack8(X1, 1); const lds_cptr va_ = (vA_), vb_ = (vB_); \
        _Pragma("unroll") for (int d = 0; d < 4; ++d) { o[d] = MFMA(ldsb128(va_ + (d * 4) * 1024), pf0, o[d]); o[d] = MFMA(ldsb128(va_ + (d * 4 + 1) * 1024), pf1, o[d]); \
            o[d] = MFMA(ldsb128(vb_ + (d * 4) * 1024), pf2, o[d]); o[d] = MFMA(ldsb128(vb_ + (d * 4 + 1) * 1024), pf3, o[d]); } } while (0)
#define STORE_O(rb_, inv_) do { char* cb_ = ws + WS_CTX + ((size_t)((rb_) * 256 + head * 8) * 64 + l) * 16; \
        _Pragma("unroll") for (int d = 0; d < 4; ++d) { _Pragma("unroll") for (int r = 0; r < 16; ++r) o[d][r] *= (inv_); \
            *(bf16x8*)(cb_ + (2 * d) * 1024) = pack8(o[d], 0); *(bf16x8*)(cb_ + (2 * d + 1) * 1024) = pack8(o[d], 1); } } while (0)
    asm volatile("s_waitcnt vmcnt(2) lgkmcnt(0)\n\ts_barrier" ::: "memory");
    if (split && w >= 4) {
#pragma unroll
        for (int ds = 0; ds < 8; ++ds) *(bf16x8*)(ws + WS_QX + ((size_t)(b * 4 + sub) * 8 + ds) * 1024 + l * 16) = qf[ds];
    }
    STAGE_ISSUE(0);
    {
        A0 = f32x16{}; A1 = f32x16{};
#pragma unroll
        for (int ds = 0; ds < 8; ++ds) {
            A0 = MFMA(ldsb128(kp0 + ds * 1024), qf[ds], A0);
            A1 = MFMA(ldsb128(kp0 + 8192 + ds * 1024), qf[ds], A1);
        }
        if (tl == 0) att_mask(A0, A1, qrow - 4 * hi);
        const float mx = half_swap_max(att_rowmax(A0, A1));
#pragma unroll
        for (int r = 0; r < 16; ++r) { A0[r] = __builtin_amdgcn_exp2f(A0[r] - mx); A1[r] = A1[r] - mx; negm[r] = -mx; }
#pragma unroll
        for (int g = 0; g < PD; ++g) fr[g] = ldsb128(kp0 + 16384 + kfrag_off(g));
    }
    asm volatile("s_waitcnt vmcnt(4) lgkmcnt(0)\n\ts_barrier" ::: "memory"); ROT();
    const bool lsplit = split && w >= 4;
    const int tfull = lsplit ? tsw : tl;
    int t = 1;
    for (; t + 1 < tfull; t += 2) {
        STAGE_ISSUE(t);     att_step<false, true>(B0, B1, A0, A1, o, negm, l_run, qf, fr, FULL_ARGS, 0, 0); WAIT_BAR4(); ROT();
        STAGE_ISSUE(t + 1); att_step<false, true>(A0, A1, B0, B1, o, negm, l_run, qf, fr, FULL_ARGS, 0, 0); WAIT_BAR4(); ROT();
    }
    if (t < tfull) {
        STAGE_ISSUE(t); att_step<false, true>(B0, B1, A0, A1, o, negm, l_run, qf, fr, FULL_ARGS, 0, 0); WAIT_BAR4(); ROT();
        A0 = B0; A1 = B1; ++t;
    }
    if (!lsplit) {
        if (tl >= 1) {
            STAGE_ISSUE(t); att_step<true, true>(B0, B1, A0, A1, o, negm, l_run, qf, fr, FULL_ARGS, qrow - t * 64 - 4 * hi, qrow - t * 64 - 4 * hi - 32); WAIT_BAR4(); ROT();
            ++t;
        } else { B0 = A0; B1 = A1; }
        if (t < NT) STAGE_ISSUE(t);
        DRAIN(B0, B1, vp0 + sl_prev, vp0 + sl_prev + 2048);
        STAMP_AT(ws, 1, 3);
        { const float inv = 1.f / half_swap_sum(l_run); STORE_O(rb, inv); }
        if (w < 4 && pi <= 5) {
#pragma unroll
            for (int j = 0; j < 2; ++j) {
                const int ch = (head * 6 + pi) * 4 + w + 768 * j;
                if (ch < 960) {
                    const char* src = ch < 512 ? (const char*)wo + (size_t)ch * 8192 : ch < 736 ? (const char*)w1 + (size_t)(ch - 512) * 8192 : (const char*)w2 + (size_t)(ch - 736) * 8192;
                    glds4(src + l * 128, lds0 + 147456 + 2048);
                }
            }
        }
        if (t < NT) { WAIT_ALL_BAR(); ROT(); ++t; }
        const int tstop = split ? tsw : NT;
        for (; t < tstop; ++t) { STAGE_ISSUE(t); WAIT_BAR4(); ROT(); }
    }
    if (split) {
        const int kb = w >= 4 ? 0 : 1, rowl = ((15 - pi) * 4 + sub) * 32 + r32 - 32 * kb - 4 * hi;
        const lds_cptr kph = kp0 + kb * 8192, vph = vp0 + kb * 2048;
#define STAGE2(t_) do { DMA_K(CL((t_) + 2), SLOT((t_) + 2)); DMA_K(CL((t_) + 3), SLOT((t_) + 3)); DMA_V(CL(t_), SLOT(t_)); DMA_V(CL((t_) + 1), SLOT((t_) + 1)); } while (0)
#define SPLIT_ARGS(t_) kph + SLOT(t_), kph + SLOT((t_) + 1), vph + SLOT((t_) - 2), vph + SLOT((t_) - 1), kph, kph
#define WAIT0_BAR() asm volatile("s_waitcnt vmcnt(0)\n\ts_barrier" ::: "memory")
        if (kb == 0) { DRAIN(A0, A1, vp0 + sl_prev, vp0 + sl_prev + 2048); }
        else {
#pragma unroll
            for (int ds = 0; ds < 8; ++ds) qf[ds] = *(const bf16x8*)(ws + WS_QX + ((size_t)(b * 4 + sub) * 8 + ds) * 1024 + l * 16);
#pragma unroll
            for (int d = 0; d < 4; ++d) o[d] = f32x16{};
            l_run = 0.f; negm = f32x16{};
        }
        STAGE2(t);
        {
            A0 = negm; A1 = negm;
#pragma unroll
            for (int ds = 0; ds < 8; ++ds) {
                A0 = MFMA(ldsb128(kph + SLOT(t) + ds * 1024), qf[ds], A0);
                A1 = MFMA(ldsb128(kph + SLOT(t + 1) + ds * 1024), qf[ds], A1);
            }
            const float rm = half_swap_max(att_rowmax(A0, A1));
            const float dl = kb == 1 ? rm : (rm > ATT_THR ? rm : 0.f);
            const float f = kb == 1 ? 1.f : __builtin_amdgcn_exp2f(-dl);
            l_run *= f;
#pragma unroll
            for (int r = 0; r < 16; ++r) { A0[r] = __builtin_amdgcn_exp2f(A0[r] - dl); A1[r] -= dl; negm[r] -= dl; }
#pragma unroll
            for (int d = 0; d < 4; ++d)
#pragma unroll
                for (int r = 0; r < 16; ++r) o[d][r] *= f;
        }
        WAIT0_BAR(); t += 2;
        for (; t + 6 < NT; t += 4) {
            STAGE2(t);     att_step<false, false>(B0, B1, A0, A1, o, negm, l_run, qf, fr, SPLIT_ARGS(t), 0, 0); WAIT0_BAR();
            STAGE2(t + 2); att_step<false, false>(A0, A1, B0, B1, o, negm, l_run, qf, fr, SPLIT_ARGS(t + 2), 0, 0); WAIT0_BAR();
        }
        for (; t < NT; t += 2) {
            STAGE2(t); att_step<true, false>(B0, B1, A0, A1, o, negm, l_run, qf, fr, SPLIT_ARGS(t), rowl - 64 * t, rowl - 64 * t - 64); WAIT0_BAR();
            A0 = B0; A1 = B1;
        }
        DRAIN(A0, A1, vph + SLOT(t - 2), vph + SLOT(t - 1));
        BAR_LGKM();
        float* mg = (float*)lds + sub * (66 * 64) + l;
        const float lsum = half_swap_sum(l_run);
        if (kb == 1) {
#pragma unroll
            for (int d = 0; d < 4; ++d)
#pragma unroll
                for (int r = 0; r < 16; ++r) mg[(d * 16 + r) * 64] = o[d][r];
            mg[64 * 64] = negm[0]; mg[65 * 64] = lsum;
        }
        BAR_LGKM();
        if (kb == 0) {
            const float nmh = mg[64 * 64], lh = mg[65 * 64], nml = negm[0];
            const float nm = __builtin_fminf(nmh, nml);
            const float fl = __builtin_amdgcn_exp2f(nm - nml), fh = __builtin_amdgcn_exp2f(nm - nmh);
            const float inv = 1.f / (lsum * fl + lh * fh);
            const float cl = fl * inv, ch = fh * inv;
#pragma unroll
            for (int d = 0; d < 4; ++d)
#pragma unroll
                for (int r = 0; r < 16; ++r) o[d][r] = o[d][r] * cl + mg[(d * 16 + r) * 64] * ch;
            STORE_O(rb, 1.f);
        }
    }
    asm volatile("s_waitcnt vmcnt(0)" ::: "memory");
    STAMP_AT(ws, 1, 4);
#undef DMA_K
#undef DMA_V
#undef ROT
#undef SLOT
#undef CL
#undef STAGE_ISSUE
#undef FULL_ARGS
#undef DRAIN
#undef STORE_O
#undef STAGE2
#undef SPLIT_ARGS
#undef WAIT0_BAR
}

template <int TQ, int TN, int KW, int BKS, bool APERM, bool CT>
struct G {
    static constexpr int NWAVE = TQ * TN * KW, RB = TN * 128, A_BYTES = 2 * TQ * BKS * 1024, W_BYTES = 16 * BKS * RB, STAGE = A_BYTES + W_BYTES;
    static constexpr int CPR = RB / 16;
    static_assert(STAGE == 32768 && (2 * TQ * BKS + W_BYTES / 1024) / NWAVE == 4, "4 glds per wave per stage, 4 x 32 KB ring");
    static DI int swz(int row) { return RB == 256 ? ((row & 3) << 2) : (((row >> 1) & 1) << 2); }
    static DI void stage(const char* AF, size_t a_rb_stride, const u16* Wp, int ldw, int kst, unsigned ldsaddr, int w, int l) {
#pragma unroll
        for (int p0 = 0; p0 < 2 * TQ * BKS; p0 += NWAVE) {
            const int p = p0 + w, rbi = p / BKS, ks = p % BKS;
            glds16(AF + rbi * a_rb_stride + (size_t)(kst * BKS + ks) * 1024 + l * 16, ldsaddr + p * 1024);
        }
#pragma unroll
        for (int p0 = 0; p0 < W_BYTES / 1024; p0 += NWAVE) {
            const int p = p0 + w, row = p * (1024 / RB) + l / CPR, c = (l % CPR) ^ swz(row);
            glds16(Wp + (size_t)(kst * BKS * 16 + row) * ldw + c * 8, ldsaddr + A_BYTES + p * 1024);
        }
    }
    static constexpr int NPA = 2 * TQ * BKS / NWAVE, NPW = W_BYTES / 1024 / NWAVE;
    static DI void stage_piece(int i, const char* AF, size_t a_rb_stride, const u16* Wp, int ldw, int kst, unsigned ldsaddr, int w, int l) {
        if (i < NPA) {
            const int p = i * NWAVE + w, rbi = p / BKS, ks = p % BKS;
            glds16(AF + rbi * a_rb_stride + (size_t)(kst * BKS + ks) * 1024 + l * 16, ldsaddr + p * 1024);
        } else {
            const int p = (i - NPA) * NWAVE + w, row = p * (1024 / RB) + l / CPR, c = (l % CPR) ^ swz(row);
            glds16(Wp + (size_t)(kst * BKS * 16 + row) * ldw + c * 8, ldsaddr + A_BYTES + p * 1024);
        }
    }
    template <bool RM, class EPI>
    static DI void run(EPI epi, const char* AF, size_t a_rb_stride, const u16* Wp, int ldw, int nstage, int rot, char* lds, float (&red)[64 / KW], int& tq, int& tn, int& kw, char* ws, int kid) {
        const int tid = threadIdx.x, l = tid & 63, hi = l >> 5;
        STAMP_AT(ws, kid, 0);
        const int w = __builtin_amdgcn_readfirstlane(tid >> 6);
        kw = w % KW; const int tt = w / KW; tq = tt / TN; tn = tt % TN;
        const unsigned lds0 = (unsigned)(uintptr_t)lds;
        const int q4 = (l & 15) >> 2, p4 = l & 3, blk = (l >> 4) & 1;
        f32x16 acc[2][2];
#pragma unroll
        for (int i = 0; i < 2; ++i) { acc[i][0] = f32x16{}; acc[i][1] = f32x16{}; }
        int kst = rot;
#define NEXTK() do { kst = (kst + 1 == nstage) ? 0 : kst + 1; } while (0)
        stage(AF, a_rb_stride, Wp, ldw, kst, lds0, w, l); NEXTK();
        stage(AF, a_rb_stride, Wp, ldw, kst, lds0 + STAGE, w, l); NEXTK();
        stage(AF, a_rb_stride, Wp, ldw, kst, lds0 + 2 * STAGE, w, l); NEXTK();
        for (int st = 0; st < nstage; ++st) {
            if (st + 2 < nstage) asm volatile("s_waitcnt vmcnt(8) lgkmcnt(0)\n\ts_barrier" ::: "memory");
            else if (st + 1 < nstage) asm volatile("s_waitcnt vmcnt(4) lgkmcnt(0)\n\ts_barrier" ::: "memory");
            else WAIT_ALL_BAR();
            if (st == 0) STAMP_AT(ws, kid, 1);
            const bool pre = st + 3 < nstage; const unsigned pdst = lds0 + ((st + 3) & 3) * STAGE;
            lds_cptr ab = (lds_cptr)lds + (st & 3) * STAGE;
            lds_cptr wb = ab + A_BYTES;
#pragma unroll
            for (int kk = 0; kk < BKS / KW; ++kk) {
                const int ks = kw * (BKS / KW) + kk;
                bf16x8 af[2], wf[2];
#pragma unroll
                for (int i = 0; i < 2; ++i) af[i] = ldsb128(ab + ((2 * tq + i) * BKS + ks) * 1024 + l * 16);
#pragma unroll
                for (int j = 0; j < 2; ++j) {
                    const int r0 = 16 * ks + (APERM ? 4 * hi : 8 * hi) + q4, r1 = r0 + (APERM ? 8 : 4);
                    const int ch = 4 * (2 * tn + j) + 2 * blk + (p4 >> 1);
                    wf[j] = wfrag(wb + r0 * RB + ((ch ^ swz(r0)) << 4) + 8 * (p4 & 1), wb + r1 * RB + ((ch ^ swz(r1)) << 4) + 8 * (p4 & 1));
                }
#pragma unroll
                for (int i = 0; i < 2; ++i)
#pragma unroll
                    for (int j = 0; j < 2; ++j) {
                        if (CT) acc[i][j] = MFMA(wf[j], af[i], acc[i][j]);
                        else acc[i][j] = MFMA(af[i], wf[j], acc[i][j]);
                        constexpr int PER = 4 / (BKS / KW);
                        const int m = i * 2 + j;
                        if (PER == 4) { SBAR(); if (pre) stage_piece(m, AF, a_rb_stride, Wp, ldw, kst, pdst, w, l); SBAR(); }
                        else if (m & 1) { SBAR(); if (pre) stage_piece(kk * 2 + (m >> 1), AF, a_rb_stride, Wp, ldw, kst, pdst, w, l); SBAR(); }
                    }
            }
            if (pre) NEXTK();
        }
        BAR_LGKM();
        STAMP_AT(ws, kid, 2);
        float* rb_ = (float*)lds;
        constexpr int RS = RM ? 68 : 64;
#pragma unroll
        for (int i = 0; i < 2; ++i)
#pragma unroll
            for (int j = 0; j < 2; ++j)
#pragma unroll
                for (int r = 0; r < 16; ++r) rb_[(w * 64 + (i * 2 + j) * 16 + r) * RS + l] = acc[i][j][r];
        BAR_LGKM();
        if constexpr (RM) {
            constexpr int C8 = TN * 8, NITEM = 64 * TQ * C8 / (64 * NWAVE);
#pragma unroll
            for (int it = 0; it < NITEM; ++it) {
                const int id = it * (64 * NWAVE) + tid, row = id / C8, c8 = id % C8;
                const int rr = row & 31, r = (rr & 3) + 4 * (rr >> 3), h = (rr >> 2) & 1;
                const int t2 = (row >> 6) * TN + (c8 >> 3), reg = (((row >> 5) & 1) * 2 + ((c8 >> 2) & 1)) * 16 + r, ln = (c8 & 3) * 8 + 32 * h;
                f32x4 a0 = {0.f, 0.f, 0.f, 0.f}, a1 = a0;
#pragma unroll
                for (int k2 = 0; k2 < KW; ++k2) {
                    const float* q_ = rb_ + ((t2 * KW + k2) * 64 + reg) * RS + ln;
                    a0 += *(const f32x4*)q_; a1 += *(const f32x4*)(q_ + 4);
                }
                epi(row, c8 * 8, a0, a1);
            }
        } else {
#pragma unroll
            for (int e = 0; e < 64 / KW; ++e) {
                float s = 0.f;
#pragma unroll
                for (int k2 = 0; k2 < KW; ++k2) s += rb_[((tt * KW + k2) * 64 + kw * (64 / KW) + e) * RS + l];
                red[e] = s;
            }
        }
        STAMP_AT(ws, kid, 3);
    }
};

typedef G<2, 2, 2, 4, true, false> G3;
constexpr int K3_LDS = 139264;
__global__ __launch_bounds__(512, 2) void k3_oproj(const u16* __restrict__ wo, char* __restrict__ ws) {
    extern __shared__ __attribute__((aligned(16))) char lds[];
    const int b = blockIdx.x, xcd = b & 7, r = b >> 3, ksl = xcd & 3, rp = (xcd >> 2) * 8 + (r >> 2), ct = r & 3;
    float red[32]; int tq, tn, kw;
    u16* part = (u16*)(ws + WS_PART) + (size_t)ksl * 2048 * 512 + (size_t)(rp * 128) * 512 + ct * 128;
    G3::run<true>([&](int row, int col, f32x4 a0, f32x4 a1) {
                      u32x4 v = {pk2(a0[0], a0[1]), pk2(a0[2], a0[3]), pk2(a1[0], a1[1]), pk2(a1[2], a1[3])};
                      *(u32x4*)(part + (size_t)row * 512 + col) = v; },
                  ws + WS_CTX + ((size_t)(rp * 4) * 256 + ksl * 64) * 1024, (size_t)256 * 1024, wo + (size_t)ksl * 1024 * 512 + ct * 128, 512, 16, (4 * ct + 2 * (r >> 2)) & 15, lds, red, tq, tn, kw, ws, 2);
    STAMP_AT(ws, 2, 4);
}

__global__ __launch_bounds__(512) void k3b_ln2(const u16* __restrict__ x, char* __restrict__ ws) {
    const int l = threadIdx.x & 63, row = blockIdx.x * 8 + (threadIdx.x >> 6);
    const u16* part = (const u16*)(ws + WS_PART) + (size_t)row * 512 + 8 * l;
    const float* mod = (const float*)(ws + WS_MOD);
    u32x4 pv[4];
#pragma unroll
    for (int s = 0; s < 4; ++s) pv[s] = *(const u32x4*)(part + (size_t)s * 2048 * 512);
    const u32x4 xv4 = *(const u32x4*)(x + (size_t)row * 512 + 8 * l);
    const f32x4 g0 = *(const f32x4*)(mod + 1024 + 8 * l), g1 = *(const f32x4*)(mod + 1024 + 8 * l + 4);
    const f32x4 c0 = *(const f32x4*)(mod + 2048 + 8 * l), c1 = *(const f32x4*)(mod + 2048 + 8 * l + 4);
    const f32x4 s0 = *(const f32x4*)(mod + 1536 + 8 * l), s1 = *(const f32x4*)(mod + 1536 + 8 * l + 4);
    float x1[8], xh[8];
#pragma unroll
    for (int j = 0; j < 4; ++j) {
        const int w0 = j >> 1, w1 = 2 + (j >> 1);
        const float a0 = (j & 1) ? (bfhi(pv[0][w0]) + bfhi(pv[1][w0])) + (bfhi(pv[2][w0]) + bfhi(pv[3][w0])) : (bflo(pv[0][w0]) + bflo(pv[1][w0])) + (bflo(pv[2][w0]) + bflo(pv[3][w0]));
        const float a1 = (j & 1) ? (bfhi(pv[0][w1]) + bfhi(pv[1][w1])) + (bfhi(pv[2][w1]) + bfhi(pv[3][w1])) : (bflo(pv[0][w1]) + bflo(pv[1][w1])) + (bflo(pv[2][w1]) + bflo(pv[3][w1]));
        const float xa = (j & 1) ? bfhi(xv4[j >> 1]) : bflo(xv4[j >> 1]), xb = (j & 1) ? bfhi(xv4[2 + (j >> 1)]) : bflo(xv4[2 + (j >> 1)]);
        x1[j] = bfr(xa + bfr(g0[j] * bfr(a0)));
        x1[4 + j] = bfr(xb + bfr(g1[j] * bfr(a1)));
    }
    u32x4 o1 = {pk2(x1[0], x1[1]), pk2(x1[2], x1[3]), pk2(x1[4], x1[5]), pk2(x1[6], x1[7])};
    *(u32x4*)(ws + WS_X1 + ((size_t)row * 512 + 8 * l) * 2) = o1;
    ln_row(x1, xh);
    float h[8];
#pragma unroll
    for (int j = 0; j < 4; ++j) { h[j] = bfr(xh[j] * bfr(1.f + c0[j])) + s0[j]; h[4 + j] = bfr(xh[4 + j] * bfr(1.f + c1[j])) + s1[j]; }
    u32x4 oh = {pk2(h[0], h[1]), pk2(h[2], h[3]), pk2(h[4], h[5]), pk2(h[6], h[7])};
    *(u32x4*)(ws + WS_H2 + ((size_t)((row >> 5) * 32 + (l >> 1)) * 64 + (row & 31) + 32 * (l & 1)) * 16) = oh;
}

typedef G<2, 2, 2, 4, false, true> G4;
__global__ __launch_bounds__(512, 2) void k4_ffn1(const u16* __restrict__ w1, char* __restrict__ ws) {
    extern __shared__ __attribute__((aligned(16))) char lds[];
    const int b = blockIdx.x, xcd = b & 7, jj = b >> 3, rp = 4 * (xcd >> 1) + jj / 7, ct = 7 * (xcd & 1) + jj % 7;
    const int l = threadIdx.x & 63;
    float red[32]; int tq, tn, kw;
    G4::run<false>(0, ws + WS_H2 + (size_t)(rp * 4) * 32 * 1024, (size_t)32 * 1024, w1 + ct * 128, DFF, 8, (jj % 7 + 2 * (jj / 7)) & 7, lds, red, tq, tn, kw, ws, 3);
#pragma unroll
    for (int j = 0; j < 2; ++j)
#pragma unroll
        for (int s = 0; s < 2; ++s) {
            const int e = j * 16 + 8 * s;
            u32x4 v = {pk2(fmaxf(red[e], 0.f), fmaxf(red[e + 1], 0.f)), pk2(fmaxf(red[e + 2], 0.f), fmaxf(red[e + 3], 0.f)),
                       pk2(fmaxf(red[e + 4], 0.f), fmaxf(red[e + 5], 0.f)), pk2(fmaxf(red[e + 6], 0.f), fmaxf(red[e + 7], 0.f))};
            const int rbq = rp * 4 + 2 * tq + kw, kstep = (ct * 128 + 64 * tn + 32 * j) / 16 + s;
            *(u32x4*)(ws + WS_H1 + ((size_t)(rbq * 112 + kstep) * 64 + l) * 16) = v;
        }
    STAMP_AT(ws, 3, 4);
}

typedef G<1, 1, 8, 8, true, false> G5;
__global__ __launch_bounds__(512, 2) void k5_ffn2(const u16* __restrict__ w2, char* __restrict__ ws, u16* __restrict__ out) {
    extern __shared__ __attribute__((aligned(16))) char lds[];
    const int b = blockIdx.x, xcd = b & 7, jj = b >> 3, rp = 8 * (xcd >> 1) + (jj >> 2), ct = 4 * (xcd & 1) + (jj & 3);
    float red[8]; int tq, tn, kw;
    const float* gb = (const float*)(ws + WS_MOD) + 2560 + ct * 64;
    const u16* x1 = (const u16*)(ws + WS_X1) + (size_t)(rp * 64) * 512 + ct * 64;
    u16* op = out + (size_t)(rp * 64) * 512 + ct * 64;
    G5::run<true>([&](int row, int col, f32x4 a0, f32x4 a1) {
                      const u32x4 xv = *(const u32x4*)(x1 + (size_t)row * 512 + col);
                      const f32x4 g0 = *(const f32x4*)(gb + col), g1 = *(const f32x4*)(gb + col + 4);
                      u32x4 v = {pk2(bflo(xv[0]) + bfr(g0[0] * bfr(a0[0])), bfhi(xv[0]) + bfr(g0[1] * bfr(a0[1]))),
                                 pk2(bflo(xv[1]) + bfr(g0[2] * bfr(a0[2])), bfhi(xv[1]) + bfr(g0[3] * bfr(a0[3]))),
                                 pk2(bflo(xv[2]) + bfr(g1[0] * bfr(a1[0])), bfhi(xv[2]) + bfr(g1[1] * bfr(a1[1]))),
                                 pk2(bflo(xv[3]) + bfr(g1[2] * bfr(a1[2])), bfhi(xv[3]) + bfr(g1[3] * bfr(a1[3])))};
                      *(u32x4*)(op + (size_t)row * 512 + col) = v; },
                  ws + WS_H1 + (size_t)(rp * 2) * 112 * 1024, (size_t)112 * 1024, w2 + ct * 64, 512, 14, (3 * (jj & 3) + 2 * (jj >> 2)) % 14, lds, red, tq, tn, kw, ws, 4);
    STAMP_AT(ws, 4, 4);
}

extern "C" void kernel_launch(void* const* d_in, const int* in_sizes, int n_in, void* d_out, int out_size, void* d_ws, size_t ws_size,
                              hipStream_t stream) {
    (void)in_sizes; (void)n_in; (void)out_size; (void)ws_size;
    const u16* x = (const u16*)d_in[0];
    const u16* ckv = (const u16*)d_in[1];
    const u16* wq = (const u16*)d_in[2];
    const u16* wuk = (const u16*)d_in[3];
    const u16* wuv = (const u16*)d_in[4];
    const u16* wo = (const u16*)d_in[5];
    const u16* w1 = (const u16*)d_in[7];
    const u16* w2 = (const u16*)d_in[8];
    const u16* cvec = (const u16*)d_in[9];
    const u16* wmod = (const u16*)d_in[10];
    char* ws = (char*)d_ws;
    hipFuncSetAttribute((const void*)k1_kvproj, hipFuncAttributeMaxDynamicSharedMemorySize, K1_LDS);
    hipFuncSetAttribute((const void*)k2_attn, hipFuncAttributeMaxDynamicSharedMemorySize, K2_LDS);
    hipFuncSetAttribute((const void*)k3_oproj, hipFuncAttributeMaxDynamicSharedMemorySize, K3_LDS);
    hipFuncSetAttribute((const void*)k4_ffn1, hipFuncAttributeMaxDynamicSharedMemorySize, K3_LDS);
    hipFuncSetAttribute((const void*)k5_ffn2, hipFuncAttributeMaxDynamicSharedMemorySize, K3_LDS);
    k1_kvproj<<<256, 512, K1_LDS, stream>>>(ckv, wuk, wuv, x, cvec, wmod, ws);
    k2_attn<<<256, 512, K2_LDS, stream>>>(wq, cvec, wmod, wo, w1, w2, ws);
    k3_oproj<<<256, 512, K3_LDS, stream>>>(wo, ws);
    k3b_ln2<<<256, 512, 0, stream>>>(x, ws);
    k4_ffn1<<<224, 512, K3_LDS, stream>>>(w1, ws);
    k5_ffn2<<<256, 512, K3_LDS, stream>>>(w2, ws, (u16*)d_out);
}
```

```cpp
#include <hip/hip_runtime.h>
#include <hip/hip_bf16.h>
#include <stdint.h>

typedef unsigned short u16;
typedef short bf16x8 __attribute__((ext_vector_type(8)));
typedef short s16x4 __attribute__((ext_vector_type(4)));
typedef short v4i16_t __attribute__((ext_vector_type(4)));
typedef float f32x16 __attribute__((ext_vector_type(16)));
typedef float f32x4 __attribute__((ext_vector_type(4)));
typedef float f32x2 __attribute__((ext_vector_type(2)));
typedef unsigned u32x4 __attribute__((ext_vector_type(4)));
typedef __bf16 bf16x2_t __attribute__((ext_vector_type(2)));
typedef __attribute__((address_space(3))) const char* lds_cptr;
#define DI __device__ __forceinline__
#define MFMA(a, b, c) __builtin_amdgcn_mfma_f32_32x32x16_bf16((a), (b), (c), 0, 0, 0)
#define SBAR() __builtin_amdgcn_sched_barrier(0)
#define STAMPX(ws_, idx) do { } while (0)
#define STAMP_AT(ws_, kid, idx) do { } while (0)

constexpr int SEQ = 2048, DM = 512, NH = 32, HD = 128, DFF = 1792;

constexpr size_t WS_KV = 0;
constexpr size_t WS_XH = WS_KV + (size_t)32 * 32 * 32768;
constexpr size_t WS_MOD = WS_XH + (size_t)64 * 32 * 1024;
constexpr size_t WS_GS = WS_MOD + 12288;
constexpr size_t WS_CTX = WS_MOD + 16384;
constexpr size_t WS_PART = WS_CTX + (size_t)64 * 256 * 1024;
constexpr size_t WS_X1 = WS_PART + (size_t)4 * 2048 * 512 * 4;
constexpr size_t WS_H2 = WS_X1 + (size_t)2048 * 512 * 2;
constexpr size_t WS_H1 = WS_H2 + (size_t)64 * 32 * 1024;

DI unsigned pk2(float a, float b) { f32x2 v = {a, b}; bf16x2_t r = __builtin_convertvector(v, bf16x2_t); return __builtin_bit_cast(unsigned, r); }
DI float bf2f(u16 v) { return __uint_as_float(((unsigned)v) << 16); }
DI float bfr(float x) { return __uint_as_float(pk2(x, 0.f) << 16); }
DI float bflo(unsigned w) { return __uint_as_float(w << 16); }
DI float bfhi(unsigned w) { return __uint_as_float(w & 0xffff0000u); }
DI bf16x8 pack8(const f32x16& a, int s) {
    u32x4 w = {pk2(a[8 * s], a[8 * s + 1]), pk2(a[8 * s + 2], a[8 * s + 3]), pk2(a[8 * s + 4], a[8 * s + 5]), pk2(a[8 * s + 6], a[8 * s + 7])};
    return __builtin_bit_cast(bf16x8, w);
}
DI int crow(int r, int hi) { return (r & 3) + 8 * (r >> 2) + 4 * hi; }
DI void glds16(const void* g, unsigned lds_base) {
    unsigned sv;
    asm volatile("s_mov_b32 %0, m0\n\ts_mov_b32 m0, %2\n\ts_nop 0\n\tglobal_load_lds_dwordx4 %1, off\n\ts_mov_b32 m0, %0"
                 : "=&s"(sv) : "v"(g), "s"(lds_base) : "memory");
}
DI void glds4(const void* g, unsigned lds_base) {
    unsigned sv;
    asm volatile("s_mov_b32 %0, m0\n\ts_mov_b32 m0, %2\n\ts_nop 0\n\tglobal_load_lds_dword %1, off\n\ts_mov_b32 m0, %0"
                 : "=&s"(sv) : "v"(g), "s"(lds_base) : "memory");
}
DI bf16x8 ldsb128(lds_cptr p) { return *(const __attribute__((address_space(3))) bf16x8*)p; }
DI s16x4 ldstr(lds_cptr p) { return __builtin_bit_cast(s16x4, __builtin_amdgcn_ds_read_tr16_b64_v4i16((__attribute__((address_space(3))) v4i16_t*)p)); }
DI bf16x8 wfrag(lds_cptr p0, lds_cptr p1) { s16x4 lo = ldstr(p0), hi = ldstr(p1); return __builtin_shufflevector(lo, hi, 0, 1, 2, 3, 4, 5, 6, 7); }
#define WAIT_ALL_BAR() asm volatile("s_waitcnt vmcnt(0) lgkmcnt(0)\n\ts_barrier" ::: "memory")
#define BAR_LGKM() asm volatile("s_waitcnt lgkmcnt(0)\n\ts_barrier" ::: "memory")
DI float wave_sum(float v) {
#pragma unroll
    for (int m = 1; m < 64; m <<= 1) v += __shfl_xor(v, m);
    return v;
}
DI float half_swap_max(float m) { auto rr = __builtin_amdgcn_permlane32_swap(__float_as_uint(m), __float_as_uint(m), false, false); return fmaxf(__uint_as_float(rr[0]), __uint_as_float(rr[1])); }
DI float half_swap_sum(float m) { auto rr = __builtin_amdgcn_permlane32_swap(__float_as_uint(m), __float_as_uint(m), false, false); return __uint_as_float(rr[0]) + __uint_as_float(rr[1]); }

DI void ln_row(const float (&xv)[8], float (&o)[8]) {
    float s = 0.f;
#pragma unroll
    for (int j = 0; j < 8; ++j) s += xv[j];
    const float mean = wave_sum(s) * (1.f / 512.f);
    float q = 0.f;
#pragma unroll
    for (int j = 0; j < 8; ++j) { const float d = xv[j] - mean; q += d * d; }
    const float var = wave_sum(q) * (1.f / 512.f);
    const float mb = bfr(mean), vb = bfr(var);
    const float den = bfr(sqrtf(bfr(vb + 1e-5f)));
#pragma unroll
    for (int j = 0; j < 8; ++j) o[j] = bfr(bfr(xv[j] - mb) / den);
}

constexpr int K1_LDS = 131072;
template <bool VT>
DI void k1_gemm(const u16* __restrict__ ckv, const u16* __restrict__ W, char* __restrict__ kvimg, char* lds, int key0, int h0, int h1, int rot,
                 const u16* __restrict__ x, const u16* __restrict__ cvec, const u16* __restrict__ wmod, char* __restrict__ ws) {
    const int tid = threadIdx.x, l = tid & 63, r32 = l & 31, hi = l >> 5;
    const int w = __builtin_amdgcn_readfirstlane(tid >> 6);
    const int wk = w >> 2, wc = w & 3;
    const unsigned lds0 = (unsigned)(uintptr_t)lds;
    const u16* asrc[4]; const u16* wsrc[4];
#pragma unroll
    for (int n = 0; n < 4; ++n) {
        const int P = n * 8 + w, row = 8 * P + (l >> 3), c = (l & 7) ^ ((row >> 1) & 7);
        asrc[n] = ckv + (size_t)(key0 + row) * 512 + c * 8;
        const int half = P >> 4, rw = 4 * (P & 15) + (l >> 4), cw = (l & 15) ^ ((rw & 3) << 2);
        wsrc[n] = W + (size_t)rw * 4096 + (half ? h1 : h0) * 128 + cw * 8;
    }
#define K1_PIECE(i, kt_, bb_) do { if ((i) < 4) glds16(asrc[i] + (kt_) * 64, (bb_) + ((i) * 8 + w) * 1024); \
        else glds16(wsrc[(i) - 4] + (size_t)(kt_) * 64 * 4096, (bb_) + 32768 + (((i) - 4) * 8 + w) * 1024); } while (0)
    {
        const int b = blockIdx.x, row = 8 * b + w, half = tid & 1, rowi = tid >> 1;
        u32x4 xv4, wv0, wv1; unsigned cv0, cv1;
        asm volatile("global_load_dwordx4 %0, %1, off" : "=v"(xv4) : "v"(x + (size_t)row * 512 + 8 * l) : "memory");
        if (b < 64) {
            asm volatile("global_load_dwordx4 %0, %1, off" : "=v"(wv0) : "v"(wmod + (size_t)rowi * 3072 + 16 * b + 8 * half) : "memory");
            asm volatile("global_load_dwordx4 %0, %1, off" : "=v"(wv1) : "v"(wmod + (size_t)(rowi + 256) * 3072 + 16 * b + 8 * half) : "memory");
            asm volatile("global_load_ushort %0, %1, off" : "=v"(cv0) : "v"(cvec + rowi) : "memory");
            asm volatile("global_load_ushort %0, %1, off" : "=v"(cv1) : "v"(cvec + rowi + 256) : "memory");
        }
#pragma unroll
        for (int i = 0; i < 8; ++i) K1_PIECE(i, rot, lds0);
        float* scr = (float*)(lds + 65536);
        asm volatile("s_waitcnt vmcnt(8)" : "+v"(xv4) :: "memory");
        {
            float xv[8], o[8];
#pragma unroll
            for (int j = 0; j < 4; ++j) { xv[2 * j] = bflo(xv4[j]); xv[2 * j + 1] = bfhi(xv4[j]); }
            ln_row(xv, o);
            u32x4 ov = {pk2(o[0], o[1]), pk2(o[2], o[3]), pk2(o[4], o[5]), pk2(o[6], o[7])};
            *(u32x4*)(ws + WS_XH + ((size_t)((row >> 5) * 32 + (l >> 1)) * 64 + (row & 31) + 32 * (l & 1)) * 16) = ov;
        }
        if (b < 64) {
            asm volatile("" : "+v"(wv0), "+v"(wv1), "+v"(cv0), "+v"(cv1));
            float a8[8];
            const float c0 = bf2f((u16)cv0), c1 = bf2f((u16)cv1);
            const float sc0 = bfr(c0 / (1.f + __expf(-c0))), sc1 = bfr(c1 / (1.f + __expf(-c1)));
#pragma unroll
            for (int j = 0; j < 4; ++j) { a8[2 * j] = sc0 * bflo(wv0[j]) + sc1 * bflo(wv1[j]); a8[2 * j + 1] = sc0 * bfhi(wv0[j]) + sc1 * bfhi(wv1[j]); }
#pragma unroll
            for (int m = 2; m < 64; m <<= 1)
#pragma unroll
                for (int j = 0; j < 8; ++j) a8[j] += __shfl_xor(a8[j], m);
            if (l < 2) {
#pragma unroll
                for (int j = 0; j < 8; ++j) scr[(w * 2 + l) * 8 + j] = a8[j];
            }
            BAR_LGKM();
            if (tid < 16) {
                float s = 0.f;
#pragma unroll
                for (int ww = 0; ww < 8; ++ww) s += scr[(ww * 2 + (tid >> 3)) * 8 + (tid & 7)];
                const int col = 16 * b + tid; const float mv = bfr(s);
                if (col < 512) ((float*)(ws + WS_GS))[512 + col] = mv;
                else ((float*)(ws + WS_GS))[col - 512] = bfr(1.f + mv);
            }
        }
    }
    f32x16 acc[4][2];
#pragma unroll
    for (int a = 0; a < 4; ++a) { acc[a][0] = f32x16{}; acc[a][1] = f32x16{}; }
    const int q4 = (l & 15) >> 2, p4 = l & 3, blk = (l >> 4) & 1;
    const int xa = (l >> 1) & 7;
    STAMP_AT(kvimg, 0, 1);
    for (int kt = 0; kt < 8; ++kt) {
        WAIT_ALL_BAR();
        if (kt == 0) STAMP_AT(kvimg, 0, 2);
        const bool pre = kt + 1 < 8;
        const int ktn = (rot + kt + 1) & 7;
        const unsigned bb = lds0 + ((kt + 1) & 1) * 65536;
        lds_cptr ab = (lds_cptr)lds + (kt & 1) * 65536;
        lds_cptr wb = ab + 32768 + (wc >> 1) * 16384;
#pragma unroll
        for (int s = 0; s < 4; ++s) {
            bf16x8 af[4], wf[2];
#pragma unroll
            for (int kb = 0; kb < 4; ++kb) af[kb] = ldsb128(ab + (128 * wk + 32 * kb + r32) * 128 + (((2 * s + hi) ^ xa) << 4));
#pragma unroll
            for (int j = 0; j < 2; ++j) {
                const int db = 2 * (wc & 1) + j;
                lds_cptr p = wb + (16 * s + 8 * hi + q4) * 256 + 64 * (db ^ q4) + 32 * blk + 16 * (p4 >> 1) + 8 * (p4 & 1);
                wf[j] = wfrag(p, p + 1024);
            }
#pragma unroll
            for (int kb = 0; kb < 4; ++kb) {
#pragma unroll
                for (int j = 0; j < 2; ++j) {
                    if (VT) acc[kb][j] = MFMA(af[kb], wf[j], acc[kb][j]);
                    else acc[kb][j] = MFMA(wf[j], af[kb], acc[kb][j]);
                }
                if ((kb & 1) == 0) { if (pre) K1_PIECE(2 * s + (kb >> 1), ktn, bb); }
            }
        }
    }
#undef K1_PIECE
    STAMP_AT(kvimg, 0, 3);
    const int head = (wc >> 1) ? h1 : h0;
#pragma unroll
    for (int kb = 0; kb < 4; ++kb) {
        const int key = key0 + 128 * wk + 32 * kb, tile = key >> 6, kbl = kb & 1;
        char* tb = kvimg + ((size_t)(head * 32 + tile) << 15);
#pragma unroll
        for (int j = 0; j < 2; ++j) {
            const int db = 2 * (wc & 1) + j;
#pragma unroll
            for (int s2 = 0; s2 < 2; ++s2) {
                const int fo = VT ? (16384 + (db * 4 + kbl * 2 + s2) * 1024) : ((kbl * 8 + db * 2 + s2) * 1024);
                *(bf16x8*)(tb + fo + l * 16) = pack8(acc[kb][j], s2);
            }
        }
    }
}

__global__ __launch_bounds__(512, 2) void k1_kvproj(const u16* __restrict__ ckv, const u16* __restrict__ wuk, const u16* __restrict__ wuv,
                                                    const u16* __restrict__ x, const u16* __restrict__ cvec, const u16* __restrict__ wmod,
                                                    char* __restrict__ ws) {
    extern __shared__ __attribute__((aligned(16))) char lds[];
    const int b = blockIdx.x, tid = threadIdx.x, l = tid & 63;
    const int w = __builtin_amdgcn_readfirstlane(tid >> 6);
    const int xcd = b & 7, r = b >> 3, kb8 = r & 7, combo = r >> 3, type = combo & 1, hp = combo >> 1;
    const int h0 = xcd + 16 * hp, h1 = h0 + 8, key0 = kb8 * 256;
    const u16* W = type ? wuv : wuk;
    STAMP_AT(ws, 0, 0);
    const int rot = (kb8 + 2 * combo) & 7;
    if (type) k1_gemm<true>(ckv, W, ws + WS_KV, lds, key0, h0, h1, rot, x, cvec, wmod, ws);
    else k1_gemm<false>(ckv, W, ws + WS_KV, lds, key0, h0, h1, rot, x, cvec, wmod, ws);
    STAMP_AT(ws, 0, 4);
}

constexpr float ATT_THR = 8.f;
#define PIN(x) asm volatile("" : "+v"(x))
#define MX3(a, b, c) __builtin_fmaxf(__builtin_fmaxf((a), (b)), (c))
DI float att_rowmax(const f32x16& p0, const f32x16& p1) {
    float a = MX3(p0[0], p0[1], p1[0]), b = MX3(p0[2], p0[3], p1[1]); a = MX3(a, p1[2], p1[3]);
#pragma unroll
    for (int r = 4; r < 16; r += 4) { a = MX3(a, p0[r], p0[r + 1]); b = MX3(b, p0[r + 2], p0[r + 3]); a = MX3(a, p1[r], p1[r + 1]); b = MX3(b, p1[r + 2], p1[r + 3]); }
    return __builtin_fmaxf(a, b);
}
DI void att_mask(f32x16& s0, f32x16& s1, int dq) {
#pragma unroll
    for (int r = 0; r < 16; ++r) {
        const int c = (r & 3) + 8 * (r >> 2);
        if (c > dq) s0[r] = -INFINITY;
        if (c + 32 > dq) s1[r] = -INFINITY;
    }
}
constexpr int PD = 6;
DI int kfrag_off(int g) { return ((g & 1) * 8 + (g >> 1)) * 1024; }
DI int vfrag_off(int g) { return ((g & 3) * 4 + (g >> 2)) * 1024; }
template <bool MASK>
DI void att_step(f32x16& C0, f32x16& C1, const f32x16& P0, f32x16& P1, f32x16 (&o)[4], f32x16& negm, float& l_run,
                 const bf16x8 (&qf)[8], bf16x8 (&fr)[8], lds_cptr kp, lds_cptr vp, lds_cptr kpn, int dq) {
    unsigned pw[16];
    float sacc = 0.f, sacc2 = 0.f;
    SBAR();
#pragma unroll
    for (int g = 0; g < 16; ++g) {
        if ((g & 1) == 0) C0 = MFMA(fr[g & 7], qf[g >> 1], g < 2 ? negm : C0);
        else C1 = MFMA(fr[g & 7], qf[g >> 1], g < 2 ? negm : C1);
        if (g + PD < 16) fr[(g + PD) & 7] = ldsb128(kp + kfrag_off(g + PD));
        else fr[(g + PD) & 7] = ldsb128(vp + vfrag_off(g + PD - 16));
        P1[g] = __builtin_amdgcn_exp2f(P1[g]); PIN(P1[g]);
        sacc += P0[g]; PIN(sacc);
        if (g & 1) { pw[g >> 1] = pk2(P0[g - 1], P0[g]); PIN(pw[g >> 1]); }
        if (g >= 1) { sacc2 += P1[g - 1]; PIN(sacc2); }
        if (g >= 2 && (g & 1) == 0) { pw[8 + ((g - 2) >> 1)] = pk2(P1[g - 2], P1[g - 1]); PIN(pw[8 + ((g - 2) >> 1)]); }
        SBAR();
    }
    sacc2 += P1[15]; pw[15] = pk2(P1[14], P1[15]);
    l_run += sacc + sacc2;
    bool resc = false; float f = 1.f;
    float ma, mb;
#pragma unroll
    for (int g = 0; g < 16; ++g) {
        { const u32x4 pv = {pw[4 * (g >> 2)], pw[4 * (g >> 2) + 1], pw[4 * (g >> 2) + 2], pw[4 * (g >> 2) + 3]};
          o[g & 3] = MFMA(fr[g & 7], __builtin_bit_cast(bf16x8, pv), o[g & 3]); }
        if (g + PD < 16) fr[(g + PD) & 7] = ldsb128(vp + vfrag_off(g + PD));
        else fr[(g + PD) & 7] = ldsb128(kpn + kfrag_off(g + PD - 16));
        if (g == 1 && MASK) att_mask(C0, C1, dq);
        if (g == 1) { ma = MX3(C0[0], C0[1], C0[2]); mb = MX3(C0[3], C0[4], C0[5]); ma = MX3(ma, C0[6], C0[7]); mb = MX3(mb, C0[8], C0[9]); PIN(ma); PIN(mb); }
        if (g == 2) { ma = MX3(ma, C0[10], C0[11]); mb = MX3(mb, C0[12], C0[13]); ma = MX3(ma, C0[14], C0[15]); mb = MX3(mb, C1[0], C1[1]); PIN(ma); PIN(mb); }
        if (g == 3) { ma = MX3(ma, C1[2], C1[3]); mb = MX3(mb, C1[4], C1[5]); ma = MX3(ma, C1[6], C1[7]); mb = MX3(mb, C1[8], C1[9]); PIN(ma); PIN(mb); }
        if (g == 4) {
            ma = MX3(ma, C1[10], C1[11]); mb = MX3(mb, C1[12], C1[13]); ma = MX3(ma, C1[14], C1[15]);
            const float rm = __builtin_fmaxf(ma, mb);
            if (__builtin_expect(__any(rm > ATT_THR), 0)) {
                const float dl = __builtin_fmaxf(half_swap_max(rm), 0.f);
                f = __builtin_amdgcn_exp2f(-dl); l_run *= f; resc = true;
#pragma unroll
                for (int r = 0; r < 16; ++r) { C0[r] -= dl; C1[r] -= dl; negm[r] -= dl; }
            }
        }
        if (g >= 5) {
            const int e0 = g < 10 ? 2 * (g - 5) : 10 + (g - 10), e1 = g < 10 ? e0 + 2 : e0 + 1;
#pragma unroll
            for (int e = e0; e < e1; ++e) { C0[e] = __builtin_amdgcn_exp2f(C0[e]); PIN(C0[e]); }
        }
        SBAR();
    }
    if (__builtin_expect(resc, 0)) {
#pragma unroll
        for (int d = 0; d < 4; ++d)
#pragma unroll
            for (int r = 0; r < 16; ++r) o[d][r] *= f;
    }
}
constexpr int K2_LDS = 147456 + 4096;
constexpr float QSCALE = 0.08838834764831845f * 1.4426950408889634f;
__global__ __launch_bounds__(512, 2) void k2_attn(const u16* __restrict__ wq, const u16* __restrict__ cvec, const u16* __restrict__ wmod,
                                                  const u16* __restrict__ wo, const u16* __restrict__ w1, const u16* __restrict__ w2, char* __restrict__ ws) {
    extern __shared__ __attribute__((aligned(16))) char lds[];
    const int b = blockIdx.x, tid = threadIdx.x, l = tid & 63, r32 = l & 31, hi = l >> 5;
    const int w = __builtin_amdgcn_readfirstlane(tid >> 6);
    const int xcd = b & 7, kk = b >> 3, head = xcd + 8 * (kk >> 3), pi = kk & 7;
    const int qb = (w < 4) ? pi : 15 - pi, sub = w & 3, rb = qb * 4 + sub;
    const int tl = 2 * qb + (sub >> 1), NT = 32 - 2 * pi;
    const unsigned lds0 = (unsigned)(uintptr_t)lds;
    STAMP_AT(ws, 1, 0);
    const char* kvh = ws + WS_KV + ((size_t)head << 20);
    const char* ksrc = kvh + w * 2048 + l * 16;
    const unsigned kdst = lds0 + w * 2048, vdst = lds0 + 65536 + w * 2048;
#define DMA_K(t_, slot_) do { const char* g_ = ksrc + (size_t)(t_) * 32768; glds16(g_, kdst + (slot_)); glds16(g_ + 1024, kdst + (slot_) + 1024); } while (0)
#define DMA_V(t_, slot_) do { const char* g_ = ksrc + (size_t)(t_) * 32768 + 16384; glds16(g_, vdst + (slot_)); glds16(g_ + 1024, vdst + (slot_) + 1024); } while (0)
    DMA_K(0, 0);
    glds16(ws + WS_GS + (w & 3) * 1024 + l * 16, lds0 + 147456 + (w & 3) * 1024);
    u32x4 xf[16];
    const char* xsrc = ws + WS_XH + ((size_t)(rb * 32) * 64 + l) * 16;
#define XISSUE(c) do { _Pragma("unroll") for (int k8 = 0; k8 < 8; ++k8) asm volatile("global_load_dwordx4 %0, %1, off" : "=v"(xf[8 * ((c) & 1) + k8]) : "v"(xsrc + (8 * (c) + k8) * 1024) : "memory"); } while (0)
#define WISSUE(c) do { _Pragma("unroll") for (int n = 0; n < 4; ++n) { const int P = (4 * (c) + n) * 8 + w, row = 4 * P + (l >> 4), cc = (l & 15) ^ ((row & 3) << 2); \
            glds16(wq + (size_t)row * 4096 + head * 128 + cc * 8, lds0 + 16384 + P * 1024); } } while (0)
    XISSUE(0); WISSUE(0); XISSUE(1); WISSUE(1);
    const int q4 = (l & 15) >> 2, p4 = l & 3, blk = (l >> 4) & 1;
    const lds_cptr gsp = (lds_cptr)lds + 147456 + 32 * hi;
    lds_cptr wp[4];
#pragma unroll
    for (int db = 0; db < 4; ++db) wp[db] = (lds_cptr)lds + 16384 + (8 * hi + q4) * 256 + 64 * (db ^ q4) + 32 * blk + 16 * (p4 >> 1) + 8 * (p4 & 1);
    bf16x8 qf[8];
    {
        f32x16 qa[4];
#pragma unroll
        for (int d = 0; d < 4; ++d) qa[d] = f32x16{};
#define XWAIT(N, c) asm volatile("s_waitcnt vmcnt(" #N ") lgkmcnt(0)\n\ts_barrier" : "+v"(xf[8 * c]), "+v"(xf[8 * c + 1]), "+v"(xf[8 * c + 2]), "+v"(xf[8 * c + 3]), \
                                 "+v"(xf[8 * c + 4]), "+v"(xf[8 * c + 5]), "+v"(xf[8 * c + 6]), "+v"(xf[8 * c + 7]) :: "memory")
#define XONE(c, k8) asm volatile("global_load_dwordx4 %0, %1, off" : "=v"(xf[8 * ((c) & 1) + (k8)]) : "v"(xsrc + (8 * (c) + (k8)) * 1024) : "memory")
#define WONE(c, n) do { const int P = (4 * (c) + (n)) * 8 + w, row = 4 * P + (l >> 4), cc = (l & 15) ^ ((row & 3) << 2); \
            glds16(wq + (size_t)row * 4096 + head * 128 + cc * 8, lds0 + 16384 + P * 1024); } while (0)
#define KVONE(off_g, dst_) glds16(ksrc + (off_g), (dst_))
#define LDF4(p_) (*(const __attribute__((address_space(3))) f32x4*)(p_))
#define HPART(j_, G_, S_, e_) pk2(fmaf(bflo(xw[j_]), G_[e_], S_[e_]), fmaf(bfhi(xw[j_]), G_[e_ + 1], S_[e_ + 1]))
#pragma unroll
        for (int c = 0; c < 4; ++c) {
            STAMPX(ws, 2 * c);
            if (c == 0) { XWAIT(12, 0); STAMP_AT(ws, 1, 1); }
            if (c == 1) { XWAIT(4, 1); }
            if (c == 2) { XWAIT(8, 0); }
            if (c == 3) { XWAIT(2, 1); }
            STAMPX(ws, 2 * c + 1);
            bf16x8 wf[2][4]; f32x4 g0, g1, s0, s1; u32x4 hc;
            {
                const int ks = 8 * c;
                g0 = LDF4(gsp + 64 * ks); g1 = LDF4(gsp + 64 * ks + 16); s0 = LDF4(gsp + 2048 + 64 * ks); s1 = LDF4(gsp + 2048 + 64 * ks + 16);
#pragma unroll
                for (int db = 0; db < 4; ++db) wf[0][db] = wfrag(wp[db] + ks * 4096, wp[db] + ks * 4096 + 1024);
                const u32x4 xw = xf[8 * (c & 1)];
                hc = (u32x4){HPART(0, g0, s0, 0), HPART(1, g0, s0, 2), HPART(2, g1, s1, 0), HPART(3, g1, s1, 2)};
                g0 = LDF4(gsp + 64 * (ks + 1)); g1 = LDF4(gsp + 64 * (ks + 1) + 16); s0 = LDF4(gsp + 2048 + 64 * (ks + 1)); s1 = LDF4(gsp + 2048 + 64 * (ks + 1) + 16);
            }
            SBAR();
#pragma unroll
            for (int k8 = 0; k8 < 8; ++k8) {
                const int ks = 8 * c + k8;
                const bf16x8 hf = __builtin_bit_cast(bf16x8, hc);
                u32x4 hn = hc;
                const u32x4 xw = xf[8 * (c & 1) + (k8 < 7 ? k8 + 1 : k8)];
#pragma unroll
                for (int db = 0; db < 4; ++db) {
                    qa[db] = MFMA(wf[k8 & 1][db], hf, qa[db]);
                    {
                        const int ix = k8 * 4 + db;
                        if (c == 0) { if (ix < 4) WONE(2, ix); }
                        if (c == 1) { if (ix < 8) XONE(2, ix); else if (ix < 12) WONE(3, ix - 8);
                                      else if (ix < 16) KVONE((size_t)(1 + ((ix - 12) >> 1)) * 32768 + ((ix - 12) & 1) * 1024, kdst + 16384 * (1 + ((ix - 12) >> 1)) + ((ix - 12) & 1) * 1024); }
                        if (c == 2) { if (ix < 8) XONE(3, ix); else if (ix < 10) KVONE(16384 + (ix - 8) * 1024, vdst + (ix - 8) * 1024); }
                        if (c == 3) { if (ix < 2) KVONE(32768 + 16384 + ix * 1024, vdst + 16384 + ix * 1024); }
                    }
                    if (k8 < 7) {
                        wf[(k8 + 1) & 1][db] = wfrag(wp[db] + (ks + 1) * 4096, wp[db] + (ks + 1) * 4096 + 1024);
                        if (db == 0) hn[0] = HPART(0, g0, s0, 0);
                        if (db == 1) hn[1] = HPART(1, g0, s0, 2);
                        if (db == 2) hn[2] = HPART(2, g1, s1, 0);
                        if (db == 3) { hn[3] = HPART(3, g1, s1, 2); }
                        PIN(hn);
                        if (db == 1 && k8 < 6) { g0 = LDF4(gsp + 64 * (ks + 2)); s0 = LDF4(gsp + 2048 + 64 * (ks + 2)); }
                        if (db == 3 && k8 < 6) { g1 = LDF4(gsp + 64 * (ks + 2) + 16); s1 = LDF4(gsp + 2048 + 64 * (ks + 2) + 16); }
                    }
                    SBAR();
                }
                hc = hn;
            }
        }
#undef XWAIT
#undef XISSUE
#undef WISSUE
#undef HPART
#undef XONE
#undef WONE
#undef KVONE
#pragma unroll
        for (int db = 0; db < 4; ++db) {
#pragma unroll
            for (int r = 0; r < 16; ++r) qa[db][r] *= QSCALE;
            qf[2 * db] = pack8(qa[db], 0); qf[2 * db + 1] = pack8(qa[db], 1);
        }
    }
    STAMP_AT(ws, 1, 2); STAMPX(ws, 8);
#define WAIT_BAR4() asm volatile("s_waitcnt vmcnt(4)\n\ts_barrier" ::: "memory")
#define WAITB(t_) do { if ((t_) < tsw) WAIT_BAR4(); else asm volatile("s_waitcnt vmcnt(0)\n\ts_barrier" ::: "memory"); } while (0)
    float l_run = 0.f;
    f32x16 o[4], negm;
#pragma unroll
    for (int d = 0; d < 4; ++d) o[d] = f32x16{};
    const int qrow = rb * 32 + r32;
    const lds_cptr kp0 = (lds_cptr)lds + l * 16, vp0 = (lds_cptr)lds + 65536 + l * 16;
    f32x16 A0, A1, B0, B1;
    bf16x8 pf0, pf1, pf2, pf3, fr[8];
    int sl_prev = 49152, sl_cur = 0;
#define ROT() do { sl_prev = sl_cur; sl_cur = (sl_cur + 16384) & 65535; } while (0)
    const int tsw = 2 * pi + 3;
    const char* hsrc = kvh + (w & 3) * 4096 + l * 16;
    const unsigned hk = lds0 + (w & 3) * 4096, hv = lds0 + 65536 + (w & 3) * 4096;
#define STAGE_ISSUE(t_) do { if ((t_) < tsw) { const int tk_ = (t_) + 3 < NT ? (t_) + 3 : NT - 1, tv_ = (t_) + 2 < NT ? (t_) + 2 : NT - 1; DMA_K(tk_, sl_prev); DMA_V(tv_, (sl_cur + 32768) & 65535); } } while (0)
#define DMA_ARGS(t_) (t_) < tsw, ksrc + (size_t)((t_) + 3 < NT ? (t_) + 3 : NT - 1) * 32768, kdst + sl_prev, ksrc + (size_t)((t_) + 2 < NT ? (t_) + 2 : NT - 1) * 32768 + 16384, vdst + ((sl_cur + 32768) & 65535)
#define STAGE_HELP(t_) do { const int tk_ = (t_) + 3 < NT ? (t_) + 3 : NT - 1, tv_ = (t_) + 2 < NT ? (t_) + 2 : NT - 1; \
        const char* gk_ = hsrc + (size_t)tk_ * 32768; const char* gv_ = hsrc + (size_t)tv_ * 32768 + 16384; const unsigned sk_ = hk + sl_prev, sv_ = hv + ((sl_cur + 32768) & 65535); \
        glds16(gk_, sk_); glds16(gk_ + 1024, sk_ + 1024); glds16(gk_ + 2048, sk_ + 2048); glds16(gk_ + 3072, sk_ + 3072); \
        glds16(gv_, sv_); glds16(gv_ + 1024, sv_ + 1024); glds16(gv_ + 2048, sv_ + 2048); glds16(gv_ + 3072, sv_ + 3072); } while (0)
    asm volatile("s_waitcnt vmcnt(2) lgkmcnt(0)\n\ts_barrier" ::: "memory");
    STAGE_ISSUE(0);
    {
        A0 = f32x16{}; A1 = f32x16{};
#pragma unroll
        for (int ds = 0; ds < 8; ++ds) {
            A0 = MFMA(ldsb128(kp0 + ds * 1024), qf[ds], A0);
            A1 = MFMA(ldsb128(kp0 + 8192 + ds * 1024), qf[ds], A1);
        }
        if (tl == 0) att_mask(A0, A1, qrow - 4 * hi);
        const float mx = half_swap_max(att_rowmax(A0, A1));
#pragma unroll
        for (int r = 0; r < 16; ++r) { A0[r] = __builtin_amdgcn_exp2f(A0[r] - mx); A1[r] = A1[r] - mx; negm[r] = -mx; }
#pragma unroll
        for (int g = 0; g < PD; ++g) fr[g] = ldsb128(kp0 + 16384 + kfrag_off(g));
    }
    WAIT_BAR4(); ROT();
    int t = 1;
    for (; t + 1 <= tl - 1; t += 2) {
        STAGE_ISSUE(t); att_step<false>(B0, B1, A0, A1, o, negm, l_run, qf, fr, kp0 + sl_cur, vp0 + sl_prev, kp0 + ((sl_cur + 16384) & 65535), 0); WAITB(t); ROT();
        STAGE_ISSUE(t + 1); att_step<false>(A0, A1, B0, B1, o, negm, l_run, qf, fr, kp0 + sl_cur, vp0 + sl_prev, kp0 + ((sl_cur + 16384) & 65535), 0); WAITB(t + 1); ROT();
    }
    if (t <= tl - 1) {
        STAGE_ISSUE(t); att_step<false>(B0, B1, A0, A1, o, negm, l_run, qf, fr, kp0 + sl_cur, vp0 + sl_prev, kp0 + ((sl_cur + 16384) & 65535), 0); WAITB(t); ROT();
        A0 = B0; A1 = B1; ++t;
    }
    if (tl >= 1) {
        STAGE_ISSUE(t); att_step<true>(B0, B1, A0, A1, o, negm, l_run, qf, fr, kp0 + sl_cur, vp0 + sl_prev, kp0 + ((sl_cur + 16384) & 65535), qrow - t * 64 - 4 * hi); WAITB(t); ROT();
        ++t;
    } else { B0 = A0; B1 = A1; }
    if (t < NT) STAGE_ISSUE(t);
    {
        float sacc = 0.f;
#pragma unroll
        for (int r = 0; r < 16; ++r) { B1[r] = __builtin_amdgcn_exp2f(B1[r]); sacc += B0[r] + B1[r]; }
        l_run += sacc;
        pf0 = pack8(B0, 0); pf1 = pack8(B0, 1); pf2 = pack8(B1, 0); pf3 = pack8(B1, 1);
        const lds_cptr vp = vp0 + sl_prev;
#pragma unroll
        for (int d = 0; d < 4; ++d) {
            o[d] = MFMA(ldsb128(vp + (d * 4 + 0) * 1024), pf0, o[d]);
            o[d] = MFMA(ldsb128(vp + (d * 4 + 1) * 1024), pf1, o[d]);
            o[d] = MFMA(ldsb128(vp + (d * 4 + 2) * 1024), pf2, o[d]);
            o[d] = MFMA(ldsb128(vp + (d * 4 + 3) * 1024), pf3, o[d]);
        }
    }
    STAMP_AT(ws, 1, 3);
    {
        const float inv = 1.f / half_swap_sum(l_run);
        char* cb = ws + WS_CTX + ((size_t)(rb * 256 + head * 8) * 64 + l) * 16;
#pragma unroll
        for (int d = 0; d < 4; ++d) {
#pragma unroll
            for (int r = 0; r < 16; ++r) o[d][r] *= inv;
            *(bf16x8*)(cb + (2 * d) * 1024) = pack8(o[d], 0);
            *(bf16x8*)(cb + (2 * d + 1) * 1024) = pack8(o[d], 1);
        }
    }
    if (t < NT) {
        WAIT_ALL_BAR(); ROT(); ++t;
        for (; t < NT && t < tsw; ++t) { STAGE_ISSUE(t); WAIT_BAR4(); ROT(); }
        if (w < 4) {
            float* scr = (float*)(lds + 147456);
            const int t0 = t;
            for (; t < NT; ++t) {
                STAGE_HELP(t);
                if (pi == 6 && t == t0) {
                    const int c8 = tid & 7, rowi = tid >> 3;
                    float a8[8];
#pragma unroll
                    for (int e = 0; e < 8; ++e) a8[e] = 0.f;
#pragma unroll
                    for (int q = 0; q < 16; ++q) {
                        const u32x4 wv = *(const u32x4*)(wmod + (size_t)(rowi + 32 * q) * 3072 + 1024 + 64 * head + 8 * c8);
                        const float cv = bf2f(cvec[rowi + 32 * q]), sc = bfr(cv / (1.f + __expf(-cv)));
#pragma unroll
                        for (int e = 0; e < 4; ++e) { a8[2 * e] += sc * bflo(wv[e]); a8[2 * e + 1] += sc * bfhi(wv[e]); }
                    }
#pragma unroll
                    for (int m = 8; m < 64; m <<= 1)
#pragma unroll
                        for (int e = 0; e < 8; ++e) a8[e] += __shfl_xor(a8[e], m);
                    if (l < 8) {
#pragma unroll
                        for (int e = 0; e < 8; ++e) scr[(w * 8 + l) * 8 + e] = a8[e];
                    }
                }
                if (pi <= 5 && t == t0 + 1) {
#pragma unroll
                    for (int j = 0; j < 2; ++j) {
                        const int ch = (head * 6 + pi) * 4 + w + 768 * j;
                        if (ch < 960) {
                            const char* src = ch < 512 ? (const char*)wo + (size_t)ch * 8192 : ch < 736 ? (const char*)w1 + (size_t)(ch - 512) * 8192 : (const char*)w2 + (size_t)(ch - 736) * 8192;
                            glds4(src + l * 128, lds0 + 147456 + 2048);
                        }
                    }
                }
                if (pi == 6 && t == t0 + 1 && tid < 64) {
                    const float s = (scr[tid] + scr[64 + tid]) + (scr[128 + tid] + scr[192 + tid]);
                    ((float*)(ws + WS_MOD))[1024 + 64 * head + tid] = bfr(s);
                }
                asm volatile("s_waitcnt vmcnt(8) lgkmcnt(0)\n\ts_barrier" ::: "memory"); ROT();
            }
        }
        else { for (; t < NT; ++t) { WAIT_ALL_BAR(); ROT(); } }
    }
    asm volatile("s_waitcnt vmcnt(0)" ::: "memory");
    STAMP_AT(ws, 1, 4);
#undef DMA_K
#undef DMA_V
#undef ROT
#undef STAGE_ISSUE
#undef STAGE_HELP
#undef DMA_ARGS
#undef WAITB
}

template <int TQ, int TN, int KW, int BKS, bool APERM, bool CT>
struct G {
    static constexpr int NWAVE = TQ * TN * KW, RB = TN * 128, A_BYTES = 2 * TQ * BKS * 1024, W_BYTES = 16 * BKS * RB, STAGE = A_BYTES + W_BYTES;
    static constexpr int CPR = RB / 16;
    static_assert(STAGE == 32768 && (2 * TQ * BKS + W_BYTES / 1024) / NWAVE == 4, "4 glds per wave per stage, 4 x 32 KB ring");
    static DI int swz(int row) { return RB == 256 ? ((row & 3) << 2) : (((row >> 1) & 1) << 2); }
    static DI void stage(const char* AF, size_t a_rb_stride, const u16* Wp, int ldw, int kst, unsigned ldsaddr, int w, int l) {
#pragma unroll
        for (int p0 = 0; p0 < 2 * TQ * BKS; p0 += NWAVE) {
            const int p = p0 + w, rbi = p / BKS, ks = p % BKS;
            glds16(AF + rbi * a_rb_stride + (size_t)(kst * BKS + ks) * 1024 + l * 16, ldsaddr + p * 1024);
        }
#pragma unroll
        for (int p0 = 0; p0 < W_BYTES / 1024; p0 += NWAVE) {
            const int p = p0 + w, row = p * (1024 / RB) + l / CPR, c = (l % CPR) ^ swz(row);
            glds16(Wp + (size_t)(kst * BKS * 16 + row) * ldw + c * 8, ldsaddr + A_BYTES + p * 1024);
        }
    }
    static constexpr int NPA = 2 * TQ * BKS / NWAVE, NPW = W_BYTES / 1024 / NWAVE;
    static DI void stage_piece(int i, const char* AF, size_t a_rb_stride, const u16* Wp, int ldw, int kst, unsigned ldsaddr, int w, int l) {
        if (i < NPA) {
            const int p = i * NWAVE + w, rbi = p / BKS, ks = p % BKS;
            glds16(AF + rbi * a_rb_stride + (size_t)(kst * BKS + ks) * 1024 + l * 16, ldsaddr + p * 1024);
        } else {
            const int p = (i - NPA) * NWAVE + w, row = p * (1024 / RB) + l / CPR, c = (l % CPR) ^ swz(row);
            glds16(Wp + (size_t)(kst * BKS * 16 + row) * ldw + c * 8, ldsaddr + A_BYTES + p * 1024);
        }
    }
    template <bool RM, class EPI>
    static DI void run(EPI epi, const char* AF, size_t a_rb_stride, const u16* Wp, int ldw, int nstage, int rot, char* lds, float (&red)[64 / KW], int& tq, int& tn, int& kw, char* ws, int kid) {
        const int tid = threadIdx.x, l = tid & 63, hi = l >> 5;
        STAMP_AT(ws, kid, 0);
        const int w = __builtin_amdgcn_readfirstlane(tid >> 6);
        kw = w % KW; const int tt = w / KW; tq = tt / TN; tn = tt % TN;
        const unsigned lds0 = (unsigned)(uintptr_t)lds;
        const int q4 = (l & 15) >> 2, p4 = l & 3, blk = (l >> 4) & 1;
        f32x16 acc[2][2];
#pragma unroll
        for (int i = 0; i < 2; ++i) { acc[i][0] = f32x16{}; acc[i][1] = f32x16{}; }
        int kst = rot;
#define NEXTK() do { kst = (kst + 1 == nstage) ? 0 : kst + 1; } while (0)
        stage(AF, a_rb_stride, Wp, ldw, kst, lds0, w, l); NEXTK();
        stage(AF, a_rb_stride, Wp, ldw, kst, lds0 + STAGE, w, l); NEXTK();
        stage(AF, a_rb_stride, Wp, ldw, kst, lds0 + 2 * STAGE, w, l); NEXTK();
        for (int st = 0; st < nstage; ++st) {
            if (st + 2 < nstage) asm volatile("s_waitcnt vmcnt(8) lgkmcnt(0)\n\ts_barrier" ::: "memory");
            else if (st + 1 < nstage) asm volatile("s_waitcnt vmcnt(4) lgkmcnt(0)\n\ts_barrier" ::: "memory");
            else WAIT_ALL_BAR();
            if (st == 0) STAMP_AT(ws, kid, 1);
            const bool pre = st + 3 < nstage; const unsigned pdst = lds0 + ((st + 3) & 3) * STAGE;
            lds_cptr ab = (lds_cptr)lds + (st & 3) * STAGE;
            lds_cptr wb = ab + A_BYTES;
#pragma unroll
            for (int kk = 0; kk < BKS / KW; ++kk) {
                const int ks = kw * (BKS / KW) + kk;
                bf16x8 af[2], wf[2];
#pragma unroll
                for (int i = 0; i < 2; ++i) af[i] = ldsb128(ab + ((2 * tq + i) * BKS + ks) * 1024 + l * 16);
#pragma unroll
                for (int j = 0; j < 2; ++j) {
                    const int r0 = 16 * ks + (APERM ? 4 * hi : 8 * hi) + q4, r1 = r0 + (APERM ? 8 : 4);
                    const int ch = 4 * (2 * tn + j) + 2 * blk + (p4 >> 1);
                    wf[j] = wfrag(wb + r0 * RB + ((ch ^ swz(r0)) << 4) + 8 * (p4 & 1), wb + r1 * RB + ((ch ^ swz(r1)) << 4) + 8 * (p4 & 1));
                }
#pragma unroll
                for (int i = 0; i < 2; ++i)
#pragma unroll
                    for (int j = 0; j < 2; ++j) {
                        if (CT) acc[i][j] = MFMA(wf[j], af[i], acc[i][j]);
                        else acc[i][j] = MFMA(af[i], wf[j], acc[i][j]);
                        constexpr int PER = 4 / (BKS / KW);
                        const int m = i * 2 + j;
                        if (PER == 4) { SBAR(); if (pre) stage_piece(m, AF, a_rb_stride, Wp, ldw, kst, pdst, w, l); SBAR(); }
                        else if (m & 1) { SBAR(); if (pre) stage_piece(kk * 2 + (m >> 1), AF, a_rb_stride, Wp, ldw, kst, pdst, w, l); SBAR(); }
                    }
            }
            if (pre) NEXTK();
        }
        BAR_LGKM();
        STAMP_AT(ws, kid, 2);
        float* rb_ = (float*)lds;
        constexpr int RS = RM ? 68 : 64;
#pragma unroll
        for (int i = 0; i < 2; ++i)
#pragma unroll
            for (int j = 0; j < 2; ++j)
#pragma unroll
                for (int r = 0; r < 16; ++r) rb_[(w * 64 + (i * 2 + j) * 16 + r) * RS + l] = acc[i][j][r];
        BAR_LGKM();
        if constexpr (RM) {
            constexpr int C8 = TN * 8, NITEM = 64 * TQ * C8 / (64 * NWAVE);
#pragma unroll
            for (int it = 0; it < NITEM; ++it) {
                const int id = it * (64 * NWAVE) + tid, row = id / C8, c8 = id % C8;
                const int rr = row & 31, r = (rr & 3) + 4 * (rr >> 3), h = (rr >> 2) & 1;
                const int t2 = (row >> 6) * TN + (c8 >> 3), reg = (((row >> 5) & 1) * 2 + ((c8 >> 2) & 1)) * 16 + r, ln = (c8 & 3) * 8 + 32 * h;
                f32x4 a0 = {0.f, 0.f, 0.f, 0.f}, a1 = a0;
#pragma unroll
                for (int k2 = 0; k2 < KW; ++k2) {
                    const float* q_ = rb_ + ((t2 * KW + k2) * 64 + reg) * RS + ln;
                    a0 += *(const f32x4*)q_; a1 += *(const f32x4*)(q_ + 4);
                }
                epi(row, c8 * 8, a0, a1);
            }
        } else {
#pragma unroll
            for (int e = 0; e < 64 / KW; ++e) {
                float s = 0.f;
#pragma unroll
                for (int k2 = 0; k2 < KW; ++k2) s += rb_[((tt * KW + k2) * 64 + kw * (64 / KW) + e) * RS + l];
                red[e] = s;
            }
        }
        STAMP_AT(ws, kid, 3);
    }
};

typedef G<2, 2, 2, 4, true, false> G3;
constexpr int K3_LDS = 139264;
__global__ __launch_bounds__(512, 2) void k3_oproj(const u16* __restrict__ wo, char* __restrict__ ws) {
    extern __shared__ __attribute__((aligned(16))) char lds[];
    const int b = blockIdx.x, xcd = b & 7, r = b >> 3, ksl = xcd & 3, rp = (xcd >> 2) * 8 + (r >> 2), ct = r & 3;
    float red[32]; int tq, tn, kw;
    u16* part = (u16*)(ws + WS_PART) + (size_t)ksl * 2048 * 512 + (size_t)(rp * 128) * 512 + ct * 128;
    G3::run<true>([&](int row, int col, f32x4 a0, f32x4 a1) {
                      u32x4 v = {pk2(a0[0], a0[1]), pk2(a0[2], a0[3]), pk2(a1[0], a1[1]), pk2(a1[2], a1[3])};
                      *(u32x4*)(part + (size_t)row * 512 + col) = v; },
                  ws + WS_CTX + ((size_t)(rp * 4) * 256 + ksl * 64) * 1024, (size_t)256 * 1024, wo + (size_t)ksl * 1024 * 512 + ct * 128, 512, 16, (4 * ct + 2 * (r >> 2)) & 15, lds, red, tq, tn, kw, ws, 2);
    STAMP_AT(ws, 2, 4);
}

__global__ __launch_bounds__(512) void k3b_ln2(const u16* __restrict__ x, char* __restrict__ ws) {
    const int l = threadIdx.x & 63, row = blockIdx.x * 8 + (threadIdx.x >> 6);
    const u16* part = (const u16*)(ws + WS_PART) + (size_t)row * 512 + 8 * l;
    const float* mod = (const float*)(ws + WS_MOD);
    u32x4 pv[4];
#pragma unroll
    for (int s = 0; s < 4; ++s) pv[s] = *(const u32x4*)(part + (size_t)s * 2048 * 512);
    const u32x4 xv4 = *(const u32x4*)(x + (size_t)row * 512 + 8 * l);
    const f32x4 g0 = *(const f32x4*)(mod + 1024 + 8 * l), g1 = *(const f32x4*)(mod + 1024 + 8 * l + 4);
    const f32x4 c0 = *(const f32x4*)(mod + 2048 + 8 * l), c1 = *(const f32x4*)(mod + 2048 + 8 * l + 4);
    const f32x4 s0 = *(const f32x4*)(mod + 1536 + 8 * l), s1 = *(const f32x4*)(mod + 1536 + 8 * l + 4);
    float x1[8], xh[8];
#pragma unroll
    for (int j = 0; j < 4; ++j) {
        const int w0 = j >> 1, w1 = 2 + (j >> 1);
        const float a0 = (j & 1) ? (bfhi(pv[0][w0]) + bfhi(pv[1][w0])) + (bfhi(pv[2][w0]) + bfhi(pv[3][w0])) : (bflo(pv[0][w0]) + bflo(pv[1][w0])) + (bflo(pv[2][w0]) + bflo(pv[3][w0]));
        const float a1 = (j & 1) ? (bfhi(pv[0][w1]) + bfhi(pv[1][w1])) + (bfhi(pv[2][w1]) + bfhi(pv[3][w1])) : (bflo(pv[0][w1]) + bflo(pv[1][w1])) + (bflo(pv[2][w1]) + bflo(pv[3][w1]));
        const float xa = (j & 1) ? bfhi(xv4[j >> 1]) : bflo(xv4[j >> 1]), xb = (j & 1) ? bfhi(xv4[2 + (j >> 1)]) : bflo(xv4[2 + (j >> 1)]);
        x1[j] = bfr(xa + bfr(g0[j] * bfr(a0)));
        x1[4 + j] = bfr(xb + bfr(g1[j] * bfr(a1)));
    }
    u32x4 o1 = {pk2(x1[0], x1[1]), pk2(x1[2], x1[3]), pk2(x1[4], x1[5]), pk2(x1[6], x1[7])};
    *(u32x4*)(ws + WS_X1 + ((size_t)row * 512 + 8 * l) * 2) = o1;
    ln_row(x1, xh);
    float h[8];
#pragma unroll
    for (int j = 0; j < 4; ++j) { h[j] = bfr(xh[j] * bfr(1.f + c0[j])) + s0[j]; h[4 + j] = bfr(xh[4 + j] * bfr(1.f + c1[j])) + s1[j]; }
    u32x4 oh = {pk2(h[0], h[1]), pk2(h[2], h[3]), pk2(h[4], h[5]), pk2(h[6], h[7])};
    *(u32x4*)(ws + WS_H2 + ((size_t)((row >> 5) * 32 + (l >> 1)) * 64 + (row & 31) + 32 * (l & 1)) * 16) = oh;
}

typedef G<2, 2, 2, 4, false, true> G4;
__global__ __launch_bounds__(512, 2) void k4_ffn1(const u16* __restrict__ w1, char* __restrict__ ws) {
    extern __shared__ __attribute__((aligned(16))) char lds[];
    const int b = blockIdx.x, xcd = b & 7, jj = b >> 3, rp = 4 * (xcd >> 1) + jj / 7, ct = 7 * (xcd & 1) + jj % 7;
    const int l = threadIdx.x & 63;
    float red[32]; int tq, tn, kw;
    G4::run<false>(0, ws + WS_H2 + (size_t)(rp * 4) * 32 * 1024, (size_t)32 * 1024, w1 + ct * 128, DFF, 8, (jj % 7 + 2 * (jj / 7)) & 7, lds, red, tq, tn, kw, ws, 3);
#pragma unroll
    for (int j = 0; j < 2; ++j)
#pragma unroll
        for (int s = 0; s < 2; ++s) {
            const int e = j * 16 + 8 * s;
            u32x4 v = {pk2(fmaxf(red[e], 0.f), fmaxf(red[e + 1], 0.f)), pk2(fmaxf(red[e + 2], 0.f), fmaxf(red[e + 3], 0.f)),
                       pk2(fmaxf(red[e + 4], 0.f), fmaxf(red[e + 5], 0.f)), pk2(fmaxf(red[e + 6], 0.f), fmaxf(red[e + 7], 0.f))};
            const int rbq = rp * 4 + 2 * tq + kw, kstep = (ct * 128 + 64 * tn + 32 * j) / 16 + s;
            *(u32x4*)(ws + WS_H1 + ((size_t)(rbq * 112 + kstep) * 64 + l) * 16) = v;
        }
    STAMP_AT(ws, 3, 4);
}

typedef G<1, 1, 8, 8, true, false> G5;
__global__ __launch_bounds__(512, 2) void k5_ffn2(const u16* __restrict__ w2, char* __restrict__ ws, u16* __restrict__ out) {
    extern __shared__ __attribute__((aligned(16))) char lds[];
    const int b = blockIdx.x, xcd = b & 7, jj = b >> 3, rp = 8 * (xcd >> 1) + (jj >> 2), ct = 4 * (xcd & 1) + (jj & 3);
    float red[8]; int tq, tn, kw;
    const float* gb = (const float*)(ws + WS_MOD) + 2560 + ct * 64;
    const u16* x1 = (const u16*)(ws + WS_X1) + (size_t)(rp * 64) * 512 + ct * 64;
    u16* op = out + (size_t)(rp * 64) * 512 + ct * 64;
    G5::run<true>([&](int row, int col, f32x4 a0, f32x4 a1) {
                      const u32x4 xv = *(const u32x4*)(x1 + (size_t)row * 512 + col);
                      const f32x4 g0 = *(const f32x4*)(gb + col), g1 = *(const f32x4*)(gb + col + 4);
                      u32x4 v = {pk2(bflo(xv[0]) + bfr(g0[0] * bfr(a0[0])), bfhi(xv[0]) + bfr(g0[1] * bfr(a0[1]))),
                                 pk2(bflo(xv[1]) + bfr(g0[2] * bfr(a0[2])), bfhi(xv[1]) + bfr(g0[3] * bfr(a0[3]))),
                                 pk2(bflo(xv[2]) + bfr(g1[0] * bfr(a1[0])), bfhi(xv[2]) + bfr(g1[1] * bfr(a1[1]))),
                                 pk2(bflo(xv[3]) + bfr(g1[2] * bfr(a1[2])), bfhi(xv[3]) + bfr(g1[3] * bfr(a1[3])))};
                      *(u32x4*)(op + (size_t)row * 512 + col) = v; },
                  ws + WS_H1 + (size_t)(rp * 2) * 112 * 1024, (size_t)112 * 1024, w2 + ct * 64, 512, 14, (3 * (jj & 3) + 2 * (jj >> 2)) % 14, lds, red, tq, tn, kw, ws, 4);
    STAMP_AT(ws, 4, 4);
}

extern "C" void kernel_launch(void* const* d_in, const int* in_sizes, int n_in, void* d_out, int out_size, void* d_ws, size_t ws_size,
                              hipStream_t stream) {
    (void)in_sizes; (void)n_in; (void)out_size; (void)ws_size;
    const u16* x = (const u16*)d_in[0];
    const u16* ckv = (const u16*)d_in[1];
    const u16* wq = (const u16*)d_in[2];
    const u16* wuk = (const u16*)d_in[3];
    const u16* wuv = (const u16*)d_in[4];
    const u16* wo = (const u16*)d_in[5];
    const u16* w1 = (const u16*)d_in[7];
    const u16* w2 = (const u16*)d_in[8];
    const u16* cvec = (const u16*)d_in[9];
    const u16* wmod = (const u16*)d_in[10];
    char* ws = (char*)d_ws;
    hipFuncSetAttribute((const void*)k1_kvproj, hipFuncAttributeMaxDynamicSharedMemorySize, K1_LDS);
    hipFuncSetAttribute((const void*)k2_attn, hipFuncAttributeMaxDynamicSharedMemorySize, K2_LDS);
    hipFuncSetAttribute((const void*)k3_oproj, hipFuncAttributeMaxDynamicSharedMemorySize, K3_LDS);
    hipFuncSetAttribute((const void*)k4_ffn1, hipFuncAttributeMaxDynamicSharedMemorySize, K3_LDS);
    hipFuncSetAttribute((const void*)k5_ffn2, hipFuncAttributeMaxDynamicSharedMemorySize, K3_LDS);
    k1_kvproj<<<256, 512, K1_LDS, stream>>>(ckv, wuk, wuv, x, cvec, wmod, ws);
    k2_attn<<<256, 512, K2_LDS, stream>>>(wq, cvec, wmod, wo, w1, w2, ws);
    k3_oproj<<<256, 512, K3_LDS, stream>>>(wo, ws);
    k3b_ln2<<<256, 512, 0, stream>>>(x, ws);
    k4_ffn1<<<224, 512, K3_LDS, stream>>>(w1, ws);
    k5_ffn2<<<256, 512, K3_LDS, stream>>>(w2, ws, (u16*)d_out);
}
```

```cpp
#include <hip/hip_runtime.h>
#include <hip/hip_bf16.h>
#include <stdint.h>

typedef unsigned short u16;
typedef short bf16x8 __attribute__((ext_vector_type(8)));
typedef short s16x4 __attribute__((ext_vector_type(4)));
typedef short v4i16_t __attribute__((ext_vector_type(4)));
typedef float f32x16 __attribute__((ext_vector_type(16)));
typedef float f32x4 __attribute__((ext_vector_type(4)));
typedef float f32x2 __attribute__((ext_vector_type(2)));
typedef unsigned u32x4 __attribute__((ext_vector_type(4)));
typedef __bf16 bf16x2_t __attribute__((ext_vector_type(2)));
typedef __attribute__((address_space(3))) const char* lds_cptr;
#define DI __device__ __forceinline__
#define MFMA(a, b, c) __builtin_amdgcn_mfma_f32_32x32x16_bf16((a), (b), (c), 0, 0, 0)
#define SBAR() __builtin_amdgcn_sched_barrier(0)

constexpr int SEQ = 2048, DM = 512, NH = 32, HD = 128, DFF = 1792;

constexpr size_t WS_KV = 0;
constexpr size_t WS_XH = WS_KV + (size_t)32 * 32 * 32768;
constexpr size_t WS_MOD = WS_XH + (size_t)64 * 32 * 1024;
constexpr size_t WS_GS = WS_MOD + 12288;
constexpr size_t WS_CTX = WS_MOD + 16384;
constexpr size_t WS_PART = WS_CTX + (size_t)64 * 256 * 1024;
constexpr size_t WS_X1 = WS_PART + (size_t)4 * 2048 * 512 * 4;
constexpr size_t WS_H2 = WS_X1 + (size_t)2048 * 512 * 2;
constexpr size_t WS_H1 = WS_H2 + (size_t)64 * 32 * 1024;

DI unsigned pk2(float a, float b) { f32x2 v = {a, b}; bf16x2_t r = __builtin_convertvector(v, bf16x2_t); return __builtin_bit_cast(unsigned, r); }
DI float bf2f(u16 v) { return __uint_as_float(((unsigned)v) << 16); }
DI float bfr(float x) { return __uint_as_float(pk2(x, 0.f) << 16); }
DI float bflo(unsigned w) { return __uint_as_float(w << 16); }
DI float bfhi(unsigned w) { return __uint_as_float(w & 0xffff0000u); }
DI bf16x8 pack8(const f32x16& a, int s) {
    u32x4 w = {pk2(a[8 * s], a[8 * s + 1]), pk2(a[8 * s + 2], a[8 * s + 3]), pk2(a[8 * s + 4], a[8 * s + 5]), pk2(a[8 * s + 6], a[8 * s + 7])};
    return __builtin_bit_cast(bf16x8, w);
}
DI int crow(int r, int hi) { return (r & 3) + 8 * (r >> 2) + 4 * hi; }
DI void glds16(const void* g, unsigned lds_base) {
    unsigned sv;
    asm volatile("s_mov_b32 %0, m0\n\ts_mov_b32 m0, %2\n\ts_nop 0\n\tglobal_load_lds_dwordx4 %1, off\n\ts_mov_b32 m0, %0"
                 : "=&s"(sv) : "v"(g), "s"(lds_base) : "memory");
}
DI void glds4(const void* g, unsigned lds_base) {
    unsigned sv;
    asm volatile("s_mov_b32 %0, m0\n\ts_mov_b32 m0, %2\n\ts_nop 0\n\tglobal_load_lds_dword %1, off\n\ts_mov_b32 m0, %0"
                 : "=&s"(sv) : "v"(g), "s"(lds_base) : "memory");
}
DI bf16x8 ldsb128(lds_cptr p) { return *(const __attribute__((address_space(3))) bf16x8*)p; }
DI s16x4 ldstr(lds_cptr p) { return __builtin_bit_cast(s16x4, __builtin_amdgcn_ds_read_tr16_b64_v4i16((__attribute__((address_space(3))) v4i16_t*)p)); }
DI bf16x8 wfrag(lds_cptr p0, lds_cptr p1) { s16x4 lo = ldstr(p0), hi = ldstr(p1); return __builtin_shufflevector(lo, hi, 0, 1, 2, 3, 4, 5, 6, 7); }
#define WAIT_ALL_BAR() asm volatile("s_waitcnt vmcnt(0) lgkmcnt(0)\n\ts_barrier" ::: "memory")
#define BAR_LGKM() asm volatile("s_waitcnt lgkmcnt(0)\n\ts_barrier" ::: "memory")
DI float wave_sum(float v) {
#pragma unroll
    for (int m = 1; m < 64; m <<= 1) v += __shfl_xor(v, m);
    return v;
}
DI float half_swap_max(float m) { auto rr = __builtin_amdgcn_permlane32_swap(__float_as_uint(m), __float_as_uint(m), false, false); return fmaxf(__uint_as_float(rr[0]), __uint_as_float(rr[1])); }
DI float half_swap_sum(float m) { auto rr = __builtin_amdgcn_permlane32_swap(__float_as_uint(m), __float_as_uint(m), false, false); return __uint_as_float(rr[0]) + __uint_as_float(rr[1]); }

DI void ln_row(const float (&xv)[8], float (&o)[8]) {
    float s = 0.f;
#pragma unroll
    for (int j = 0; j < 8; ++j) s += xv[j];
    const float mean = wave_sum(s) * (1.f / 512.f);
    float q = 0.f;
#pragma unroll
    for (int j = 0; j < 8; ++j) { const float d = xv[j] - mean; q += d * d; }
    const float var = wave_sum(q) * (1.f / 512.f);
    const float mb = bfr(mean), vb = bfr(var);
    const float den = bfr(sqrtf(bfr(vb + 1e-5f)));
#pragma unroll
    for (int j = 0; j < 8; ++j) o[j] = bfr(bfr(xv[j] - mb) / den);
}

constexpr int K1_LDS = 131072;
template <bool VT>
DI void k1_gemm(const u16* __restrict__ ckv, const u16* __restrict__ W, char* __restrict__ kvimg, char* lds, int key0, int h0, int h1, int rot,
                 const u16* __restrict__ x, const u16* __restrict__ cvec, const u16* __restrict__ wmod, char* __restrict__ ws) {
    const int tid = threadIdx.x, l = tid & 63, r32 = l & 31, hi = l >> 5;
    const int w = __builtin_amdgcn_readfirstlane(tid >> 6);
    const int wk = w >> 2, wc = w & 3;
    const unsigned lds0 = (unsigned)(uintptr_t)lds;
    const u16* asrc[4]; const u16* wsrc[4];
#pragma unroll
    for (int n = 0; n < 4; ++n) {
        const int P = n * 8 + w, row = 8 * P + (l >> 3), c = (l & 7) ^ ((row >> 1) & 7);
        asrc[n] = ckv + (size_t)(key0 + row) * 512 + c * 8;
        const int half = P >> 4, rw = 4 * (P & 15) + (l >> 4), cw = (l & 15) ^ ((rw & 3) << 2);
        wsrc[n] = W + (size_t)rw * 4096 + (half ? h1 : h0) * 128 + cw * 8;
    }
#define K1_PIECE(i, kt_, bb_) do { if ((i) < 4) glds16(asrc[i] + (kt_) * 64, (bb_) + ((i) * 8 + w) * 1024); \
        else glds16(wsrc[(i) - 4] + (size_t)(kt_) * 64 * 4096, (bb_) + 32768 + (((i) - 4) * 8 + w) * 1024); } while (0)
    {
        const int b = blockIdx.x, row = 8 * b + w, half = tid & 1, rowi = tid >> 1;
        u32x4 xv4, wv0, wv1; unsigned cv0, cv1;
        asm volatile("global_load_dwordx4 %0, %1, off" : "=v"(xv4) : "v"(x + (size_t)row * 512 + 8 * l) : "memory");
        if (b < 64) {
            asm volatile("global_load_dwordx4 %0, %1, off" : "=v"(wv0) : "v"(wmod + (size_t)rowi * 3072 + 16 * b + 8 * half) : "memory");
            asm volatile("global_load_dwordx4 %0, %1, off" : "=v"(wv1) : "v"(wmod + (size_t)(rowi + 256) * 3072 + 16 * b + 8 * half) : "memory");
            asm volatile("global_load_ushort %0, %1, off" : "=v"(cv0) : "v"(cvec + rowi) : "memory");
            asm volatile("global_load_ushort %0, %1, off" : "=v"(cv1) : "v"(cvec + rowi + 256) : "memory");
        }
#pragma unroll
        for (int i = 0; i < 8; ++i) K1_PIECE(i, rot, lds0);
        float* scr = (float*)(lds + 65536);
        asm volatile("s_waitcnt vmcnt(8)" : "+v"(xv4) :: "memory");
        {
            float xv[8], o[8];
#pragma unroll
            for (int j = 0; j < 4; ++j) { xv[2 * j] = bflo(xv4[j]); xv[2 * j + 1] = bfhi(xv4[j]); }
            ln_row(xv, o);
            u32x4 ov = {pk2(o[0], o[1]), pk2(o[2], o[3]), pk2(o[4], o[5]), pk2(o[6], o[7])};
            *(u32x4*)(ws + WS_XH + ((size_t)((row >> 5) * 32 + (l >> 1)) * 64 + (row & 31) + 32 * (l & 1)) * 16) = ov;
        }
        if (b < 64) {
            asm volatile("" : "+v"(wv0), "+v"(wv1), "+v"(cv0), "+v"(cv1));
            float a8[8];
            const float c0 = bf2f((u16)cv0), c1 = bf2f((u16)cv1);
            const float sc0 = bfr(c0 / (1.f + __expf(-c0))), sc1 = bfr(c1 / (1.f + __expf(-c1)));
#pragma unroll
            for (int j = 0; j < 4; ++j) { a8[2 * j] = sc0 * bflo(wv0[j]) + sc1 * bflo(wv1[j]); a8[2 * j + 1] = sc0 * bfhi(wv0[j]) + sc1 * bfhi(wv1[j]); }
#pragma unroll
            for (int m = 2; m < 64; m <<= 1)
#pragma unroll
                for (int j = 0; j < 8; ++j) a8[j] += __shfl_xor(a8[j], m);
            if (l < 2) {
#pragma unroll
                for (int j = 0; j < 8; ++j) scr[(w * 2 + l) * 8 + j] = a8[j];
            }
            BAR_LGKM();
            if (tid < 16) {
                float s = 0.f;
#pragma unroll
                for (int ww = 0; ww < 8; ++ww) s += scr[(ww * 2 + (tid >> 3)) * 8 + (tid & 7)];
                const int col = 16 * b + tid; const float mv = bfr(s);
                if (col < 512) ((float*)(ws + WS_GS))[512 + col] = mv;
                else ((float*)(ws + WS_GS))[col - 512] = bfr(1.f + mv);
            }
        }
    }
    f32x16 acc[4][2];
#pragma unroll
    for (int a = 0; a < 4; ++a) { acc[a][0] = f32x16{}; acc[a][1] = f32x16{}; }
    const int q4 = (l & 15) >> 2, p4 = l & 3, blk = (l >> 4) & 1;
    const int xa = (l >> 1) & 7;

    for (int kt = 0; kt < 8; ++kt) {
        WAIT_ALL_BAR();
        const bool pre = kt + 1 < 8;
        const int ktn = (rot + kt + 1) & 7;
        const unsigned bb = lds0 + ((kt + 1) & 1) * 65536;
        lds_cptr ab = (lds_cptr)lds + (kt & 1) * 65536;
        lds_cptr wb = ab + 32768 + (wc >> 1) * 16384;
#pragma unroll
        for (int s = 0; s < 4; ++s) {
            bf16x8 af[4], wf[2];
#pragma unroll
            for (int kb = 0; kb < 4; ++kb) af[kb] = ldsb128(ab + (128 * wk + 32 * kb + r32) * 128 + (((2 * s + hi) ^ xa) << 4));
#pragma unroll
            for (int j = 0; j < 2; ++j) {
                const int db = 2 * (wc & 1) + j;
                lds_cptr p = wb + (16 * s + 8 * hi + q4) * 256 + 64 * (db ^ q4) + 32 * blk + 16 * (p4 >> 1) + 8 * (p4 & 1);
                wf[j] = wfrag(p, p + 1024);
            }
#pragma unroll
            for (int kb = 0; kb < 4; ++kb) {
#pragma unroll
                for (int j = 0; j < 2; ++j) {
                    if (VT) acc[kb][j] = MFMA(af[kb], wf[j], acc[kb][j]);
                    else acc[kb][j] = MFMA(wf[j], af[kb], acc[kb][j]);
                }
                if ((kb & 1) == 0) { if (pre) K1_PIECE(2 * s + (kb >> 1), ktn, bb); }
            }
        }
    }
#undef K1_PIECE

    const int head = (wc >> 1) ? h1 : h0;
#pragma unroll
    for (int kb = 0; kb < 4; ++kb) {
        const int key = key0 + 128 * wk + 32 * kb, tile = key >> 6, kbl = kb & 1;
        char* tb = kvimg + ((size_t)(head * 32 + tile) << 15);
#pragma unroll
        for (int j = 0; j < 2; ++j) {
            const int db = 2 * (wc & 1) + j;
#pragma unroll
            for (int s2 = 0; s2 < 2; ++s2) {
                const int fo = VT ? (16384 + (db * 4 + kbl * 2 + s2) * 1024) : ((kbl * 8 + db * 2 + s2) * 1024);
                *(bf16x8*)(tb + fo + l * 16) = pack8(acc[kb][j], s2);
            }
        }
    }
}

__global__ __launch_bounds__(512, 2) void k1_kvproj(const u16* __restrict__ ckv, const u16* __restrict__ wuk, const u16* __restrict__ wuv,
                                                    const u16* __restrict__ x, const u16* __restrict__ cvec, const u16* __restrict__ wmod,
                                                    char* __restrict__ ws) {
    extern __shared__ __attribute__((aligned(16))) char lds[];
    const int b = blockIdx.x, tid = threadIdx.x, l = tid & 63;
    const int w = __builtin_amdgcn_readfirstlane(tid >> 6);
    const int xcd = b & 7, r = b >> 3, kb8 = r & 7, combo = r >> 3, type = combo & 1, hp = combo >> 1;
    const int h0 = xcd + 16 * hp, h1 = h0 + 8, key0 = kb8 * 256;
    const u16* W = type ? wuv : wuk;

    const int rot = (kb8 + 2 * combo) & 7;
    if (type) k1_gemm<true>(ckv, W, ws + WS_KV, lds, key0, h0, h1, rot, x, cvec, wmod, ws);
    else k1_gemm<false>(ckv, W, ws + WS_KV, lds, key0, h0, h1, rot, x, cvec, wmod, ws);

}

constexpr float ATT_THR = 8.f;
#define PIN(x) asm volatile("" : "+v"(x))
#define MX3(a, b, c) __builtin_fmaxf(__builtin_fmaxf((a), (b)), (c))
DI float att_rowmax(const f32x16& p0, const f32x16& p1) {
    float a = MX3(p0[0], p0[1], p1[0]), b = MX3(p0[2], p0[3], p1[1]); a = MX3(a, p1[2], p1[3]);
#pragma unroll
    for (int r = 4; r < 16; r += 4) { a = MX3(a, p0[r], p0[r + 1]); b = MX3(b, p0[r + 2], p0[r + 3]); a = MX3(a, p1[r], p1[r + 1]); b = MX3(b, p1[r + 2], p1[r + 3]); }
    return __builtin_fmaxf(a, b);
}
DI void att_mask(f32x16& s0, f32x16& s1, int dq) {
#pragma unroll
    for (int r = 0; r < 16; ++r) {
        const int c = (r & 3) + 8 * (r >> 2);
        if (c > dq) s0[r] = -INFINITY;
        if (c + 32 > dq) s1[r] = -INFINITY;
    }
}
constexpr int PD = 6;
DI int kfrag_off(int g) { return ((g & 1) * 8 + (g >> 1)) * 1024; }
DI int vfrag_off(int g) { return ((g & 3) * 4 + (g >> 2)) * 1024; }
template <bool MASK>
DI void att_step(f32x16& C0, f32x16& C1, const f32x16& P0, f32x16& P1, f32x16 (&o)[4], f32x16& negm, float& l_run,
                 const bf16x8 (&qf)[8], bf16x8 (&fr)[8], lds_cptr kp, lds_cptr vp, lds_cptr kpn, int dq) {
    unsigned pw[16];
    float sacc = 0.f, sacc2 = 0.f;
    SBAR();
#pragma unroll
    for (int g = 0; g < 16; ++g) {
        if ((g & 1) == 0) C0 = MFMA(fr[g & 7], qf[g >> 1], g < 2 ? negm : C0);
        else C1 = MFMA(fr[g & 7], qf[g >> 1], g < 2 ? negm : C1);
        if (g + PD < 16) fr[(g + PD) & 7] = ldsb128(kp + kfrag_off(g + PD));
        else fr[(g + PD) & 7] = ldsb128(vp + vfrag_off(g + PD - 16));
        P1[g] = __builtin_amdgcn_exp2f(P1[g]); PIN(P1[g]);
        sacc += P0[g]; PIN(sacc);
        if (g & 1) { pw[g >> 1] = pk2(P0[g - 1], P0[g]); PIN(pw[g >> 1]); }
        if (g >= 1) { sacc2 += P1[g - 1]; PIN(sacc2); }
        if (g >= 2 && (g & 1) == 0) { pw[8 + ((g - 2) >> 1)] = pk2(P1[g - 2], P1[g - 1]); PIN(pw[8 + ((g - 2) >> 1)]); }
        SBAR();
    }
    sacc2 += P1[15]; pw[15] = pk2(P1[14], P1[15]);
    l_run += sacc + sacc2;
    bool resc = false; float f = 1.f;
    float ma, mb;
#pragma unroll
    for (int g = 0; g < 16; ++g) {
        { const u32x4 pv = {pw[4 * (g >> 2)], pw[4 * (g >> 2) + 1], pw[4 * (g >> 2) + 2], pw[4 * (g >> 2) + 3]};
          o[g & 3] = MFMA(fr[g & 7], __builtin_bit_cast(bf16x8, pv), o[g & 3]); }
        if (g + PD < 16) fr[(g + PD) & 7] = ldsb128(vp + vfrag_off(g + PD));
        else fr[(g + PD) & 7] = ldsb128(kpn + kfrag_off(g + PD - 16));
        if (g == 1 && MASK) att_mask(C0, C1, dq);
        if (g == 1) { ma = MX3(C0[0], C0[1], C0[2]); mb = MX3(C0[3], C0[4], C0[5]); ma = MX3(ma, C0[6], C0[7]); mb = MX3(mb, C0[8], C0[9]); PIN(ma); PIN(mb); }
        if (g == 2) { ma = MX3(ma, C0[10], C0[11]); mb = MX3(mb, C0[12], C0[13]); ma = MX3(ma, C0[14], C0[15]); mb = MX3(mb, C1[0], C1[1]); PIN(ma); PIN(mb); }
        if (g == 3) { ma = MX3(ma, C1[2], C1[3]); mb = MX3(mb, C1[4], C1[5]); ma = MX3(ma, C1[6], C1[7]); mb = MX3(mb, C1[8], C1[9]); PIN(ma); PIN(mb); }
        if (g == 4) {
            ma = MX3(ma, C1[10], C1[11]); mb = MX3(mb, C1[12], C1[13]); ma = MX3(ma, C1[14], C1[15]);
            const float rm = __builtin_fmaxf(ma, mb);
            if (__builtin_expect(__any(rm > ATT_THR), 0)) {
                const float dl = __builtin_fmaxf(half_swap_max(rm), 0.f);
                f = __builtin_amdgcn_exp2f(-dl); l_run *= f; resc = true;
#pragma unroll
                for (int r = 0; r < 16; ++r) { C0[r] -= dl; C1[r] -= dl; negm[r] -= dl; }
            }
        }
        if (g >= 5) {
            const int e0 = g < 10 ? 2 * (g - 5) : 10 + (g - 10), e1 = g < 10 ? e0 + 2 : e0 + 1;
#pragma unroll
            for (int e = e0; e < e1; ++e) { C0[e] = __builtin_amdgcn_exp2f(C0[e]); PIN(C0[e]); }
        }
        SBAR();
    }
    if (__builtin_expect(resc, 0)) {
#pragma unroll
        for (int d = 0; d < 4; ++d)
#pragma unroll
            for (int r = 0; r < 16; ++r) o[d][r] *= f;
    }
}
constexpr int K2_LDS = 147456 + 4096;
constexpr float QSCALE = 0.08838834764831845f * 1.4426950408889634f;
__global__ __launch_bounds__(512, 2) void k2_attn(const u16* __restrict__ wq, const u16* __restrict__ cvec, const u16* __restrict__ wmod,
                                                  const u16* __restrict__ wo, const u16* __restrict__ w1, const u16* __restrict__ w2, char* __restrict__ ws) {
    extern __shared__ __attribute__((aligned(16))) char lds[];
    const int b = blockIdx.x, tid = threadIdx.x, l = tid & 63, r32 = l & 31, hi = l >> 5;
    const int w = __builtin_amdgcn_readfirstlane(tid >> 6);
    const int xcd = b & 7, kk = b >> 3, head = xcd + 8 * (kk >> 3), pi = kk & 7;
    const int qb = (w < 4) ? pi : 15 - pi, sub = w & 3, rb = qb * 4 + sub;
    const int tl = 2 * qb + (sub >> 1), NT = 32 - 2 * pi;
    const unsigned lds0 = (unsigned)(uintptr_t)lds;

    const char* kvh = ws + WS_KV + ((size_t)head << 20);
    const char* ksrc = kvh + w * 2048 + l * 16;
    const unsigned kdst = lds0 + w * 2048, vdst = lds0 + 65536 + w * 2048;
#define DMA_K(t_, slot_) do { const char* g_ = ksrc + (size_t)(t_) * 32768; glds16(g_, kdst + (slot_)); glds16(g_ + 1024, kdst + (slot_) + 1024); } while (0)
#define DMA_V(t_, slot_) do { const char* g_ = ksrc + (size_t)(t_) * 32768 + 16384; glds16(g_, vdst + (slot_)); glds16(g_ + 1024, vdst + (slot_) + 1024); } while (0)
    DMA_K(0, 0);
    glds16(ws + WS_GS + (w & 3) * 1024 + l * 16, lds0 + 147456 + (w & 3) * 1024);
    u32x4 xf[16];
    const char* xsrc = ws + WS_XH + ((size_t)(rb * 32) * 64 + l) * 16;
#define XISSUE(c) do { _Pragma("unroll") for (int k8 = 0; k8 < 8; ++k8) asm volatile("global_load_dwordx4 %0, %1, off" : "=v"(xf[8 * ((c) & 1) + k8]) : "v"(xsrc + (8 * (c) + k8) * 1024) : "memory"); } while (0)
#define WISSUE(c) do { _Pragma("unroll") for (int n = 0; n < 4; ++n) { const int P = (4 * (c) + n) * 8 + w, row = 4 * P + (l >> 4), cc = (l & 15) ^ ((row & 3) << 2); \
            glds16(wq + (size_t)row * 4096 + head * 128 + cc * 8, lds0 + 16384 + P * 1024); } } while (0)
    XISSUE(0); WISSUE(0); XISSUE(1); WISSUE(1);
    const int q4 = (l & 15) >> 2, p4 = l & 3, blk = (l >> 4) & 1;
    const lds_cptr gsp = (lds_cptr)lds + 147456 + 32 * hi;
    lds_cptr wp[4];
#pragma unroll
    for (int db = 0; db < 4; ++db) wp[db] = (lds_cptr)lds + 16384 + (8 * hi + q4) * 256 + 64 * (db ^ q4) + 32 * blk + 16 * (p4 >> 1) + 8 * (p4 & 1);
    bf16x8 qf[8];
    {
        f32x16 qa[4];
#pragma unroll
        for (int d = 0; d < 4; ++d) qa[d] = f32x16{};
#define XWAIT(N, c) asm volatile("s_waitcnt vmcnt(" #N ") lgkmcnt(0)\n\ts_barrier" : "+v"(xf[8 * c]), "+v"(xf[8 * c + 1]), "+v"(xf[8 * c + 2]), "+v"(xf[8 * c + 3]), \
                                 "+v"(xf[8 * c + 4]), "+v"(xf[8 * c + 5]), "+v"(xf[8 * c + 6]), "+v"(xf[8 * c + 7]) :: "memory")
#define XONE(c, k8) asm volatile("global_load_dwordx4 %0, %1, off" : "=v"(xf[8 * ((c) & 1) + (k8)]) : "v"(xsrc + (8 * (c) + (k8)) * 1024) : "memory")
#define WONE(c, n) do { const int P = (4 * (c) + (n)) * 8 + w, row = 4 * P + (l >> 4), cc = (l & 15) ^ ((row & 3) << 2); \
            glds16(wq + (size_t)row * 4096 + head * 128 + cc * 8, lds0 + 16384 + P * 1024); } while (0)
#define KVONE(off_g, dst_) glds16(ksrc + (off_g), (dst_))
#define LDF4(p_) (*(const __attribute__((address_space(3))) f32x4*)(p_))
#define HPART(j_, G_, S_, e_) pk2(fmaf(bflo(xw[j_]), G_[e_], S_[e_]), fmaf(bfhi(xw[j_]), G_[e_ + 1], S_[e_ + 1]))
#pragma unroll
        for (int c = 0; c < 4; ++c) {

            if (c == 0) { XWAIT(12, 0); }
            if (c == 1) { XWAIT(4, 1); }
            if (c == 2) { XWAIT(8, 0); }
            if (c == 3) { XWAIT(2, 1); }

            bf16x8 wf[2][4]; f32x4 g0, g1, s0, s1; u32x4 hc;
            {
                const int ks = 8 * c;
                g0 = LDF4(gsp + 64 * ks); g1 = LDF4(gsp + 64 * ks + 16); s0 = LDF4(gsp + 2048 + 64 * ks); s1 = LDF4(gsp + 2048 + 64 * ks + 16);
#pragma unroll
                for (int db = 0; db < 4; ++db) wf[0][db] = wfrag(wp[db] + ks * 4096, wp[db] + ks * 4096 + 1024);
                const u32x4 xw = xf[8 * (c & 1)];
                hc = (u32x4){HPART(0, g0, s0, 0), HPART(1, g0, s0, 2), HPART(2, g1, s1, 0), HPART(3, g1, s1, 2)};
                g0 = LDF4(gsp + 64 * (ks + 1)); g1 = LDF4(gsp + 64 * (ks + 1) + 16); s0 = LDF4(gsp + 2048 + 64 * (ks + 1)); s1 = LDF4(gsp + 2048 + 64 * (ks + 1) + 16);
            }
            SBAR();
#pragma unroll
            for (int k8 = 0; k8 < 8; ++k8) {
                const int ks = 8 * c + k8;
                const bf16x8 hf = __builtin_bit_cast(bf16x8, hc);
                u32x4 hn = hc;
                const u32x4 xw = xf[8 * (c & 1) + (k8 < 7 ? k8 + 1 : k8)];
#pragma unroll
                for (int db = 0; db < 4; ++db) {
                    qa[db] = MFMA(wf[k8 & 1][db], hf, qa[db]);
                    {
                        const int ix = k8 * 4 + db;
                        if (c == 0) { if (ix < 4) WONE(2, ix); }
                        if (c == 1) { if (ix < 8) XONE(2, ix); else if (ix < 12) WONE(3, ix - 8);
                                      else if (ix < 16) KVONE((size_t)(1 + ((ix - 12) >> 1)) * 32768 + ((ix - 12) & 1) * 1024, kdst + 16384 * (1 + ((ix - 12) >> 1)) + ((ix - 12) & 1) * 1024); }
                        if (c == 2) { if (ix < 8) XONE(3, ix); else if (ix < 10) KVONE(16384 + (ix - 8) * 1024, vdst + (ix - 8) * 1024); }
                        if (c == 3) { if (ix < 2) KVONE(32768 + 16384 + ix * 1024, vdst + 16384 + ix * 1024); }
                    }
                    if (k8 < 7) {
                        wf[(k8 + 1) & 1][db] = wfrag(wp[db] + (ks + 1) * 4096, wp[db] + (ks + 1) * 4096 + 1024);
                        if (db == 0) hn[0] = HPART(0, g0, s0, 0);
                        if (db == 1) hn[1] = HPART(1, g0, s0, 2);
                        if (db == 2) hn[2] = HPART(2, g1, s1, 0);
                        if (db == 3) { hn[3] = HPART(3, g1, s1, 2); }
                        PIN(hn);
                        if (db == 1 && k8 < 6) { g0 = LDF4(gsp + 64 * (ks + 2)); s0 = LDF4(gsp + 2048 + 64 * (ks + 2)); }
                        if (db == 3 && k8 < 6) { g1 = LDF4(gsp + 64 * (ks + 2) + 16); s1 = LDF4(gsp + 2048 + 64 * (ks + 2) + 16); }
                    }
                    SBAR();
                }
                hc = hn;
            }
        }
#undef XWAIT
#undef XISSUE
#undef WISSUE
#undef HPART
#undef XONE
#undef WONE
#undef KVONE
#pragma unroll
        for (int db = 0; db < 4; ++db) {
#pragma unroll
            for (int r = 0; r < 16; ++r) qa[db][r] *= QSCALE;
            qf[2 * db] = pack8(qa[db], 0); qf[2 * db + 1] = pack8(qa[db], 1);
        }
    }

#define WAIT_BAR4() asm volatile("s_waitcnt vmcnt(4)\n\ts_barrier" ::: "memory")
#define WAITB(t_) do { if ((t_) < tsw) WAIT_BAR4(); else asm volatile("s_waitcnt vmcnt(0)\n\ts_barrier" ::: "memory"); } while (0)
    float l_run = 0.f;
    f32x16 o[4], negm;
#pragma unroll
    for (int d = 0; d < 4; ++d) o[d] = f32x16{};
    const int qrow = rb * 32 + r32;
    const lds_cptr kp0 = (lds_cptr)lds + l * 16, vp0 = (lds_cptr)lds + 65536 + l * 16;
    f32x16 A0, A1, B0, B1;
    bf16x8 pf0, pf1, pf2, pf3, fr[8];
    int sl_prev = 49152, sl_cur = 0;
#define ROT() do { sl_prev = sl_cur; sl_cur = (sl_cur + 16384) & 65535; } while (0)
    const int tsw = 2 * pi + 3;
    const char* hsrc = kvh + (w & 3) * 4096 + l * 16;
    const unsigned hk = lds0 + (w & 3) * 4096, hv = lds0 + 65536 + (w & 3) * 4096;
#define STAGE_ISSUE(t_) do { if ((t_) < tsw) { const int tk_ = (t_) + 3 < NT ? (t_) + 3 : NT - 1, tv_ = (t_) + 2 < NT ? (t_) + 2 : NT - 1; DMA_K(tk_, sl_prev); DMA_V(tv_, (sl_cur + 32768) & 65535); } } while (0)
#define DMA_ARGS(t_) (t_) < tsw, ksrc + (size_t)((t_) + 3 < NT ? (t_) + 3 : NT - 1) * 32768, kdst + sl_prev, ksrc + (size_t)((t_) + 2 < NT ? (t_) + 2 : NT - 1) * 32768 + 16384, vdst + ((sl_cur + 32768) & 65535)
#define STAGE_HELP(t_) do { const int tk_ = (t_) + 3 < NT ? (t_) + 3 : NT - 1, tv_ = (t_) + 2 < NT ? (t_) + 2 : NT - 1; \
        const char* gk_ = hsrc + (size_t)tk_ * 32768; const char* gv_ = hsrc + (size_t)tv_ * 32768 + 16384; const unsigned sk_ = hk + sl_prev, sv_ = hv + ((sl_cur + 32768) & 65535); \
        glds16(gk_, sk_); glds16(gk_ + 1024, sk_ + 1024); glds16(gk_ + 2048, sk_ + 2048); glds16(gk_ + 3072, sk_ + 3072); \
        glds16(gv_, sv_); glds16(gv_ + 1024, sv_ + 1024); glds16(gv_ + 2048, sv_ + 2048); glds16(gv_ + 3072, sv_ + 3072); } while (0)
    asm volatile("s_waitcnt vmcnt(2) lgkmcnt(0)\n\ts_barrier" ::: "memory");
    STAGE_ISSUE(0);
    {
        A0 = f32x16{}; A1 = f32x16{};
#pragma unroll
        for (int ds = 0; ds < 8; ++ds) {
            A0 = MFMA(ldsb128(kp0 + ds * 1024), qf[ds], A0);
            A1 = MFMA(ldsb128(kp0 + 8192 + ds * 1024), qf[ds], A1);
        }
        if (tl == 0) att_mask(A0, A1, qrow - 4 * hi);
        const float mx = half_swap_max(att_rowmax(A0, A1));
#pragma unroll
        for (int r = 0; r < 16; ++r) { A0[r] = __builtin_amdgcn_exp2f(A0[r] - mx); A1[r] = A1[r] - mx; negm[r] = -mx; }
#pragma unroll
        for (int g = 0; g < PD; ++g) fr[g] = ldsb128(kp0 + 16384 + kfrag_off(g));
    }
    WAIT_BAR4(); ROT();
    int t = 1;
    for (; t + 1 <= tl - 1; t += 2) {
        STAGE_ISSUE(t); att_step<false>(B0, B1, A0, A1, o, negm, l_run, qf, fr, kp0 + sl_cur, vp0 + sl_prev, kp0 + ((sl_cur + 16384) & 65535), 0); WAITB(t); ROT();
        STAGE_ISSUE(t + 1); att_step<false>(A0, A1, B0, B1, o, negm, l_run, qf, fr, kp0 + sl_cur, vp0 + sl_prev, kp0 + ((sl_cur + 16384) & 65535), 0); WAITB(t + 1); ROT();
    }
    if (t <= tl - 1) {
        STAGE_ISSUE(t); att_step<false>(B0, B1, A0, A1, o, negm, l_run, qf, fr, kp0 + sl_cur, vp0 + sl_prev, kp0 + ((sl_cur + 16384) & 65535), 0); WAITB(t); ROT();
        A0 = B0; A1 = B1; ++t;
    }
    if (tl >= 1) {
        STAGE_ISSUE(t); att_step<true>(B0, B1, A0, A1, o, negm, l_run, qf, fr, kp0 + sl_cur, vp0 + sl_prev, kp0 + ((sl_cur + 16384) & 65535), qrow - t * 64 - 4 * hi); WAITB(t); ROT();
        ++t;
    } else { B0 = A0; B1 = A1; }
    if (t < NT) STAGE_ISSUE(t);
    {
        float sacc = 0.f;
#pragma unroll
        for (int r = 0; r < 16; ++r) { B1[r] = __builtin_amdgcn_exp2f(B1[r]); sacc += B0[r] + B1[r]; }
        l_run += sacc;
        pf0 = pack8(B0, 0); pf1 = pack8(B0, 1); pf2 = pack8(B1, 0); pf3 = pack8(B1, 1);
        const lds_cptr vp = vp0 + sl_prev;
#pragma unroll
        for (int d = 0; d < 4; ++d) {
            o[d] = MFMA(ldsb128(vp + (d * 4 + 0) * 1024), pf0, o[d]);
            o[d] = MFMA(ldsb128(vp + (d * 4 + 1) * 1024), pf1, o[d]);
            o[d] = MFMA(ldsb128(vp + (d * 4 + 2) * 1024), pf2, o[d]);
            o[d] = MFMA(ldsb128(vp + (d * 4 + 3) * 1024), pf3, o[d]);
        }
    }

    {
        const float inv = 1.f / half_swap_sum(l_run);
        char* cb = ws + WS_CTX + ((size_t)(rb * 256 + head * 8) * 64 + l) * 16;
#pragma unroll
        for (int d = 0; d < 4; ++d) {
#pragma unroll
            for (int r = 0; r < 16; ++r) o[d][r] *= inv;
            *(bf16x8*)(cb + (2 * d) * 1024) = pack8(o[d], 0);
            *(bf16x8*)(cb + (2 * d + 1) * 1024) = pack8(o[d], 1);
        }
    }
    if (t < NT) {
        WAIT_ALL_BAR(); ROT(); ++t;
        for (; t < NT && t < tsw; ++t) { STAGE_ISSUE(t); WAIT_BAR4(); ROT(); }
        if (w < 4) {
            float* scr = (float*)(lds + 147456);
            const int t0 = t;
            for (; t < NT; ++t) {
                STAGE_HELP(t);
                if (pi == 6 && t == t0) {
                    const int c8 = tid & 7, rowi = tid >> 3;
                    float a8[8];
#pragma unroll
                    for (int e = 0; e < 8; ++e) a8[e] = 0.f;
#pragma unroll
                    for (int q = 0; q < 16; ++q) {
                        const u32x4 wv = *(const u32x4*)(wmod + (size_t)(rowi + 32 * q) * 3072 + 1024 + 64 * head + 8 * c8);
                        const float cv = bf2f(cvec[rowi + 32 * q]), sc = bfr(cv / (1.f + __expf(-cv)));
#pragma unroll
                        for (int e = 0; e < 4; ++e) { a8[2 * e] += sc * bflo(wv[e]); a8[2 * e + 1] += sc * bfhi(wv[e]); }
                    }
#pragma unroll
                    for (int m = 8; m < 64; m <<= 1)
#pragma unroll
                        for (int e = 0; e < 8; ++e) a8[e] += __shfl_xor(a8[e], m);
                    if (l < 8) {
#pragma unroll
                        for (int e = 0; e < 8; ++e) scr[(w * 8 + l) * 8 + e] = a8[e];
                    }
                }
                if (pi <= 5 && t == t0 + 1) {
#pragma unroll
                    for (int j = 0; j < 2; ++j) {
                        const int ch = (head * 6 + pi) * 4 + w + 768 * j;
                        if (ch < 960) {
                            const char* src = ch < 512 ? (const char*)wo + (size_t)ch * 8192 : ch < 736 ? (const char*)w1 + (size_t)(ch - 512) * 8192 : (const char*)w2 + (size_t)(ch - 736) * 8192;
                            glds4(src + l * 128, lds0 + 147456 + 2048);
                        }
                    }
                }
                if (pi == 6 && t == t0 + 1 && tid < 64) {
                    const float s = (scr[tid] + scr[64 + tid]) + (scr[128 + tid] + scr[192 + tid]);
                    ((float*)(ws + WS_MOD))[1024 + 64 * head + tid] = bfr(s);
                }
                asm volatile("s_waitcnt vmcnt(8) lgkmcnt(0)\n\ts_barrier" ::: "memory"); ROT();
            }
        }
        else { for (; t < NT; ++t) { WAIT_ALL_BAR(); ROT(); } }
    }
    asm volatile("s_waitcnt vmcnt(0)" ::: "memory");

#undef DMA_K
#undef DMA_V
#undef ROT
#undef STAGE_ISSUE
#undef STAGE_HELP
#undef DMA_ARGS
#undef WAITB
}

template <int TQ, int TN, int KW, int BKS, bool APERM, bool CT>
struct G {
    static constexpr int NWAVE = TQ * TN * KW, RB = TN * 128, A_BYTES = 2 * TQ * BKS * 1024, W_BYTES = 16 * BKS * RB, STAGE = A_BYTES + W_BYTES;
    static constexpr int CPR = RB / 16;
    static_assert(STAGE == 32768 && (2 * TQ * BKS + W_BYTES / 1024) / NWAVE == 4, "4 glds per wave per stage, 4 x 32 KB ring");
    static DI int swz(int row) { return RB == 256 ? ((row & 3) << 2) : (((row >> 1) & 1) << 2); }
    static DI void stage(const char* AF, size_t a_rb_stride, const u16* Wp, int ldw, int kst, unsigned ldsaddr, int w, int l) {
#pragma unroll
        for (int p0 = 0; p0 < 2 * TQ * BKS; p0 += NWAVE) {
            const int p = p0 + w, rbi = p / BKS, ks = p % BKS;
            glds16(AF + rbi * a_rb_stride + (size_t)(kst * BKS + ks) * 1024 + l * 16, ldsaddr + p * 1024);
        }
#pragma unroll
        for (int p0 = 0; p0 < W_BYTES / 1024; p0 += NWAVE) {
            const int p = p0 + w, row = p * (1024 / RB) + l / CPR, c = (l % CPR) ^ swz(row);
            glds16(Wp + (size_t)(kst * BKS * 16 + row) * ldw + c * 8, ldsaddr + A_BYTES + p * 1024);
        }
    }
    static constexpr int NPA = 2 * TQ * BKS / NWAVE, NPW = W_BYTES / 1024 / NWAVE;
    static DI void stage_piece(int i, const char* AF, size_t a_rb_stride, const u16* Wp, int ldw, int kst, unsigned ldsaddr, int w, int l) {
        if (i < NPA) {
            const int p = i * NWAVE + w, rbi = p / BKS, ks = p % BKS;
            glds16(AF + rbi * a_rb_stride + (size_t)(kst * BKS + ks) * 1024 + l * 16, ldsaddr + p * 1024);
        } else {
            const int p = (i - NPA) * NWAVE + w, row = p * (1024 / RB) + l / CPR, c = (l % CPR) ^ swz(row);
            glds16(Wp + (size_t)(kst * BKS * 16 + row) * ldw + c * 8, ldsaddr + A_BYTES + p * 1024);
        }
    }
    template <bool RM, class EPI>
    static DI void run(EPI epi, const char* AF, size_t a_rb_stride, const u16* Wp, int ldw, int nstage, int rot, char* lds, float (&red)[64 / KW], int& tq, int& tn, int& kw, char* ws, int kid) {
        const int tid = threadIdx.x, l = tid & 63, hi = l >> 5;

        const int w = __builtin_amdgcn_readfirstlane(tid >> 6);
        kw = w % KW; const int tt = w / KW; tq = tt / TN; tn = tt % TN;
        const unsigned lds0 = (unsigned)(uintptr_t)lds;
        const int q4 = (l & 15) >> 2, p4 = l & 3, blk = (l >> 4) & 1;
        f32x16 acc[2][2];
#pragma unroll
        for (int i = 0; i < 2; ++i) { acc[i][0] = f32x16{}; acc[i][1] = f32x16{}; }
        int kst = rot;
#define NEXTK() do { kst = (kst + 1 == nstage) ? 0 : kst + 1; } while (0)
        stage(AF, a_rb_stride, Wp, ldw, kst, lds0, w, l); NEXTK();
        stage(AF, a_rb_stride, Wp, ldw, kst, lds0 + STAGE, w, l); NEXTK();
        stage(AF, a_rb_stride, Wp, ldw, kst, lds0 + 2 * STAGE, w, l); NEXTK();
        for (int st = 0; st < nstage; ++st) {
            if (st + 2 < nstage) asm volatile("s_waitcnt vmcnt(8) lgkmcnt(0)\n\ts_barrier" ::: "memory");
            else if (st + 1 < nstage) asm volatile("s_waitcnt vmcnt(4) lgkmcnt(0)\n\ts_barrier" ::: "memory");
            else WAIT_ALL_BAR();
            const bool pre = st + 3 < nstage; const unsigned pdst = lds0 + ((st + 3) & 3) * STAGE;
            lds_cptr ab = (lds_cptr)lds + (st & 3) * STAGE;
            lds_cptr wb = ab + A_BYTES;
#pragma unroll
            for (int kk = 0; kk < BKS / KW; ++kk) {
                const int ks = kw * (BKS / KW) + kk;
                bf16x8 af[2], wf[2];
#pragma unroll
                for (int i = 0; i < 2; ++i) af[i] = ldsb128(ab + ((2 * tq + i) * BKS + ks) * 1024 + l * 16);
#pragma unroll
                for (int j = 0; j < 2; ++j) {
                    const int r0 = 16 * ks + (APERM ? 4 * hi : 8 * hi) + q4, r1 = r0 + (APERM ? 8 : 4);
                    const int ch = 4 * (2 * tn + j) + 2 * blk + (p4 >> 1);
                    wf[j] = wfrag(wb + r0 * RB + ((ch ^ swz(r0)) << 4) + 8 * (p4 & 1), wb + r1 * RB + ((ch ^ swz(r1)) << 4) + 8 * (p4 & 1));
                }
#pragma unroll
                for (int i = 0; i < 2; ++i)
#pragma unroll
                    for (int j = 0; j < 2; ++j) {
                        if (CT) acc[i][j] = MFMA(wf[j], af[i], acc[i][j]);
                        else acc[i][j] = MFMA(af[i], wf[j], acc[i][j]);
                        constexpr int PER = 4 / (BKS / KW);
                        const int m = i * 2 + j;
                        if (PER == 4) { SBAR(); if (pre) stage_piece(m, AF, a_rb_stride, Wp, ldw, kst, pdst, w, l); SBAR(); }
                        else if (m & 1) { SBAR(); if (pre) stage_piece(kk * 2 + (m >> 1), AF, a_rb_stride, Wp, ldw, kst, pdst, w, l); SBAR(); }
                    }
            }
            if (pre) NEXTK();
        }
        BAR_LGKM();

        float* rb_ = (float*)lds;
        constexpr int RS = RM ? 68 : 64;
#pragma unroll
        for (int i = 0; i < 2; ++i)
#pragma unroll
            for (int j = 0; j < 2; ++j)
#pragma unroll
                for (int r = 0; r < 16; ++r) rb_[(w * 64 + (i * 2 + j) * 16 + r) * RS + l] = acc[i][j][r];
        BAR_LGKM();
        if constexpr (RM) {
            constexpr int C8 = TN * 8, NITEM = 64 * TQ * C8 / (64 * NWAVE);
#pragma unroll
            for (int it = 0; it < NITEM; ++it) {
                const int id = it * (64 * NWAVE) + tid, row = id / C8, c8 = id % C8;
                const int rr = row & 31, r = (rr & 3) + 4 * (rr >> 3), h = (rr >> 2) & 1;
                const int t2 = (row >> 6) * TN + (c8 >> 3), reg = (((row >> 5) & 1) * 2 + ((c8 >> 2) & 1)) * 16 + r, ln = (c8 & 3) * 8 + 32 * h;
                f32x4 a0 = {0.f, 0.f, 0.f, 0.f}, a1 = a0;
#pragma unroll
                for (int k2 = 0; k2 < KW; ++k2) {
                    const float* q_ = rb_ + ((t2 * KW + k2) * 64 + reg) * RS + ln;
                    a0 += *(const f32x4*)q_; a1 += *(const f32x4*)(q_ + 4);
                }
                epi(row, c8 * 8, a0, a1);
            }
        } else {
#pragma unroll
            for (int e = 0; e < 64 / KW; ++e) {
                float s = 0.f;
#pragma unroll
                for (int k2 = 0; k2 < KW; ++k2) s += rb_[((tt * KW + k2) * 64 + kw * (64 / KW) + e) * RS + l];
                red[e] = s;
            }
        }

    }
};

typedef G<2, 2, 2, 4, true, false> G3;
constexpr int K3_LDS = 139264;
__global__ __launch_bounds__(512, 2) void k3_oproj(const u16* __restrict__ wo, char* __restrict__ ws) {
    extern __shared__ __attribute__((aligned(16))) char lds[];
    const int b = blockIdx.x, xcd = b & 7, r = b >> 3, ksl = xcd & 3, rp = (xcd >> 2) * 8 + (r >> 2), ct = r & 3;
    float red[32]; int tq, tn, kw;
    u16* part = (u16*)(ws + WS_PART) + (size_t)ksl * 2048 * 512 + (size_t)(rp * 128) * 512 + ct * 128;
    G3::run<true>([&](int row, int col, f32x4 a0, f32x4 a1) {
                      u32x4 v = {pk2(a0[0], a0[1]), pk2(a0[2], a0[3]), pk2(a1[0], a1[1]), pk2(a1[2], a1[3])};
                      *(u32x4*)(part + (size_t)row * 512 + col) = v; },
                  ws + WS_CTX + ((size_t)(rp * 4) * 256 + ksl * 64) * 1024, (size_t)256 * 1024, wo + (size_t)ksl * 1024 * 512 + ct * 128, 512, 16, (4 * ct + 2 * (r >> 2)) & 15, lds, red, tq, tn, kw, ws, 2);

}

__global__ __launch_bounds__(512) void k3b_ln2(const u16* __restrict__ x, char* __restrict__ ws) {
    const int l = threadIdx.x & 63, row = blockIdx.x * 8 + (threadIdx.x >> 6);
    const u16* part = (const u16*)(ws + WS_PART) + (size_t)row * 512 + 8 * l;
    const float* mod = (const float*)(ws + WS_MOD);
    u32x4 pv[4];
#pragma unroll
    for (int s = 0; s < 4; ++s) pv[s] = *(const u32x4*)(part + (size_t)s * 2048 * 512);
    const u32x4 xv4 = *(const u32x4*)(x + (size_t)row * 512 + 8 * l);
    const f32x4 g0 = *(const f32x4*)(mod + 1024 + 8 * l), g1 = *(const f32x4*)(mod + 1024 + 8 * l + 4);
    const f32x4 c0 = *(const f32x4*)(mod + 2048 + 8 * l), c1 = *(const f32x4*)(mod + 2048 + 8 * l + 4);
    const f32x4 s0 = *(const f32x4*)(mod + 1536 + 8 * l), s1 = *(const f32x4*)(mod + 1536 + 8 * l + 4);
    float x1[8], xh[8];
#pragma unroll
    for (int j = 0; j < 4; ++j) {
        const int w0 = j >> 1, w1 = 2 + (j >> 1);
        const float a0 = (j & 1) ? (bfhi(pv[0][w0]) + bfhi(pv[1][w0])) + (bfhi(pv[2][w0]) + bfhi(pv[3][w0])) : (bflo(pv[0][w0]) + bflo(pv[1][w0])) + (bflo(pv[2][w0]) + bflo(pv[3][w0]));
        const float a1 = (j & 1) ? (bfhi(pv[0][w1]) + bfhi(pv[1][w1])) + (bfhi(pv[2][w1]) + bfhi(pv[3][w1])) : (bflo(pv[0][w1]) + bflo(pv[1][w1])) + (bflo(pv[2][w1]) + bflo(pv[3][w1]));
        const float xa = (j & 1) ? bfhi(xv4[j >> 1]) : bflo(xv4[j >> 1]), xb = (j & 1) ? bfhi(xv4[2 + (j >> 1)]) : bflo(xv4[2 + (j >> 1)]);
        x1[j] = bfr(xa + bfr(g0[j] * bfr(a0)));
        x1[4 + j] = bfr(xb + bfr(g1[j] * bfr(a1)));
    }
    u32x4 o1 = {pk2(x1[0], x1[1]), pk2(x1[2], x1[3]), pk2(x1[4], x1[5]), pk2(x1[6], x1[7])};
    *(u32x4*)(ws + WS_X1 + ((size_t)row * 512 + 8 * l) * 2) = o1;
    ln_row(x1, xh);
    float h[8];
#pragma unroll
    for (int j = 0; j < 4; ++j) { h[j] = bfr(xh[j] * bfr(1.f + c0[j])) + s0[j]; h[4 + j] = bfr(xh[4 + j] * bfr(1.f + c1[j])) + s1[j]; }
    u32x4 oh = {pk2(h[0], h[1]), pk2(h[2], h[3]), pk2(h[4], h[5]), pk2(h[6], h[7])};
    *(u32x4*)(ws + WS_H2 + ((size_t)((row >> 5) * 32 + (l >> 1)) * 64 + (row & 31) + 32 * (l & 1)) * 16) = oh;
}

typedef G<2, 2, 2, 4, false, true> G4;
__global__ __launch_bounds__(512, 2) void k4_ffn1(const u16* __restrict__ w1, char* __restrict__ ws) {
    extern __shared__ __attribute__((aligned(16))) char lds[];
    const int b = blockIdx.x, xcd = b & 7, jj = b >> 3, rp = 4 * (xcd >> 1) + jj / 7, ct = 7 * (xcd & 1) + jj % 7;
    const int l = threadIdx.x & 63;
    float red[32]; int tq, tn, kw;
    G4::run<false>(0, ws + WS_H2 + (size_t)(rp * 4) * 32 * 1024, (size_t)32 * 1024, w1 + ct * 128, DFF, 8, (jj % 7 + 2 * (jj / 7)) & 7, lds, red, tq, tn, kw, ws, 3);
#pragma unroll
    for (int j = 0; j < 2; ++j)
#pragma unroll
        for (int s = 0; s < 2; ++s) {
            const int e = j * 16 + 8 * s;
            u32x4 v = {pk2(fmaxf(red[e], 0.f), fmaxf(red[e + 1], 0.f)), pk2(fmaxf(red[e + 2], 0.f), fmaxf(red[e + 3], 0.f)),
                       pk2(fmaxf(red[e + 4], 0.f), fmaxf(red[e + 5], 0.f)), pk2(fmaxf(red[e + 6], 0.f), fmaxf(red[e + 7], 0.f))};
            const int rbq = rp * 4 + 2 * tq + kw, kstep = (ct * 128 + 64 * tn + 32 * j) / 16 + s;
            *(u32x4*)(ws + WS_H1 + ((size_t)(rbq * 112 + kstep) * 64 + l) * 16) = v;
        }

}

typedef G<1, 1, 8, 8, true, false> G5;
__global__ __launch_bounds__(512, 2) void k5_ffn2(const u16* __restrict__ w2, char* __restrict__ ws, u16* __restrict__ out) {
    extern __shared__ __attribute__((aligned(16))) char lds[];
    const int b = blockIdx.x, xcd = b & 7, jj = b >> 3, rp = 8 * (xcd >> 1) + (jj >> 2), ct = 4 * (xcd & 1) + (jj & 3);
    float red[8]; int tq, tn, kw;
    const float* gb = (const float*)(ws + WS_MOD) + 2560 + ct * 64;
    const u16* x1 = (const u16*)(ws + WS_X1) + (size_t)(rp * 64) * 512 + ct * 64;
    u16* op = out + (size_t)(rp * 64) * 512 + ct * 64;
    G5::run<true>([&](int row, int col, f32x4 a0, f32x4 a1) {
                      const u32x4 xv = *(const u32x4*)(x1 + (size_t)row * 512 + col);
                      const f32x4 g0 = *(const f32x4*)(gb + col), g1 = *(const f32x4*)(gb + col + 4);
                      u32x4 v = {pk2(bflo(xv[0]) + bfr(g0[0] * bfr(a0[0])), bfhi(xv[0]) + bfr(g0[1] * bfr(a0[1]))),
                                 pk2(bflo(xv[1]) + bfr(g0[2] * bfr(a0[2])), bfhi(xv[1]) + bfr(g0[3] * bfr(a0[3]))),
                                 pk2(bflo(xv[2]) + bfr(g1[0] * bfr(a1[0])), bfhi(xv[2]) + bfr(g1[1] * bfr(a1[1]))),
                                 pk2(bflo(xv[3]) + bfr(g1[2] * bfr(a1[2])), bfhi(xv[3]) + bfr(g1[3] * bfr(a1[3])))};
                      *(u32x4*)(op + (size_t)row * 512 + col) = v; },
                  ws + WS_H1 + (size_t)(rp * 2) * 112 * 1024, (size_t)112 * 1024, w2 + ct * 64, 512, 14, (3 * (jj & 3) + 2 * (jj >> 2)) % 14, lds, red, tq, tn, kw, ws, 4);

}

extern "C" void kernel_launch(void* const* d_in, const int* in_sizes, int n_in, void* d_out, int out_size, void* d_ws, size_t ws_size,
                              hipStream_t stream) {
    (void)in_sizes; (void)n_in; (void)out_size; (void)ws_size;
    const u16* x = (const u16*)d_in[0];
    const u16* ckv = (const u16*)d_in[1];
    const u16* wq = (const u16*)d_in[2];
    const u16* wuk = (const u16*)d_in[3];
    const u16* wuv = (const u16*)d_in[4];
    const u16* wo = (const u16*)d_in[5];
    const u16* w1 = (const u16*)d_in[7];
    const u16* w2 = (const u16*)d_in[8];
    const u16* cvec = (const u16*)d_in[9];
    const u16* wmod = (const u16*)d_in[10];
    char* ws = (char*)d_ws;
    (void)hipFuncSetAttribute((const void*)k1_kvproj, hipFuncAttributeMaxDynamicSharedMemorySize, K1_LDS);
    (void)hipFuncSetAttribute((const void*)k2_attn, hipFuncAttributeMaxDynamicSharedMemorySize, K2_LDS);
    (void)hipFuncSetAttribute((const void*)k3_oproj, hipFuncAttributeMaxDynamicSharedMemorySize, K3_LDS);
    (void)hipFuncSetAttribute((const void*)k4_ffn1, hipFuncAttributeMaxDynamicSharedMemorySize, K3_LDS);
    (void)hipFuncSetAttribute((const void*)k5_ffn2, hipFuncAttributeMaxDynamicSharedMemorySize, K3_LDS);
    k1_kvproj<<<256, 512, K1_LDS, stream>>>(ckv, wuk, wuv, x, cvec, wmod, ws);
    k2_attn<<<256, 512, K2_LDS, stream>>>(wq, cvec, wmod, wo, w1, w2, ws);
    k3_oproj<<<256, 512, K3_LDS, stream>>>(wo, ws);
    k3b_ln2<<<256, 512, 0, stream>>>(x, ws);
    k4_ffn1<<<224, 512, K3_LDS, stream>>>(w1, ws);
    k5_ffn2<<<256, 512, K3_LDS, stream>>>(w2, ws, (u16*)d_out);
}
```
